```python
import torch

def _softmax(x, axis=-1):
    return torch.softmax(x, dim=axis)
def _gelu(x):
    return torch.nn.functional.gelu(x, approximate="tanh")
def _silu(x):
    return torch.nn.functional.silu(x)
def _sigmoid(x):
    return torch.sigmoid(x)
def _layernorm(x, axis=-1, eps=1e-5):
    m = x.mean(dim=axis, keepdim=True)
    v = x.var(dim=axis, keepdim=True, unbiased=False)
    return (x - m) / torch.sqrt(v + eps)
def _scatter_add(tgt, idx, src, axis=0):
    return tgt.clone().index_add_(axis, idx, src)
def _segment_sum(src, seg, n):
    out = torch.zeros((n,) + src.shape[1:], dtype=src.dtype)
    return out.index_add_(0, seg, src)
def _pad(x, pw):
    flat = tuple(v for pair in reversed(pw) for v in pair)
    return torch.nn.functional.pad(x, flat)

kernel_name = 'parallel_sum__gqa_block_cached__glu_gated_reglu__decode'

def setup_inputs():

    _w = (lambda a: (lambda t: t.to(torch.bfloat16) if t.is_floating_point() else t)(torch.as_tensor(a)))
    def _A_setup():
        B, Sq, D = 1, 1, 448
        Hq, Hkv, Sk, Dh = 16, 8, 65536, 256
        assert Hq % Hkv == 0, (Hq, Hkv)
        return {
            "x": _w((lambda *s: torch.randn(*s, dtype=torch.bfloat16) * 0.05)(B, Sq, D)),
            "k_cache": _w((lambda *s: torch.randn(*s, dtype=torch.bfloat16) * 3.2)(B, Hkv, Sk, Dh)),
            "v_cache": _w((lambda *s: torch.randn(*s, dtype=torch.bfloat16) * 1.6)(B, Hkv, Sk, Dh)),
            "wq": _w((lambda *s: torch.randn(*s, dtype=torch.bfloat16) * 3.2)(D, Hq * Dh) * (D ** -0.5)),
            "wo": _w((lambda *s: torch.randn(*s, dtype=torch.bfloat16) * 1.6)(Hq * Dh, D) * ((Hq * Dh) ** -0.5)),
        }
    _A_in = _A_setup()

    def _B_setup():
        B, S, D, F = 1, 1, 448, 1792
        return {
            "x": _w((lambda *s: torch.randn(*s, dtype=torch.bfloat16) * 0.05)(B, S, D)),
            "W_gate": _w((lambda *s: torch.randn(*s, dtype=torch.bfloat16) * 1.6)(D, F) * (2.0 / D) ** 0.5),
            "W_up": _w((lambda *s: torch.randn(*s, dtype=torch.bfloat16) * 1.6)(D, F) * (2.0 / D) ** 0.5),
            "W_down": _w((lambda *s: torch.randn(*s, dtype=torch.bfloat16) * 1.6)(F, D) * (1.0 / F) ** 0.5),
        }
    _B_in = _B_setup()

    return {
        "A_x": _A_in["x"],
        "A_k_cache": _A_in["k_cache"],
        "A_v_cache": _A_in["v_cache"],
        "A_wq": _A_in["wq"],
        "A_wo": _A_in["wo"],
        "B_x": _B_in["x"],
        "B_W_gate": _B_in["W_gate"],
        "B_W_up": _B_in["W_up"],
        "B_W_down": _B_in["W_down"],
    }

def reference(A_x, A_k_cache, A_v_cache, A_wq, A_wo, B_x, B_W_gate, B_W_up, B_W_down):
    def _A_ref(x, k_cache, v_cache, wq, wo):
        B, Sq, D = x.shape
        Hq, Dh = 16, 256
        Hkv = k_cache.shape[1]
        rep = Hq // Hkv
        q = (lambda x, *d: x.permute(*d))((x @ wq).reshape(B, Sq, Hq, Dh), 0, 2, 1, 3)
        k = (lambda x, n, axis: torch.repeat_interleave(x, n, dim=axis))(k_cache, rep, axis=1)
        v = (lambda x, n, axis: torch.repeat_interleave(x, n, dim=axis))(v_cache, rep, axis=1)
        scores = torch.einsum("bhqd,bhkd->bhqk", q, k) * (Dh ** -0.5)
        p = _softmax(scores, axis=-1)
        o = torch.einsum("bhqk,bhkd->bhqd", p, v)
        return (lambda x, *d: x.permute(*d))(o, 0, 2, 1, 3).reshape(B, Sq, Hq * Dh) @ wo

    def _B_ref(x, W_gate, W_up, W_down):
        gate = torch.einsum("bsd,df->bsf", x, W_gate)
        up = torch.einsum("bsd,df->bsf", x, W_up)
        gate = gate * (lambda x, dt: x.to(dt))(gate > 0, gate.dtype)
        h = gate * up
        return torch.einsum("bsf,fd->bsd", h, W_down)

    x = A_x
    xn = _layernorm(x, axis=-1)
    return x + _A_ref(xn, A_k_cache, A_v_cache, A_wq, A_wo) + _B_ref(xn, B_W_gate, B_W_up, B_W_down)
```

```cpp
#include <hip/hip_runtime.h>
#include <hip/hip_bf16.h>
#include <cstdint>

namespace {

typedef unsigned u32x4 __attribute__((ext_vector_type(4)));
typedef unsigned u32x2 __attribute__((ext_vector_type(2)));
typedef float f32x4 __attribute__((ext_vector_type(4)));
typedef float f32x2 __attribute__((ext_vector_type(2)));
typedef __bf16 bf16x8 __attribute__((ext_vector_type(8)));
typedef __bf16 bf16x2 __attribute__((ext_vector_type(2)));
typedef __amdgpu_buffer_rsrc_t rsrc_t;

constexpr int D = 448, SK = 65536, DH = 256, FF = 1792, NQ = 4096;
constexpr int NWG = 256, NTHR = 512;
#define HT 2
#define CPR 8
constexpr int NRED = D / CPR;
constexpr int TILES = 128;
constexpr int ST = (TILES - 4 * HT) / 4;
constexpr float C2 = 0.0625f * 1.4426950408889634f;
constexpr float NEG_BIG = -3.0e38f;
constexpr unsigned SPIN_CAP = 1u << 22;

constexpr unsigned WS_EPOCH = 0;
constexpr unsigned WS_QG = 4096;
constexpr unsigned WS_ML = WS_QG + 8 * 32 * 8 * 8;
constexpr unsigned WS_PO = WS_ML + 8 * 2 * 32 * 16;
constexpr unsigned WS_YA = WS_PO + 8 * 32 * 32 * 16 * 8;
constexpr unsigned WS_YB = WS_YA + 256 * D * 8;
constexpr unsigned WS_END = WS_YB + 224 * D * 8;

constexpr int L_RED = 0;
constexpr int L_RED2 = 512;
constexpr int L_Q = 1024;
constexpr int L_ML = 2048;
constexpr int L_FLAG = 2304;
constexpr int L_YB = 2560;
constexpr int L_YA = 3072;
constexpr int L_ACC = 4096;
constexpr int LDS_BYTES = 84 * 1024;

constexpr int AUX_NT = 2, AUX_SC1 = 16;
#define SB() __builtin_amdgcn_sched_barrier(0)

__device__ __forceinline__ float bf_lo(unsigned u) { return __builtin_bit_cast(float, u << 16); }
__device__ __forceinline__ float bf_hi(unsigned u) { return __builtin_bit_cast(float, u & 0xffff0000u); }
__device__ __forceinline__ unsigned pk_bf16(float lo, float hi) {
  bf16x2 v = {(__bf16)lo, (__bf16)hi};
  return __builtin_bit_cast(unsigned, v);
}
__device__ __forceinline__ float rnd_bf16(float x) { return (float)(__bf16)x; }
__device__ __forceinline__ float dot2(unsigned a, unsigned b, float acc) {
  return __builtin_amdgcn_fdot2_f32_bf16(__builtin_bit_cast(bf16x2, a), __builtin_bit_cast(bf16x2, b), acc, false);
}
template <int CTRL> __device__ __forceinline__ float dpp(float x) {
  return __builtin_bit_cast(float, __builtin_amdgcn_mov_dpp(__builtin_bit_cast(int, x), CTRL, 0xf, 0xf, true));
}
constexpr int XOR1 = 0xB1, XOR2 = 0x4E, XOR7 = 0x141, XOR15 = 0x140, XOR8 = 0x128, ROR4 = 0x124;
__device__ __forceinline__ float x16_max(float x) {
  auto s = __builtin_amdgcn_permlane16_swap(__float_as_uint(x), __float_as_uint(x), false, false);
  return fmaxf(__uint_as_float(s[0]), __uint_as_float(s[1]));
}
__device__ __forceinline__ float x32_max(float x) {
  auto s = __builtin_amdgcn_permlane32_swap(__float_as_uint(x), __float_as_uint(x), false, false);
  return fmaxf(__uint_as_float(s[0]), __uint_as_float(s[1]));
}
__device__ __forceinline__ float x16_sum(float x) {
  auto s = __builtin_amdgcn_permlane16_swap(__float_as_uint(x), __float_as_uint(x), false, false);
  return __uint_as_float(s[0]) + __uint_as_float(s[1]);
}
__device__ __forceinline__ float x32_sum(float x) {
  auto s = __builtin_amdgcn_permlane32_swap(__float_as_uint(x), __float_as_uint(x), false, false);
  return __uint_as_float(s[0]) + __uint_as_float(s[1]);
}
__device__ __forceinline__ float row16_sum(float x) {
  x += dpp<XOR1>(x); x += dpp<XOR2>(x); x += dpp<XOR7>(x); x += dpp<XOR15>(x);
  return x;
}
__device__ __forceinline__ float wave_sum(float x) { return x32_sum(x16_sum(row16_sum(x))); }
__device__ __forceinline__ void lds_barrier() {
  asm volatile("s_waitcnt lgkmcnt(0)" ::: "memory");
  __builtin_amdgcn_s_barrier();
  asm volatile("" ::: "memory");
}
__device__ __forceinline__ rsrc_t mk_rsrc(const void* p, unsigned bytes) {
  return __builtin_amdgcn_make_buffer_rsrc(const_cast<void*>(p), 0, bytes, 0x00020000);
}
#define STAMP(slot) do {} while (0)

struct Args {
  const unsigned short* x;
  const unsigned char* k;
  const unsigned char* v;
  const unsigned char* wq;
  const unsigned char* wo;
  const unsigned char* wg;
  const unsigned char* wu;
  const unsigned char* wd;
  unsigned short* out;
  unsigned char* ws;
};

__device__ __forceinline__ void issue_k(u32x4 (&r)[8], rsrc_t rs, unsigned voff, unsigned soff) {
#pragma unroll
  for (int i = 0; i < 8; ++i) r[i] = __builtin_amdgcn_raw_buffer_load_b128(rs, voff + i * 64, soff, AUX_NT);
}
__device__ __forceinline__ void issue_v(u32x4 (&r)[8], rsrc_t rs, unsigned voff, unsigned soff) {
#pragma unroll
  for (int i = 0; i < 8; ++i) r[i] = __builtin_amdgcn_raw_buffer_load_b128(rs, voff + i * 256, soff, AUX_NT);
}

__device__ __forceinline__ void half_sum8(float (&s)[8]) {
#pragma unroll
  for (int i = 0; i < 8; ++i) s[i] = x16_sum(row16_sum(s[i]));
}
__device__ __forceinline__ void fma8(float (&s)[8], float xv, u32x4 w) {
#pragma unroll
  for (int i = 0; i < 4; ++i) { s[2 * i] = fmaf(xv, bf_lo(w[i]), s[2 * i]); s[2 * i + 1] = fmaf(xv, bf_hi(w[i]), s[2 * i + 1]); }
}

__global__ void __launch_bounds__(NTHR, 2) fused_decode(Args a) {
  extern __shared__ __attribute__((aligned(16))) unsigned char lds[];
  const int tid = threadIdx.x, lane = tid & 63, wave = __builtin_amdgcn_readfirstlane(tid >> 6);
  const int wg = blockIdx.x, h = wg >> 5, s = wg & 31;
  const int c = lane & 15, g = lane >> 4;
  STAMP(0);

  const rsrc_t r_ws = mk_rsrc(a.ws, WS_END);
  const rsrc_t r_x = mk_rsrc(a.x, D * 2);
  const rsrc_t r_wq = mk_rsrc(a.wq, D * NQ * 2);
  const rsrc_t r_k = mk_rsrc(a.k, 8u * SK * DH * 2);
  const rsrc_t r_v = mk_rsrc(a.v, 8u * SK * DH * 2);

  const int qsl = (s & 7) * 4 + (s >> 3);
  const int hf = lane >> 5, row0 = wave * 32 + (lane & 31);
  const unsigned wq_off = (unsigned)row0 * (NQ * 2) + (unsigned)(h * 512 + qsl * 16 + hf * 8) * 2;
  const u32x4 wq0 = __builtin_amdgcn_raw_buffer_load_b128(r_wq, wq_off, 0, AUX_NT);
  const u32x4 wq1 = __builtin_amdgcn_raw_buffer_load_b128(r_wq, wq_off, 256 * NQ * 2, AUX_NT);
  const u32x4 xv = __builtin_amdgcn_raw_buffer_load_b128(r_x, lane * 16, 0, 0);
  const unsigned short xr0 = __builtin_amdgcn_raw_buffer_load_b16(r_x, row0 * 2, 0, 0);
  const unsigned short xr1 = __builtin_amdgcn_raw_buffer_load_b16(r_x, row0 * 2, 512, 0);
  const unsigned epoch = __builtin_amdgcn_raw_buffer_load_b32(r_ws, 0, WS_EPOCH, AUX_SC1);
  SB();

  const int ntile = wave < 4 ? ST : HT;
  const unsigned tstride = (HT == ST ? 8 : 4) * 8192u;
  unsigned toff = (unsigned)(h * SK + s * 2048) * 512u + (HT == ST ? wave * 8192u : (wave < 4 ? wave * 8192u : (4u * ST + wave - 4) * 8192u));
  const unsigned vk = c * 512 + g * 16, vv = g * 2048 + c * 16;
  u32x4 kr[8], vr[8];
  if (HT == 0) toff = wave < 4 ? toff : 0x40000000u;
  issue_k(kr, r_k, vk, toff); SB();
  issue_v(vr, r_v, vv, toff); SB();

  float mean_b, den_b;
  {
    float xs[8];
#pragma unroll
    for (int i = 0; i < 4; ++i) { xs[2 * i] = bf_lo(xv[i]); xs[2 * i + 1] = bf_hi(xv[i]); }
    float sm = 0.f;
#pragma unroll
    for (int i = 0; i < 8; ++i) sm += xs[i];
    const float mean = wave_sum(sm) * (1.0f / D);
    float sq = 0.f;
#pragma unroll
    for (int i = 0; i < 8; ++i) { const float d = xs[i] - mean; sq = fmaf(d, d, sq); }
    sq = lane < D / 8 ? sq : 0.f;
    const float var = wave_sum(sq) * (1.0f / D);
    mean_b = rnd_bf16(mean);
    den_b = rnd_bf16(sqrtf(rnd_bf16(rnd_bf16(var) + 1e-5f)));
  }
  const float xn0 = rnd_bf16(rnd_bf16(bf_lo(xr0) - mean_b) / den_b);
  const float xn1 = row0 + 256 < D ? rnd_bf16(rnd_bf16(bf_lo(xr1) - mean_b) / den_b) : 0.f;
  const unsigned tag = __builtin_amdgcn_readfirstlane(epoch) + 1u;
  {
    float sacc[8] = {0.f, 0.f, 0.f, 0.f, 0.f, 0.f, 0.f, 0.f};
    fma8(sacc, xn0, wq0); fma8(sacc, xn1, wq1);
    half_sum8(sacc);
    if ((lane & 31) == 0) {
      f32x4* p = reinterpret_cast<f32x4*>(lds + L_RED + wave * 64 + hf * 32);
      p[0] = f32x4{sacc[0], sacc[1], sacc[2], sacc[3]}; p[1] = f32x4{sacc[4], sacc[5], sacc[6], sacc[7]};
    }
    if (tid == 0) *reinterpret_cast<unsigned*>(lds + L_FLAG) = 0u;
  }
  lds_barrier();
  bool timed_out = false;
  if (wave == 7) {
    if (lane < 8) {
      float q0 = 0.f, q1 = 0.f;
#pragma unroll
      for (int w = 0; w < 8; ++w) { const f32x2 t = *reinterpret_cast<const f32x2*>(lds + L_RED + w * 64 + lane * 8); q0 += t[0]; q1 += t[1]; }
      __builtin_amdgcn_raw_buffer_store_b64(u32x2{pk_bf16(q0, q1), tag}, r_ws, lane * 8, WS_QG + (h * 32 + qsl) * 64, AUX_SC1);
    }
    STAMP(1);
    u32x4 g0, g1;
    for (unsigned spin = 0;; ++spin) {
      asm volatile("" ::: "memory");
      g0 = __builtin_amdgcn_raw_buffer_load_b128(r_ws, lane * 32, WS_QG + h * 2048, AUX_SC1);
      g1 = __builtin_amdgcn_raw_buffer_load_b128(r_ws, lane * 32 + 16, WS_QG + h * 2048, AUX_SC1);
      const bool ok = g0[1] == tag && g0[3] == tag && g1[1] == tag && g1[3] == tag;
      if (__builtin_amdgcn_ballot_w64(!ok) == 0) break;
      if (spin > SPIN_CAP) { timed_out = true; break; }
      __builtin_amdgcn_s_sleep(1);
    }
    *reinterpret_cast<u32x4*>(lds + L_Q + lane * 16) = u32x4{g0[0], g0[2], g1[0], g1[2]};
  }
  lds_barrier();
  STAMP(2);
  u32x4 qf[8];
#pragma unroll
  for (int i = 0; i < 8; ++i) qf[i] = *reinterpret_cast<const u32x4*>(lds + L_Q + (c & 1) * 512 + (4 * i + g) * 16);

  float m = NEG_BIG, mc = NEG_BIG * C2, l = 0.f;
  f32x4 acc0 = {0.f, 0.f, 0.f, 0.f}, acc1 = {0.f, 0.f, 0.f, 0.f};
  const int msel = c >> 1;
  for (int it = 0; it < ntile; ++it) {
    toff = it + 1 < ntile ? toff + tstride : 0x40000000u;
    f32x4 sc = {0.f, 0.f, 0.f, 0.f};
#pragma unroll
    for (int i = 0; i < 8; ++i)
      sc = __builtin_amdgcn_mfma_f32_16x16x32_bf16(__builtin_bit_cast(bf16x8, kr[i]), __builtin_bit_cast(bf16x8, qf[i]), sc, 0, 0, 0);
    SB();
    issue_k(kr, r_k, vk, toff);
    SB();
    const unsigned s01 = pk_bf16(sc[0], sc[1]), s23 = pk_bf16(sc[2], sc[3]);
    const float s0 = bf_lo(s01), s1 = bf_hi(s01), s2 = bf_lo(s23), s3 = bf_hi(s23);
    float mx = fmaxf(fmaxf(s0, s1), fmaxf(s2, s3));
    if (__builtin_amdgcn_ballot_w64(mx > m) != 0) {
      mx = x32_max(x16_max(mx));
      const float mn = fmaxf(m, mx);
      const float al = __builtin_amdgcn_exp2f((m - mn) * C2);
      const float ot = dpp<XOR1>(al);
      const float a0 = (c & 1) ? ot : al, a1 = (c & 1) ? al : ot;
      m = mn; mc = mn * C2; l *= al;
      acc0[0] *= a0; acc0[1] *= a1; acc0[2] *= a0; acc0[3] *= a1;
      acc1[0] *= a0; acc1[1] *= a1; acc1[2] *= a0; acc1[3] *= a1;
    }
    const float p0 = __builtin_amdgcn_exp2f(fmaf(s0, C2, -mc)), p1 = __builtin_amdgcn_exp2f(fmaf(s1, C2, -mc));
    const float p2 = __builtin_amdgcn_exp2f(fmaf(s2, C2, -mc)), p3 = __builtin_amdgcn_exp2f(fmaf(s3, C2, -mc));
    l += (p0 + p1) + (p2 + p3);
    const unsigned pk0 = pk_bf16(p0, p1), pk1 = pk_bf16(p2, p3);
#pragma unroll
    for (int q = 0; q < 4; ++q) {
      const u32x4 pa = {msel == 2 * q ? pk0 : 0u, msel == 2 * q ? pk1 : 0u, msel == 2 * q + 1 ? pk0 : 0u, msel == 2 * q + 1 ? pk1 : 0u};
      constexpr unsigned SEL_LO = 0x05040100u, SEL_HI = 0x07060302u;
      const unsigned sel = (q & 1) ? SEL_HI : SEL_LO;
      const int d0 = q >> 1, d1 = 2 + (q >> 1);
      const u32x4 b0 = {__builtin_amdgcn_perm(vr[2][d0], vr[0][d0], sel), __builtin_amdgcn_perm(vr[6][d0], vr[4][d0], sel),
                        __builtin_amdgcn_perm(vr[3][d0], vr[1][d0], sel), __builtin_amdgcn_perm(vr[7][d0], vr[5][d0], sel)};
      const u32x4 b1 = {__builtin_amdgcn_perm(vr[2][d1], vr[0][d1], sel), __builtin_amdgcn_perm(vr[6][d1], vr[4][d1], sel),
                        __builtin_amdgcn_perm(vr[3][d1], vr[1][d1], sel), __builtin_amdgcn_perm(vr[7][d1], vr[5][d1], sel)};
      acc0 = __builtin_amdgcn_mfma_f32_16x16x32_bf16(__builtin_bit_cast(bf16x8, pa), __builtin_bit_cast(bf16x8, b0), acc0, 0, 0, 0);
      acc1 = __builtin_amdgcn_mfma_f32_16x16x32_bf16(__builtin_bit_cast(bf16x8, pa), __builtin_bit_cast(bf16x8, b1), acc1, 0, 0, 0);
    }
    SB();
    issue_v(vr, r_v, vv, toff);
    SB();
  }
  STAMP(3);
  {
    const float lt = x32_sum(x16_sum(l));
    if (lane < 2) *reinterpret_cast<f32x2*>(lds + L_ML + wave * 16 + lane * 8) = f32x2{m, lt};
    float* rec = reinterpret_cast<float*>(lds + L_ACC + wave * 2048) + 8 * c + g;
#pragma unroll
    for (int k = 0; k < 4; ++k) { rec[(k & 1) * 256 + (k >> 1) * 128] = acc0[k]; rec[(k & 1) * 256 + (k >> 1) * 128 + 4] = acc1[k]; }
  }

  const int u = tid - 256;
  const bool is_red = wg < NRED;
  unsigned wop0[8], wop1[8];
  if (wave >= 4) {
    const rsrc_t r_wo = mk_rsrc(a.wo, NQ * D * 2);
    const unsigned coff = u < D / 2 ? u * 4 : 0x40000000u;
    unsigned wraw[16];
#pragma unroll
    for (int j = 0; j < 16; ++j) wraw[j] = __builtin_amdgcn_raw_buffer_load_b32(r_wo, coff, (h * 512 + s * 16 + j) * (D * 2), AUX_NT);
    if ((wg >> 3) < 28) {
      const int fs = 28 * (wg & 7) + (wg >> 3);
      const rsrc_t r_g = mk_rsrc(a.wg, D * FF * 2), r_u = mk_rsrc(a.wu, D * FF * 2), r_d = mk_rsrc(a.wd, FF * D * 2);
      const int fr = (wave - 4) * 64 + lane;
      const unsigned goff = (unsigned)fr * (FF * 2) + fs * 16;
      const u32x4 g0 = __builtin_amdgcn_raw_buffer_load_b128(r_g, goff, 0, AUX_NT), g1 = __builtin_amdgcn_raw_buffer_load_b128(r_g, goff, 256 * FF * 2, AUX_NT);
      const u32x4 u0 = __builtin_amdgcn_raw_buffer_load_b128(r_u, goff, 0, AUX_NT), u1 = __builtin_amdgcn_raw_buffer_load_b128(r_u, goff, 256 * FF * 2, AUX_NT);
      const unsigned short y0 = __builtin_amdgcn_raw_buffer_load_b16(r_x, fr * 2, 0, 0), y1 = __builtin_amdgcn_raw_buffer_load_b16(r_x, fr * 2, 512, 0);
      unsigned draw[8];
#pragma unroll
      for (int j = 0; j < 8; ++j) draw[j] = __builtin_amdgcn_raw_buffer_load_b32(r_d, coff, (fs * 8 + j) * (D * 2), AUX_NT);
      const float yn0 = rnd_bf16(rnd_bf16(bf_lo(y0) - mean_b) / den_b);
      const float yn1 = fr + 256 < D ? rnd_bf16(rnd_bf16(bf_lo(y1) - mean_b) / den_b) : 0.f;
      float sg[8] = {0.f, 0.f, 0.f, 0.f, 0.f, 0.f, 0.f, 0.f}, su[8] = {0.f, 0.f, 0.f, 0.f, 0.f, 0.f, 0.f, 0.f};
      fma8(sg, yn0, g0); fma8(sg, yn1, g1); fma8(su, yn0, u0); fma8(su, yn1, u1);
#pragma unroll
      for (int i = 0; i < 8; ++i) { sg[i] = wave_sum(sg[i]); su[i] = wave_sum(su[i]); }
      if (lane == 0) {
        f32x4* p = reinterpret_cast<f32x4*>(lds + L_RED2 + (wave - 4) * 64);
        p[0] = f32x4{sg[0], sg[1], sg[2], sg[3]}; p[1] = f32x4{sg[4], sg[5], sg[6], sg[7]};
        p[2] = f32x4{su[0], su[1], su[2], su[3]}; p[3] = f32x4{su[4], su[5], su[6], su[7]};
        asm volatile("s_waitcnt lgkmcnt(0)" ::: "memory");
        __hip_atomic_fetch_add(reinterpret_cast<unsigned*>(lds + L_FLAG), 1u, __ATOMIC_RELAXED, __HIP_MEMORY_SCOPE_WORKGROUP);
      }
      for (unsigned spin = 0; spin < SPIN_CAP; ++spin) {
        if (__hip_atomic_load(reinterpret_cast<unsigned*>(lds + L_FLAG), __ATOMIC_RELAXED, __HIP_MEMORY_SCOPE_WORKGROUP) >= 4u) break;
        __builtin_amdgcn_s_sleep(1);
      }
      asm volatile("" ::: "memory");
      float yb0 = 0.f, yb1 = 0.f;
#pragma unroll
      for (int j = 0; j < 8; ++j) {
        float gs = 0.f, us = 0.f;
#pragma unroll
        for (int w = 0; w < 4; ++w) { gs += *reinterpret_cast<const float*>(lds + L_RED2 + w * 64 + j * 4); us += *reinterpret_cast<const float*>(lds + L_RED2 + w * 64 + 32 + j * 4); }
        gs = rnd_bf16(gs); us = rnd_bf16(us);
        const float hv = rnd_bf16((gs > 0.f ? gs : gs * 0.f) * us);
        yb0 = fmaf(hv, bf_lo(draw[j]), yb0); yb1 = fmaf(hv, bf_hi(draw[j]), yb1);
      }
      const unsigned yoff = u < D / 2 ? (unsigned)((2 * u / CPR) * (224 * CPR) + (2 * u % CPR)) * 8 : 0x40000000u;
      __builtin_amdgcn_raw_buffer_store_b128(u32x4{__float_as_uint(yb0), tag, __float_as_uint(yb1), tag}, r_ws, yoff, WS_YB + fs * (CPR * 8), AUX_SC1);
    }
#pragma unroll
    for (int j = 0; j < 8; ++j) { wop0[j] = __builtin_amdgcn_perm(wraw[2 * j + 1], wraw[2 * j], 0x05040100u); wop1[j] = __builtin_amdgcn_perm(wraw[2 * j + 1], wraw[2 * j], 0x07060302u); }
    if (is_red) {
      constexpr int NLD = (224 * CPR / 2 + 255) / 256;
      float b0, b1;
      for (unsigned spin = 0;; ++spin) {
        asm volatile("" ::: "memory");
        bool ok = true; b0 = 0.f; b1 = 0.f;
        u32x4 t[NLD];
#pragma unroll
        for (int k = 0; k < NLD; ++k) t[k] = __builtin_amdgcn_raw_buffer_load_b128(r_ws, (u + 256 * k) * 16, WS_YB + wg * (224 * CPR * 8), AUX_SC1);
#pragma unroll
        for (int k = 0; k < NLD; ++k) {
          const bool live = u + 256 * k < 224 * CPR / 2;
          ok = ok && (!live || (t[k][1] == tag && t[k][3] == tag));
          b0 += live ? __uint_as_float(t[k][0]) : 0.f; b1 += live ? __uint_as_float(t[k][2]) : 0.f;
        }
        if (__builtin_amdgcn_ballot_w64(!ok) == 0) break;
        if (spin > SPIN_CAP) { timed_out = true; break; }
        __builtin_amdgcn_s_sleep(32);
      }
      if (CPR / 2 <= 8) { b0 += dpp<XOR8>(b0); b1 += dpp<XOR8>(b1); }
      if (CPR / 2 <= 4) { b0 += dpp<ROR4>(b0); b1 += dpp<ROR4>(b1); }
      if (CPR / 2 <= 2) { b0 += dpp<XOR2>(b0); b1 += dpp<XOR2>(b1); }
      if (CPR / 2 <= 1) { b0 += dpp<XOR1>(b0); b1 += dpp<XOR1>(b1); }
      b0 = x32_sum(x16_sum(b0)); b1 = x32_sum(x16_sum(b1));
      if (timed_out) b0 = __builtin_nanf("");
      if (lane < CPR / 2) *reinterpret_cast<f32x2*>(lds + L_YB + (wave - 4) * 64 + lane * 8) = f32x2{b0, b1};
    }
  }
  STAMP(4);
  lds_barrier();
  STAMP(5);
  {
    const int hd = tid >> 8, dim = tid & 255;
    float mw[8], lw[8];
#pragma unroll
    for (int w = 0; w < 8; ++w) { const f32x2 t = *reinterpret_cast<const f32x2*>(lds + L_ML + w * 16 + hd * 8); mw[w] = t[0]; lw[w] = t[1]; }
    float M = mw[0];
#pragma unroll
    for (int w = 1; w < 8; ++w) M = fmaxf(M, mw[w]);
    float O = 0.f, L = 0.f;
#pragma unroll
    for (int w = 0; w < 8; ++w) {
      const float wt = __builtin_amdgcn_exp2f((mw[w] - M) * C2);
      O = fmaf(wt, *reinterpret_cast<const float*>(lds + L_ACC + w * 2048 + tid * 4), O);
      L = fmaf(wt, lw[w], L);
    }
    __builtin_amdgcn_raw_buffer_store_b64(u32x2{__float_as_uint(O), tag}, r_ws, (tid >> 4) * 4096 + (tid & 15) * 8, WS_PO + h * (32 * 4096) + s * 128, AUX_SC1);
    if (dim == 0) __builtin_amdgcn_raw_buffer_store_b128(u32x4{__float_as_uint(M), tag, __float_as_uint(L), tag}, r_ws, hd * 512, WS_ML + h * 1024 + s * 16, AUX_SC1);
  }
  STAMP(6);
  if (wave >= 4) {
    u32x4 po[4], ml[4];
    for (unsigned spin = 0;; ++spin) {
      asm volatile("" ::: "memory");
#pragma unroll
      for (int k = 0; k < 4; ++k) {
        po[k] = __builtin_amdgcn_raw_buffer_load_b128(r_ws, lane * 16 + k * 1024, WS_PO + (h * 32 + s) * 4096, AUX_SC1);
        ml[k] = __builtin_amdgcn_raw_buffer_load_b128(r_ws, (lane >> 3) * 16 + k * 128, WS_ML + h * 1024 + (s >> 4) * 512, AUX_SC1);
      }
      bool ok = true;
#pragma unroll
      for (int k = 0; k < 4; ++k) ok = ok && po[k][1] == tag && po[k][3] == tag && ml[k][1] == tag && ml[k][3] == tag;
      if (__builtin_amdgcn_ballot_w64(!ok) == 0) break;
      if (spin > SPIN_CAP) { timed_out = true; break; }
      __builtin_amdgcn_s_sleep(1);
    }
    STAMP(7);
    float M = fmaxf(fmaxf(__uint_as_float(ml[0][0]), __uint_as_float(ml[1][0])), fmaxf(__uint_as_float(ml[2][0]), __uint_as_float(ml[3][0])));
    M = fmaxf(M, dpp<XOR8>(M)); M = x32_max(x16_max(M));
    float o0 = 0.f, o1 = 0.f, L = 0.f;
#pragma unroll
    for (int k = 0; k < 4; ++k) {
      const float wt = __builtin_amdgcn_exp2f((__uint_as_float(ml[k][0]) - M) * C2);
      o0 = fmaf(wt, __uint_as_float(po[k][0]), o0); o1 = fmaf(wt, __uint_as_float(po[k][2]), o1); L = fmaf(wt, __uint_as_float(ml[k][2]), L);
    }
    o0 += dpp<XOR8>(o0); o1 += dpp<XOR8>(o1); L += dpp<XOR8>(L);
    o0 = x32_sum(x16_sum(o0)); o1 = x32_sum(x16_sum(o1)); L = x32_sum(x16_sum(L));
    const float inv = 1.0f / L;
    const unsigned opk = pk_bf16(o0 * inv, o1 * inv);
    float y0 = 0.f, y1 = 0.f;
#pragma unroll
    for (int j = 0; j < 8; ++j) { const unsigned oj = __builtin_amdgcn_readlane(opk, j); y0 = dot2(wop0[j], oj, y0); y1 = dot2(wop1[j], oj, y1); }
    if (timed_out) y0 = __builtin_nanf("");
    const unsigned yoff = u < D / 2 ? (unsigned)((2 * u / CPR) * (256 * CPR) + (2 * u % CPR)) * 8 : 0x40000000u;
    __builtin_amdgcn_raw_buffer_store_b128(u32x4{__float_as_uint(y0), tag, __float_as_uint(y1), tag}, r_ws, yoff, WS_YA + wg * (CPR * 8), AUX_SC1);
    STAMP(8);
  }
  if (!is_red) return;
  {
    constexpr int NLD = (256 * CPR / 2 + NTHR - 1) / NTHR;
    float b0, b1;
    for (unsigned spin = 0;; ++spin) {
      asm volatile("" ::: "memory");
      bool ok = true; b0 = 0.f; b1 = 0.f;
      u32x4 t[NLD];
#pragma unroll
      for (int k = 0; k < NLD; ++k) t[k] = __builtin_amdgcn_raw_buffer_load_b128(r_ws, (tid + NTHR * k) * 16, WS_YA + wg * (256 * CPR * 8), AUX_SC1);
#pragma unroll
      for (int k = 0; k < NLD; ++k) {
        const bool live = tid + NTHR * k < 256 * CPR / 2;
        ok = ok && (!live || (t[k][1] == tag && t[k][3] == tag));
        b0 += live ? __uint_as_float(t[k][0]) : 0.f; b1 += live ? __uint_as_float(t[k][2]) : 0.f;
      }
      if (__builtin_amdgcn_ballot_w64(!ok) == 0) break;
      if (spin > SPIN_CAP) { timed_out = true; break; }
      __builtin_amdgcn_s_sleep(1);
    }
    STAMP(9);
    if (CPR / 2 <= 8) { b0 += dpp<XOR8>(b0); b1 += dpp<XOR8>(b1); }
    if (CPR / 2 <= 4) { b0 += dpp<ROR4>(b0); b1 += dpp<ROR4>(b1); }
    if (CPR / 2 <= 2) { b0 += dpp<XOR2>(b0); b1 += dpp<XOR2>(b1); }
    if (CPR / 2 <= 1) { b0 += dpp<XOR1>(b0); b1 += dpp<XOR1>(b1); }
    b0 = x32_sum(x16_sum(b0)); b1 = x32_sum(x16_sum(b1));
    if (timed_out) b0 = __builtin_nanf("");
    if (lane < CPR / 2) *reinterpret_cast<f32x2*>(lds + L_YA + wave * 64 + lane * 8) = f32x2{b0, b1};
    lds_barrier();
    if (tid < CPR) {
      float ya = 0.f, yb = 0.f;
#pragma unroll
      for (int w = 0; w < 8; ++w) ya += *reinterpret_cast<const float*>(lds + L_YA + w * 64 + tid * 4);
#pragma unroll
      for (int w = 0; w < 4; ++w) yb += *reinterpret_cast<const float*>(lds + L_YB + w * 64 + tid * 4);
      const int col = wg * CPR + tid;
      const float xo = bf_lo(a.x[col]);
      const float r = rnd_bf16(xo + rnd_bf16(ya)) + rnd_bf16(yb);
      a.out[col] = (unsigned short)(pk_bf16(r, r) & 0xffffu);
    }
    if (wg == 0 && tid == 64) __builtin_amdgcn_raw_buffer_store_b32(tag, r_ws, 0, WS_EPOCH, AUX_SC1);
    STAMP(10);
  }
}

}

extern "C" void kernel_launch(void* const* d_in, const int* in_sizes, int n_in, void* d_out, int out_size, void* d_ws, size_t ws_size,
                              hipStream_t stream) {
  (void)in_sizes; (void)n_in; (void)out_size; (void)ws_size;
  Args a;
  a.x = (const unsigned short*)d_in[0];
  a.k = (const unsigned char*)d_in[1];
  a.v = (const unsigned char*)d_in[2];
  a.wq = (const unsigned char*)d_in[3];
  a.wo = (const unsigned char*)d_in[4];
  a.wg = (const unsigned char*)d_in[6];
  a.wu = (const unsigned char*)d_in[7];
  a.wd = (const unsigned char*)d_in[8];
  a.out = (unsigned short*)d_out;
  a.ws = (unsigned char*)d_ws;
  (void)hipFuncSetAttribute((const void*)fused_decode, hipFuncAttributeMaxDynamicSharedMemorySize, LDS_BYTES);
  hipLaunchKernelGGL(fused_decode, dim3(NWG), dim3(NTHR), LDS_BYTES, stream, a);
}
```

```cpp
#include <hip/hip_runtime.h>
#include <hip/hip_bf16.h>
#include <cstdint>

namespace {

typedef unsigned u32x4 __attribute__((ext_vector_type(4)));
typedef unsigned u32x2 __attribute__((ext_vector_type(2)));
typedef float f32x4 __attribute__((ext_vector_type(4)));
typedef float f32x2 __attribute__((ext_vector_type(2)));
typedef __bf16 bf16x8 __attribute__((ext_vector_type(8)));
typedef __bf16 bf16x2 __attribute__((ext_vector_type(2)));
typedef __amdgpu_buffer_rsrc_t rsrc_t;

constexpr int D = 448, SK = 65536, DH = 256, FF = 1792, NQ = 4096;
constexpr int NWG = 256, NTHR = 512;
#define CH_W 7
#define NI 5
#define PRETOUCH 1
#define HK_PRIO 2
#define ST_PRIO 0
#define CP_V 48
#define CP_WAIT 300
#define CP_GAIN 16
#define CP_OFF 30
#define LASTFAST 0
#define WAVEBAL 1
#define Q_FIRST 300
#define Q_SLEEP 0
#define GU_AT 2600
#define CPR 8
constexpr int NRED = D / CPR;
constexpr unsigned TILE = 8192;
constexpr unsigned OOB = 0x40000000u;
constexpr float C2 = 0.0625f * 1.4426950408889634f;
constexpr unsigned SPIN_CAP = 1u << 22;

constexpr unsigned WS_EPOCH = 0;
constexpr unsigned LSTR = 4352;
constexpr unsigned WS_PO = 4096;
constexpr unsigned WS_YA = WS_PO + 8 * 32 * 32 * 16 * 8;
constexpr unsigned WS_GU = WS_YA + 256 * D * 8;
constexpr unsigned WS_QG = WS_GU + 224 * 4 * 128;
constexpr unsigned WS_ML = WS_QG + 128 * LSTR;
constexpr unsigned WS_END = WS_ML + 256 * LSTR;

constexpr int L_RED = 0;
constexpr int L_CTL = 1024;
constexpr int C_PART = L_CTL, C_QRDY = L_CTL + 4, C_TAG = L_CTL + 12, C_LANDED = L_CTL + 16, C_KHELP = L_CTL + 20, C_VDONE = L_CTL + 24, C_FAST = L_CTL + 28,
              C_HRDY = L_CTL + 32, C_HOP1 = L_CTL + 36;
constexpr int L_ML = 1088;
constexpr int L_SC = 1152;
constexpr int L_H = 1184;
constexpr int L_YA = 1216;
constexpr int L_Q = 1792;
constexpr int L_XN = 2816;
constexpr int L_ACC = 4864;
constexpr int L_P = L_ACC + 4 * 2048;
constexpr int LDS_BYTES = 84 * 1024;

constexpr int AUX_NT = 2, AUX_SC1 = 16;
#define SB() __builtin_amdgcn_sched_barrier(0)
#define FENCE() asm volatile("" ::: "memory")

__device__ __forceinline__ float bf_lo(unsigned u) { return __builtin_bit_cast(float, u << 16); }
__device__ __forceinline__ float bf_hi(unsigned u) { return __builtin_bit_cast(float, u & 0xffff0000u); }
__device__ __forceinline__ unsigned pk_bf16(float lo, float hi) {
  bf16x2 v = {(__bf16)lo, (__bf16)hi};
  return __builtin_bit_cast(unsigned, v);
}
__device__ __forceinline__ float rnd_bf16(float x) { return (float)(__bf16)x; }
__device__ __forceinline__ float dot2(unsigned a, unsigned b, float acc) {
  return __builtin_amdgcn_fdot2_f32_bf16(__builtin_bit_cast(bf16x2, a), __builtin_bit_cast(bf16x2, b), acc, false);
}
template <int CTRL> __device__ __forceinline__ float dpp(float x) {
  return __builtin_bit_cast(float, __builtin_amdgcn_mov_dpp(__builtin_bit_cast(int, x), CTRL, 0xf, 0xf, true));
}
constexpr int XOR1 = 0xB1, XOR2 = 0x4E, XOR7 = 0x141, XOR15 = 0x140, XOR8 = 0x128, ROR4 = 0x124;
__device__ __forceinline__ float x16_max(float x) {
  auto s = __builtin_amdgcn_permlane16_swap(__float_as_uint(x), __float_as_uint(x), false, false);
  return fmaxf(__uint_as_float(s[0]), __uint_as_float(s[1]));
}
__device__ __forceinline__ float x32_max(float x) {
  auto s = __builtin_amdgcn_permlane32_swap(__float_as_uint(x), __float_as_uint(x), false, false);
  return fmaxf(__uint_as_float(s[0]), __uint_as_float(s[1]));
}
__device__ __forceinline__ float x16_sum(float x) {
  auto s = __builtin_amdgcn_permlane16_swap(__float_as_uint(x), __float_as_uint(x), false, false);
  return __uint_as_float(s[0]) + __uint_as_float(s[1]);
}
__device__ __forceinline__ float x32_sum(float x) {
  auto s = __builtin_amdgcn_permlane32_swap(__float_as_uint(x), __float_as_uint(x), false, false);
  return __uint_as_float(s[0]) + __uint_as_float(s[1]);
}
__device__ __forceinline__ float wave_sum(float x) {
  x += dpp<XOR1>(x); x += dpp<XOR2>(x); x += dpp<XOR7>(x); x += dpp<XOR15>(x);
  return x32_sum(x16_sum(x));
}
__device__ __forceinline__ float parity_sum(float x) {
  x += dpp<XOR2>(x); x += dpp<ROR4>(x); x += dpp<XOR8>(x);
  return x32_sum(x16_sum(x));
}
__device__ __forceinline__ float parity_max(float x) {
  x = fmaxf(x, dpp<XOR2>(x)); x = fmaxf(x, dpp<ROR4>(x)); x = fmaxf(x, dpp<XOR8>(x));
  return x32_max(x16_max(x));
}
__device__ __forceinline__ void lds_barrier() {
  asm volatile("s_waitcnt lgkmcnt(0)" ::: "memory");
  __builtin_amdgcn_s_barrier();
  FENCE();
}
__device__ __forceinline__ rsrc_t mk_rsrc(const void* p, unsigned bytes) {
  return __builtin_amdgcn_make_buffer_rsrc(const_cast<void*>(p), 0, bytes, 0x00020000);
}
__device__ __forceinline__ unsigned lds_ld(unsigned char* lds, int off) {
  return __builtin_amdgcn_readfirstlane(__hip_atomic_load(reinterpret_cast<unsigned*>(lds + off), __ATOMIC_RELAXED, __HIP_MEMORY_SCOPE_WORKGROUP));
}
template <int SLEEP = 1>
__device__ __forceinline__ void lds_wait_ge(unsigned char* lds, int off, unsigned want) {
#pragma nounroll
  for (unsigned spin = 0; spin < SPIN_CAP * 8u; ++spin) {
    if (lds_ld(lds, off) >= want) break;
    __builtin_amdgcn_s_sleep(SLEEP);
  }
  FENCE();
}
__device__ __forceinline__ void lds_arrive(unsigned char* lds, int off, int lane) {
  asm volatile("s_waitcnt lgkmcnt(0)" ::: "memory");
  if (lane == 0) __hip_atomic_fetch_add(reinterpret_cast<unsigned*>(lds + off), 1u, __ATOMIC_RELAXED, __HIP_MEMORY_SCOPE_WORKGROUP);
}
__device__ __forceinline__ unsigned now() { return (unsigned)__builtin_amdgcn_s_memrealtime(); }
template <int SLEEP>
__device__ __forceinline__ void sleep_until(unsigned t0, unsigned ticks) {
#pragma nounroll
  for (unsigned spin = 0; spin < SPIN_CAP && now() - t0 < ticks; ++spin) __builtin_amdgcn_s_sleep(SLEEP);
}
__device__ __forceinline__ void chain_wait(unsigned char* lds, int idx) {
#pragma nounroll
  for (unsigned spin = 0; spin < SPIN_CAP * 8u; ++spin) {
    if ((int)lds_ld(lds, C_LANDED) >= idx + 1 - CH_W || lds_ld(lds, C_QRDY) != 0u) break;
    __builtin_amdgcn_s_sleep(1);
  }
  FENCE();
}
template <int N, int SLEEP>
__device__ __forceinline__ bool gather(u32x4 (&v)[N], rsrc_t rs, const unsigned (&voff)[N], unsigned soff, unsigned tag) {
  bool have[N];
#pragma unroll
  for (int k = 0; k < N; ++k) { have[k] = voff[k] == OOB; v[k] = u32x4{0u, 0u, 0u, 0u}; }
#pragma nounroll
  for (unsigned spin = 0; spin < SPIN_CAP; ++spin) {
    FENCE();
    u32x4 t[N];
#pragma unroll
    for (int k = 0; k < N; ++k) t[k] = __builtin_amdgcn_raw_buffer_load_b128(rs, have[k] ? OOB : voff[k], soff, AUX_SC1);
    bool all = true;
#pragma unroll
    for (int k = 0; k < N; ++k) {
      const bool now = !have[k] && t[k][1] == tag && t[k][3] == tag;
      v[k][0] = now ? t[k][0] : v[k][0]; v[k][2] = now ? t[k][2] : v[k][2];
      have[k] = have[k] || now; all = all && have[k];
    }
    if (__builtin_amdgcn_ballot_w64(!all) == 0) return true;
    if (SLEEP > 0) __builtin_amdgcn_s_sleep(SLEEP);
  }
  return false;
}
#define STAMP(slot) do {} while (0)
#define PROG() do {} while (0)
#define PROG_OUT() do {} while (0)
#define PROGL() do {} while (0)

struct Args {
  const unsigned short* x;
  const unsigned char* k;
  const unsigned char* v;
  const unsigned char* wq;
  const unsigned char* wo;
  const unsigned char* wg;
  const unsigned char* wu;
  const unsigned char* wd;
  unsigned short* out;
  unsigned char* ws;
};

__host__ __device__ constexpr int piece(int e, int n) { return n < 2 ? 2 * e + n : 2 * n + e; }
__device__ __forceinline__ void issue(u32x4 (&r)[8], rsrc_t rs, unsigned v0, unsigned v1, unsigned soff) {
#pragma unroll
  for (int i = 0; i < 8; ++i) { r[i] = __builtin_amdgcn_raw_buffer_load_b128(rs, ((i & 1) ? v1 : v0) + (i >> 1) * 512, soff, AUX_NT); SB(); }
}
__device__ __forceinline__ f32x4 mfma(u32x4 x, u32x4 y, f32x4 acc) {
  return __builtin_amdgcn_mfma_f32_16x16x32_bf16(__builtin_bit_cast(bf16x8, x), __builtin_bit_cast(bf16x8, y), acc, 0, 0, 0);
}
__device__ __forceinline__ void qk_mfma(const u32x4 (&r)[8], const u32x4 (&qf)[2], f32x4 (&d)[4]) {
#pragma unroll
  for (int j = 0; j < 4; ++j) d[j] = mfma(qf[1], r[2 * j + 1], mfma(qf[0], r[2 * j], f32x4{0.f, 0.f, 0.f, 0.f}));
  SB();
}
__device__ __forceinline__ unsigned short qk_score(const f32x4 (&d)[4], bool b0, bool b1) {
  float v[4];
#pragma unroll
  for (int j = 0; j < 4; ++j) {
    float e0 = d[j][0], e1 = d[j][1], e2 = d[j][2], e3 = d[j][3];
    asm("" : "+v"(e0), "+v"(e2));
    const float lo = b0 ? e1 : e0, hi = b0 ? e3 : e2;
    v[j] = b1 ? hi : lo;
  }
  const float u0 = (b0 ? v[1] : v[0]) + dpp<XOR1>(b0 ? v[0] : v[1]);
  const float u1 = (b0 ? v[3] : v[2]) + dpp<XOR1>(b0 ? v[2] : v[3]);
  const float w = (b1 ? u1 : u0) + dpp<XOR2>(b1 ? u0 : u1);
  return (unsigned short)(pk_bf16(w, w) & 0xffffu);
}
__device__ __forceinline__ void pv_mfma(const u32x4 (&vr)[8], u32x2 pk, int msel, f32x4& acc0, f32x4& acc1) {
#pragma unroll
  for (int q = 0; q < 4; ++q) {
    const u32x4 pa = {msel == 2 * q ? pk[0] : 0u, msel == 2 * q ? pk[1] : 0u, msel == 2 * q + 1 ? pk[0] : 0u, msel == 2 * q + 1 ? pk[1] : 0u};
    const unsigned sel = (q & 1) ? 0x07060302u : 0x05040100u;
    const int d0 = q >> 1, d1 = 2 + (q >> 1);
    const u32x4 b0 = {__builtin_amdgcn_perm(vr[2][d0], vr[0][d0], sel), __builtin_amdgcn_perm(vr[6][d0], vr[4][d0], sel),
                      __builtin_amdgcn_perm(vr[3][d0], vr[1][d0], sel), __builtin_amdgcn_perm(vr[7][d0], vr[5][d0], sel)};
    const u32x4 b1 = {__builtin_amdgcn_perm(vr[2][d1], vr[0][d1], sel), __builtin_amdgcn_perm(vr[6][d1], vr[4][d1], sel),
                      __builtin_amdgcn_perm(vr[3][d1], vr[1][d1], sel), __builtin_amdgcn_perm(vr[7][d1], vr[5][d1], sel)};
    acc0 = mfma(pa, b0, acc0);
    acc1 = mfma(pa, b1, acc1);
  }
  SB();
}
__device__ __forceinline__ void fma8(float (&s)[8], float xv, u32x4 w) {
#pragma unroll
  for (int i = 0; i < 4; ++i) { s[2 * i] = fmaf(xv, bf_lo(w[i]), s[2 * i]); s[2 * i + 1] = fmaf(xv, bf_hi(w[i]), s[2 * i + 1]); }
}
template <bool WHOLE>
__device__ __forceinline__ void load_qf(u32x4 (&qf)[2], const unsigned char* lds, int c, int g) {
#pragma unroll
  for (int e = 0; e < 2; ++e) qf[e] = *reinterpret_cast<const u32x4*>(lds + L_Q + ((c >> 2) & 1) * 512 + ((WHOLE ? 4 * e + g : piece(e, g)) * 4 + (c & 3)) * 16);
}

__global__ void __launch_bounds__(NTHR, 2) fused_decode(Args a) {
  extern __shared__ __attribute__((aligned(16))) unsigned char lds[];
  const int tid = threadIdx.x, lane = tid & 63, wave = __builtin_amdgcn_readfirstlane(tid >> 6);
  const int wg = blockIdx.x, h = wg >> 5, s = wg & 31;
  const int c = lane & 15, g = lane >> 4;
  STAMP(0);
  const rsrc_t r_ws = mk_rsrc(a.ws, WS_END);
  const rsrc_t r_k = mk_rsrc(a.k, 8u * SK * DH * 2);
  const unsigned wg_off = (unsigned)wg * (128 * TILE);
  const unsigned vw0 = (c >> 2) * 2048 + g * 64 + (c & 3) * 16, vw1 = vw0 + 256;
  const bool b0 = (c & 1) != 0, b1 = (c & 2) != 0;
  const int sc_lane = (g & 1) * 32 + c * 2;
  bool timed_out = false;
  unsigned tag;
  unsigned wop0[8], wop1[8];
  float y0 = 0.f, y1 = 0.f;

  if (wave < 4) {
    __builtin_amdgcn_s_setprio(3);
    const unsigned t_start = now();
    const bool qwg = (s & 8) == 0;
    const rsrc_t r_x = mk_rsrc(a.x, D * 2);
    const rsrc_t r_wq = mk_rsrc(a.wq, qwg ? D * NQ * 2 : 0);
    const int qsl = (s & 7) * 2 + (s >> 4);
    const u32x4 xv = __builtin_amdgcn_raw_buffer_load_b128(r_x, lane * 16, 0, 0);
    const unsigned epoch = __builtin_amdgcn_raw_buffer_load_b32(r_ws, 0, WS_EPOCH, AUX_SC1);
    const int qrow = (lane >> 2) + 16 * wave;
    const unsigned wq_off = (unsigned)qrow * (NQ * 2) + (unsigned)(h * 512 + qsl * 32) * 2 + (lane & 3) * 16;
    u32x4 wv[7];
#pragma unroll
    for (int i = 0; i < 7; ++i) wv[i] = __builtin_amdgcn_raw_buffer_load_b128(r_wq, wq_off, i * 64 * NQ * 2, AUX_NT);
    SB();
    if (tid < 16) *reinterpret_cast<unsigned*>(lds + L_CTL + tid * 4) = 0u;
    lds_barrier();

    {
      float xs[8];
#pragma unroll
      for (int i = 0; i < 4; ++i) { xs[2 * i] = bf_lo(xv[i]); xs[2 * i + 1] = bf_hi(xv[i]); }
      float sm = 0.f;
#pragma unroll
      for (int i = 0; i < 8; ++i) sm += xs[i];
      const float mean = wave_sum(sm) * (1.0f / D);
      float sq = 0.f;
#pragma unroll
      for (int i = 0; i < 8; ++i) { const float d = xs[i] - mean; sq = fmaf(d, d, sq); }
      sq = lane < D / 8 ? sq : 0.f;
      const float var = wave_sum(sq) * (1.0f / D);
      const float mean_b = rnd_bf16(mean);
      const float den_b = rnd_bf16(sqrtf(rnd_bf16(rnd_bf16(var) + 1e-5f)));
#pragma unroll
      for (int i = 0; i < 8; ++i) xs[i] = rnd_bf16(rnd_bf16(xs[i] - mean_b) / den_b);
      f32x4* xp = reinterpret_cast<f32x4*>(lds + L_XN + lane * 32);
      xp[0] = f32x4{xs[0], xs[1], xs[2], xs[3]}; xp[1] = f32x4{xs[4], xs[5], xs[6], xs[7]};
      FENCE();
    }
    tag = __builtin_amdgcn_readfirstlane(epoch) + 1u;
    if (qwg) {
      float sacc[8] = {0.f, 0.f, 0.f, 0.f, 0.f, 0.f, 0.f, 0.f};
#pragma unroll
      for (int i = 0; i < 7; ++i) fma8(sacc, *reinterpret_cast<const float*>(lds + L_XN + (qrow + 64 * i) * 4), wv[i]);
#pragma unroll
      for (int i = 0; i < 8; ++i) { float t = sacc[i]; t += dpp<ROR4>(t); t += dpp<XOR8>(t); sacc[i] = x32_sum(x16_sum(t)); }
      if (lane < 4) {
        f32x4* p = reinterpret_cast<f32x4*>(lds + L_RED + wave * 256 + lane * 32);
        p[0] = f32x4{sacc[0], sacc[1], sacc[2], sacc[3]}; p[1] = f32x4{sacc[4], sacc[5], sacc[6], sacc[7]};
      }
      lds_arrive(lds, C_PART, lane);
      if (wave == 0) {
        lds_wait_ge(lds, C_PART, 4u);
        if (lane < 16) {
          float q0 = 0.f, q1 = 0.f;
#pragma unroll
          for (int w = 0; w < 4; ++w) { const f32x2 t = *reinterpret_cast<const f32x2*>(lds + L_RED + w * 256 + lane * 8); q0 += t[0]; q1 += t[1]; }
          __builtin_amdgcn_raw_buffer_store_b64(u32x2{pk_bf16(q0, q1), tag}, r_ws, lane * 8, WS_QG + (h * 16 + qsl) * LSTR, AUX_SC1);
        }
        STAMP(1);
      }
    }
    if (wave == 0) {
      u32x4 qg[2];
      const unsigned qo[2] = {(lane >> 2) * LSTR + (lane & 3) * 32, (lane >> 2) * LSTR + (lane & 3) * 32 + 16};
      sleep_until<1>(t_start, Q_FIRST);
      timed_out = !gather<2, Q_SLEEP>(qg, r_ws, qo, WS_QG + h * 16 * LSTR, tag);
      *reinterpret_cast<u32x4*>(lds + L_Q + lane * 16) = u32x4{qg[0][0], qg[0][2], qg[1][0], qg[1][2]};
      if (lane == 0) *reinterpret_cast<unsigned*>(lds + C_TAG) = tag;
      lds_arrive(lds, C_QRDY, lane);
    }
    if (wave != 0) {
      __builtin_amdgcn_s_setprio(HK_PRIO);
      const int slot = NI + wave - 1;
      u32x4 kt[4][8];
#pragma unroll
      for (int i = 0; i < 4; ++i) { chain_wait(lds, 4 * slot + i); issue(kt[i], r_k, vw0, vw1, wg_off + (4 * slot + i) * TILE); }
      asm volatile("s_waitcnt vmcnt(24)" ::: "memory"); lds_arrive(lds, C_LANDED, lane); PROGL();
      asm volatile("s_waitcnt vmcnt(16)" ::: "memory"); lds_arrive(lds, C_LANDED, lane); PROGL();
      asm volatile("s_waitcnt vmcnt(8)" ::: "memory"); lds_arrive(lds, C_LANDED, lane); PROGL();
      asm volatile("s_waitcnt vmcnt(0)" ::: "memory"); lds_arrive(lds, C_LANDED, lane); PROGL();
      lds_wait_ge(lds, C_QRDY, 1u);
      u32x4 qf[2];
      load_qf<true>(qf, lds, c, g);
#pragma unroll
      for (int i = 0; i < 4; ++i) {
        f32x4 d[4];
        qk_mfma(kt[i], qf, d);
        *reinterpret_cast<unsigned short*>(lds + L_P + i * 2048 + slot * 64 + sc_lane) = qk_score(d, b0, b1);
        PROG();
      }
      lds_arrive(lds, C_KHELP, lane);
      __builtin_amdgcn_s_setprio(3);
    }
    STAMP(3);
    const int fb = h * 16 + (s & 7) + ((s >> 4) << 3);
    if (!qwg && fb < 112) {
      const rsrc_t r_gu = mk_rsrc((wave & 1) ? a.wu : a.wg, D * FF * 2);
      const int frow = (fb / 28) * 112 + (wave >> 1) * 56 + (lane >> 3);
      const unsigned goff = (unsigned)frow * (FF * 2) + (fb % 28) * 128 + (lane & 7) * 16;
#pragma unroll
      for (int i = 0; i < 7; ++i) wv[i] = __builtin_amdgcn_raw_buffer_load_b128(r_gu, goff, i * 8 * FF * 2, AUX_NT);
      float sg[8] = {0.f, 0.f, 0.f, 0.f, 0.f, 0.f, 0.f, 0.f};
#pragma unroll
      for (int i = 0; i < 7; ++i) fma8(sg, *reinterpret_cast<const float*>(lds + L_XN + (frow + 8 * i) * 4), wv[i]);
#pragma unroll
      for (int i = 0; i < 8; ++i) { float t = sg[i]; t += dpp<XOR8>(t); sg[i] = x32_sum(x16_sum(t)); }
      if (lane < 8) {
        f32x4* p = reinterpret_cast<f32x4*>(lds + L_RED + wave * 256 + lane * 32);
        p[0] = f32x4{sg[0], sg[1], sg[2], sg[3]}; p[1] = f32x4{sg[4], sg[5], sg[6], sg[7]};
      }
      lds_arrive(lds, C_PART, lane);
      if (wave == 1) {
        lds_wait_ge(lds, C_PART, 4u);
        const int o = ((lane >> 2) & 1) * 256 + (lane >> 3) * 32 + (lane & 3) * 8;
        const f32x2 t0 = *reinterpret_cast<const f32x2*>(lds + L_RED + o), t1 = *reinterpret_cast<const f32x2*>(lds + L_RED + 512 + o);
        __builtin_amdgcn_raw_buffer_store_b128(u32x4{__float_as_uint(t0[0] + t1[0]), tag, __float_as_uint(t0[1] + t1[1]), tag}, r_ws,
                                               (lane >> 3) * 512 + (lane & 7) * 16, WS_GU + (fb % 28) * 4096 + (fb / 28) * 128, AUX_SC1);
      }
    }
    {
      const rsrc_t r_wo = mk_rsrc(a.wo, NQ * D * 2);
      const unsigned coff = tid < D / 2 ? tid * 4 : OOB;
      const bool ffn = wg < FF / 8;
      const rsrc_t r_d = mk_rsrc(a.wd, ffn ? FF * D * 2 : 0);
      unsigned draw[8], wraw[16];
#pragma unroll
      for (int j = 0; j < 8; ++j) draw[j] = __builtin_amdgcn_raw_buffer_load_b32(r_d, coff, (wg * 8 + j) * (D * 2), AUX_NT);
#pragma unroll
      for (int j = 0; j < 16; ++j) wraw[j] = __builtin_amdgcn_raw_buffer_load_b32(r_wo, coff, (wg * 16 + j) * (D * 2), AUX_NT);
#pragma unroll
      for (int j = 0; j < 8; ++j) { wop0[j] = __builtin_amdgcn_perm(wraw[2 * j + 1], wraw[2 * j], 0x05040100u); wop1[j] = __builtin_amdgcn_perm(wraw[2 * j + 1], wraw[2 * j], 0x07060302u); }
      if (wave == 3) {
        sleep_until<32>(t_start, GU_AT);
        u32x4 gu[1];
        const unsigned go[1] = {(ffn && lane < 32) ? (unsigned)lane * 16 : OOB};
        if (!gather<1, 64>(gu, r_ws, go, WS_GU + wg * 512, tag)) timed_out = true;
        float g0 = __uint_as_float(gu[0][0]), g1 = __uint_as_float(gu[0][2]);
        g0 += dpp<XOR8>(g0); g1 += dpp<XOR8>(g1);
        g0 = rnd_bf16(x16_sum(g0)); g1 = rnd_bf16(x16_sum(g1));
        const float u0 = dpp<ROR4>(g0), u1 = dpp<ROR4>(g1);
        const float h0 = rnd_bf16((g0 > 0.f ? g0 : g0 * 0.f) * u0), h1 = rnd_bf16((g1 > 0.f ? g1 : g1 * 0.f) * u1);
        if (lane < 4) *reinterpret_cast<f32x2*>(lds + L_H + lane * 8) = f32x2{h0, h1};
        lds_arrive(lds, C_HRDY, lane);
      }
      lds_wait_ge<32>(lds, C_HRDY, 1u);
#pragma unroll
      for (int j = 0; j < 8; ++j) {
        const float hv = *reinterpret_cast<const float*>(lds + L_H + j * 4);
        y0 = fmaf(hv, bf_lo(draw[j]), y0); y1 = fmaf(hv, bf_hi(draw[j]), y1);
      }
      asm volatile("" : "+v"(y0), "+v"(y1), "+v"(wop0[0]), "+v"(wop0[1]), "+v"(wop0[2]), "+v"(wop0[3]), "+v"(wop0[4]), "+v"(wop0[5]), "+v"(wop0[6]), "+v"(wop0[7]),
                        "+v"(wop1[0]), "+v"(wop1[1]), "+v"(wop1[2]), "+v"(wop1[3]), "+v"(wop1[4]), "+v"(wop1[5]), "+v"(wop1[6]), "+v"(wop1[7]));
    }
    STAMP(11);
    if (wave == 2) {
      const rsrc_t r_v = mk_rsrc(a.v, 8u * SK * DH * 2), r_o = mk_rsrc(a.out, D * 2);
      const unsigned tv = __builtin_amdgcn_raw_buffer_load_b32(r_v, lane < 16 ? lane * 65536 : OOB, wg_off, AUX_NT);
      const unsigned to = __builtin_amdgcn_raw_buffer_load_b32(r_o, lane < 1 ? 0 : OOB, 0, AUX_NT);
      asm volatile("" :: "v"(tv), "v"(to));
    }
    if (wave == 1) {
      lds_wait_ge<8>(lds, C_VDONE, 0x10000u * 4u + CP_V);
      const unsigned t_cp = now();
      const int hd = lane & 1;
      float mw[4], lw[4];
#pragma unroll
      for (int w = 0; w < 4; ++w) { const f32x2 t = *reinterpret_cast<const f32x2*>(lds + L_ML + w * 16 + hd * 8); mw[w] = t[0]; lw[w] = t[1]; }
      const float M = fmaxf(fmaxf(mw[0], mw[1]), fmaxf(mw[2], mw[3]));
      float L = 0.f;
#pragma unroll
      for (int w = 0; w < 4; ++w) L = fmaf(__builtin_amdgcn_exp2f((mw[w] - M) * C2), lw[w], L);
      __builtin_amdgcn_raw_buffer_store_b128(u32x4{(lane & 2) ? t_cp : __float_as_uint(M), tag, (lane & 2) ? t_cp : __float_as_uint(L), tag}, r_ws,
                                             lane < 8 ? lane * 16 : OOB, WS_ML + wg * LSTR, AUX_SC1);
      sleep_until<16>(t_cp, CP_WAIT);
      u32x4 ml[2];
      const unsigned mo[2] = {(lane >> 2) * LSTR + (lane & 3) * 16, (16 + (lane >> 2)) * LSTR + (lane & 3) * 16};
      if (!gather<2, 32>(ml, r_ws, mo, WS_ML + h * 32 * LSTR, tag)) timed_out = true;
      const float Ma = __uint_as_float(ml[0][0]), Mb = __uint_as_float(ml[1][0]);
      float Mg = fmaxf(Ma, Mb);
      Mg = fmaxf(Mg, dpp<ROR4>(Mg)); Mg = fmaxf(Mg, dpp<XOR8>(Mg)); Mg = x32_max(x16_max(Mg));
      float Lg = __builtin_amdgcn_exp2f((Ma - Mg) * C2) * __uint_as_float(ml[0][2]) + __builtin_amdgcn_exp2f((Mb - Mg) * C2) * __uint_as_float(ml[1][2]);
      Lg = (lane & 2) ? (float)(int)(t_cp - ml[0][0]) + (float)(int)(t_cp - ml[1][0]) : Lg;
      Lg += dpp<ROR4>(Lg); Lg += dpp<XOR8>(Lg); Lg = x32_sum(x16_sum(Lg));
      if (lane < 2) {
#pragma unroll
        for (int w = 0; w < 4; ++w) *reinterpret_cast<float*>(lds + L_SC + w * 8 + lane * 4) = timed_out ? __builtin_nanf("") : __builtin_amdgcn_exp2f((mw[w] - Mg) * C2) / Lg;
      }
      if (lane == 2) {
        const float nf = fminf((Lg * (1.0f / 32) + (float)CP_OFF) * (0.01f * CP_GAIN), 128.f);
        if (nf >= 1.f) *reinterpret_cast<unsigned*>(lds + C_FAST) = (*reinterpret_cast<const unsigned*>(lds + C_VDONE) & 0xffffu) + 8u + (unsigned)nf;
      }
      STAMP(14);
    }
  } else {
    const int w = wave - 4;
    const rsrc_t r_v = mk_rsrc(a.v, 8u * SK * DH * 2);
    const unsigned vv0 = g * 2048 + piece(0, c >> 2) * 64 + (c & 3) * 16, vv1 = g * 2048 + piece(1, c >> 2) * 64 + (c & 3) * 16;
    const unsigned vk0 = (c >> 2) * 2048 + piece(0, g) * 64 + (c & 3) * 16, vk1 = (c >> 2) * 2048 + piece(1, g) * 64 + (c & 3) * 16;
    const unsigned t0 = wg_off + w * TILE;
    u32x4 ra[8], rb[8];
    __builtin_amdgcn_s_setprio(ST_PRIO);
    u32x4 rx[NI - 2][8];
    if (s & 8) issue(ra, r_k, vw0, vw1, t0);
    lds_barrier();
    if (!(s & 8)) issue(ra, r_k, vw0, vw1, t0);
#define CHAIN_STEP(R, SLOT) do { \
      constexpr bool first_ = CH_W < 5;     \
      if (first_ && lds_ld(lds, C_QRDY) == 0u) { asm volatile("s_waitcnt vmcnt(0)" ::: "memory"); lds_arrive(lds, C_LANDED, lane); PROGL(); } \
      chain_wait(lds, 4 * (SLOT) + w); \
      issue(R, r_k, vw0, vw1, t0 + 4 * (SLOT) * TILE); \
      if (!first_ && lds_ld(lds, C_QRDY) == 0u) { asm volatile("s_waitcnt vmcnt(8)" ::: "memory"); lds_arrive(lds, C_LANDED, lane); PROGL(); } } while (0)
    CHAIN_STEP(rb, 1);
#pragma unroll
    for (int i = 2; i < NI; ++i) CHAIN_STEP(rx[i - 2], i);
#undef CHAIN_STEP
    if (lds_ld(lds, C_QRDY) == 0u) { asm volatile("s_waitcnt vmcnt(0)" ::: "memory"); lds_arrive(lds, C_LANDED, lane); PROGL(); }
    lds_wait_ge(lds, C_QRDY, 1u);
    tag = lds_ld(lds, C_TAG);
    STAMP(2);
    u32x4 qf[2];
    load_qf<true>(qf, lds, c, g);
    unsigned char* const pl = lds + L_P + w * 2048;
    {
      f32x4 d[4];
      unsigned short* sw = reinterpret_cast<unsigned short*>(pl + sc_lane);
      unsigned toff = t0 + 4 * (NI + 3) * TILE;
#define K_STEP(R, ISSUE_NEXT, SLOT) do { qk_mfma(R, qf, d); PROG(); ISSUE_NEXT; sw[(SLOT) * 32] = qk_score(d, b0, b1); } while (0)
      K_STEP(ra, issue(ra, r_k, vk0, vk1, toff), 0);
      K_STEP(rb, issue(rb, r_k, vk0, vk1, toff + 4 * TILE), 1);
#pragma unroll
      for (int i = 2; i < NI; ++i) K_STEP(rx[i - 2], (void)0, i);
      load_qf<false>(qf, lds, c, g);
      sw += (NI + 3) * 32;
      for (int it = 1; it < (29 - NI) / 2; ++it) {
        toff += 8 * TILE;
        K_STEP(ra, issue(ra, r_k, vk0, vk1, toff), 0);
        K_STEP(rb, issue(rb, r_k, vk0, vk1, toff + 4 * TILE), 1);
        sw += 2 * 32;
      }
      K_STEP(ra, issue(ra, r_v, vv0, vv1, t0), 0);
      K_STEP(rb, issue(rb, r_v, vv0, vv1, t0 + 4 * TILE), 1);
#undef K_STEP
    }
    STAMP(12);
    lds_wait_ge(lds, C_KHELP, 3u);
    {
      u32x4* pp = reinterpret_cast<u32x4*>(pl + lane * 32);
      u32x4 t[2] = {pp[0], pp[1]};
      float mx = bf_lo(t[0][0]);
#pragma unroll
      for (int j = 0; j < 2; ++j)
#pragma unroll
        for (int i = 0; i < 4; ++i) mx = fmaxf(mx, fmaxf(bf_lo(t[j][i]), bf_hi(t[j][i])));
      mx = parity_max(mx);
      const float mc = mx * C2;
      float lp = 0.f;
#pragma unroll
      for (int j = 0; j < 2; ++j) {
#pragma unroll
        for (int i = 0; i < 4; ++i) {
          const float p0 = __builtin_amdgcn_exp2f(fmaf(bf_lo(t[j][i]), C2, -mc)), p1 = __builtin_amdgcn_exp2f(fmaf(bf_hi(t[j][i]), C2, -mc));
          lp += p0 + p1; t[j][i] = pk_bf16(p0, p1);
        }
        pp[j] = t[j];
      }
      lp = parity_sum(lp);
      if (lane < 2) *reinterpret_cast<f32x2*>(lds + L_ML + w * 16 + lane * 8) = f32x2{mx, lp};
      asm volatile("s_waitcnt lgkmcnt(0)" ::: "memory");
      if (lane == 0) __hip_atomic_fetch_add(reinterpret_cast<unsigned*>(lds + C_VDONE), 0x10000u, __ATOMIC_RELAXED, __HIP_MEMORY_SCOPE_WORKGROUP);
    }
    f32x4 acc0 = {0.f, 0.f, 0.f, 0.f}, acc1 = {0.f, 0.f, 0.f, 0.f};
    {
      const int msel = c >> 1;
      const unsigned char* pr = pl + (c & 1) * 32 + g * 8;
      const unsigned vf0 = g * 2048 + c * 16;
      float* const rec = reinterpret_cast<float*>(lds + L_ACC + w * 2048) + 8 * (c & 3) + g;
      auto put = [&](bool whole) {
        const int e0 = (whole ? c >> 2 : piece(0, c >> 2)) * 32, e1 = (whole ? 4 + (c >> 2) : piece(1, c >> 2)) * 32;
#pragma unroll
        for (int k = 0; k < 4; ++k) { float* r = rec + (k & 1) * 256 + ((k >> 1) ? e1 : e0); r[0] = acc0[k]; r[4] = acc1[k]; }
      };
      auto get = [&](bool whole) {
        const int e0 = (whole ? c >> 2 : piece(0, c >> 2)) * 32, e1 = (whole ? 4 + (c >> 2) : piece(1, c >> 2)) * 32;
#pragma unroll
        for (int k = 0; k < 4; ++k) { const float* r = rec + (k & 1) * 256 + ((k >> 1) ? e1 : e0); acc0[k] = r[0]; acc1[k] = r[4]; }
      };
      unsigned toff = t0 + 8 * TILE;
      bool cur = false;
#define V_STEP(R, ISSUE_NEXT) do { const u32x2 pk_ = *reinterpret_cast<const u32x2*>(pr); pr += 64; pv_mfma(R, pk_, msel, acc0, acc1); PROG(); ISSUE_NEXT; } while (0)
      for (int it = 0; it < 15; ++it) {
        const u32x2 cw = *reinterpret_cast<const u32x2*>(lds + C_VDONE);
        const bool nxt = (unsigned)(8 * it + 8 + w) < __builtin_amdgcn_readfirstlane(cw[1]) || 8 * it + 8 >= 128 - LASTFAST;
        if ((unsigned)(8 * it + 4) < (__builtin_amdgcn_readfirstlane(cw[0]) & 0xffffu)) __builtin_amdgcn_s_setprio(1); else __builtin_amdgcn_s_setprio(0);
        const unsigned n0 = nxt ? vf0 : vv0, n1 = nxt ? vf0 + 256 : vv1;
        V_STEP(ra, issue(ra, r_v, n0, n1, toff));
        V_STEP(rb, issue(rb, r_v, n0, n1, toff + 4 * TILE));
        if (lane == 0) __hip_atomic_fetch_add(reinterpret_cast<unsigned*>(lds + C_VDONE), 2u, __ATOMIC_RELAXED, __HIP_MEMORY_SCOPE_WORKGROUP);
        if (nxt != cur) { put(cur); FENCE(); get(nxt); cur = nxt; }
        FENCE();
        toff += 8 * TILE;
      }
      V_STEP(ra, (void)0);
      V_STEP(rb, (void)0);
#undef V_STEP
      STAMP(3);
      put(cur);
    }
#pragma unroll
    for (int j = 0; j < 8; ++j) { wop0[j] = 0u; wop1[j] = 0u; }
  }
  PROG_OUT();
  STAMP(4);
  lds_barrier();
  STAMP(5);
  {
    float O = 0.f;
#pragma unroll
    for (int w = 0; w < 4; ++w)
      O = fmaf(*reinterpret_cast<const float*>(lds + L_SC + w * 8 + (tid >> 8) * 4), *reinterpret_cast<const float*>(lds + L_ACC + w * 2048 + tid * 4), O);
    __builtin_amdgcn_raw_buffer_store_b64(u32x2{__float_as_uint(O), tag}, r_ws, (tid >> 4) * 4096 + (tid & 15) * 8, WS_PO + h * (32 * 4096) + s * 128, AUX_SC1);
  }
  STAMP(6);
  if (wave < 4) {
    u32x4 po[4];
    const unsigned oo[4] = {(unsigned)lane * 16, (unsigned)lane * 16 + 1024, (unsigned)lane * 16 + 2048, (unsigned)lane * 16 + 3072};
    if (!gather<4, 0>(po, r_ws, oo, WS_PO + wg * 4096, tag)) timed_out = true;
    STAMP(7);
    float o0 = (__uint_as_float(po[0][0]) + __uint_as_float(po[1][0])) + (__uint_as_float(po[2][0]) + __uint_as_float(po[3][0]));
    float o1 = (__uint_as_float(po[0][2]) + __uint_as_float(po[1][2])) + (__uint_as_float(po[2][2]) + __uint_as_float(po[3][2]));
    o0 += dpp<XOR8>(o0); o1 += dpp<XOR8>(o1);
    o0 = x32_sum(x16_sum(o0)); o1 = x32_sum(x16_sum(o1));
    const unsigned opk = pk_bf16(o0, o1);
#pragma unroll
    for (int j = 0; j < 8; ++j) { const unsigned oj = __builtin_amdgcn_readlane(opk, j); y0 = dot2(wop0[j], oj, y0); y1 = dot2(wop1[j], oj, y1); }
    if (timed_out) y0 = __builtin_nanf("");
    const unsigned yoff = tid < D / 2 ? (unsigned)((2 * tid / CPR) * (256 * CPR) + (2 * tid % CPR)) * 8 : OOB;
    __builtin_amdgcn_raw_buffer_store_b128(u32x4{__float_as_uint(y0), tag, __float_as_uint(y1), tag}, r_ws, yoff, WS_YA + wg * (CPR * 8), AUX_SC1);
    STAMP(8);
    if (wave == 0) lds_arrive(lds, C_HOP1, lane);
  }
  if (wg >= NRED) return;
  lds_wait_ge(lds, C_HOP1, 1u);
  {
    constexpr int NLD = (256 * CPR / 2 + NTHR - 1) / NTHR;
    const float xo = tid < CPR ? bf_lo(a.x[wg * CPR + tid]) : 0.f;
    u32x4 t[NLD];
    unsigned yo[NLD];
#pragma unroll
    for (int k = 0; k < NLD; ++k) yo[k] = tid + NTHR * k < 256 * CPR / 2 ? (unsigned)(tid + NTHR * k) * 16 : OOB;
    if (!gather<NLD, 0>(t, r_ws, yo, WS_YA + wg * (256 * CPR * 8), tag)) timed_out = true;
    STAMP(9);
    float s0 = 0.f, s1 = 0.f;
#pragma unroll
    for (int k = 0; k < NLD; ++k) { s0 += __uint_as_float(t[k][0]); s1 += __uint_as_float(t[k][2]); }
    if (CPR / 2 <= 8) { s0 += dpp<XOR8>(s0); s1 += dpp<XOR8>(s1); }
    if (CPR / 2 <= 4) { s0 += dpp<ROR4>(s0); s1 += dpp<ROR4>(s1); }
    if (CPR / 2 <= 2) { s0 += dpp<XOR2>(s0); s1 += dpp<XOR2>(s1); }
    if (CPR / 2 <= 1) { s0 += dpp<XOR1>(s0); s1 += dpp<XOR1>(s1); }
    s0 = x32_sum(x16_sum(s0)); s1 = x32_sum(x16_sum(s1));
    if (timed_out) s0 = __builtin_nanf("");
    if (lane < CPR / 2) *reinterpret_cast<f32x2*>(lds + L_YA + wave * 64 + lane * 8) = f32x2{s0, s1};
    lds_barrier();
    if (tid < CPR) {
      float r = xo;
#pragma unroll
      for (int w = 0; w < 8; ++w) r += *reinterpret_cast<const float*>(lds + L_YA + w * 64 + tid * 4);
      a.out[wg * CPR + tid] = (unsigned short)(pk_bf16(r, r) & 0xffffu);
    }
    if (wg == 0 && tid == 64) __builtin_amdgcn_raw_buffer_store_b32(tag, r_ws, 0, WS_EPOCH, AUX_SC1);
    STAMP(10);
  }
}

}

extern "C" void kernel_launch(void* const* d_in, const int* in_sizes, int n_in, void* d_out, int out_size, void* d_ws, size_t ws_size,
                              hipStream_t stream) {
  (void)in_sizes; (void)n_in; (void)out_size; (void)ws_size;
  Args a;
  a.x = (const unsigned short*)d_in[0];
  a.k = (const unsigned char*)d_in[1];
  a.v = (const unsigned char*)d_in[2];
  a.wq = (const unsigned char*)d_in[3];
  a.wo = (const unsigned char*)d_in[4];
  a.wg = (const unsigned char*)d_in[6];
  a.wu = (const unsigned char*)d_in[7];
  a.wd = (const unsigned char*)d_in[8];
  a.out = (unsigned short*)d_out;
  a.ws = (unsigned char*)d_ws;
  (void)hipFuncSetAttribute((const void*)fused_decode, hipFuncAttributeMaxDynamicSharedMemorySize, LDS_BYTES);
  hipLaunchKernelGGL(fused_decode, dim3(NWG), dim3(NTHR), LDS_BYTES, stream, a);
}
```

```cpp
#include <hip/hip_runtime.h>
#include <hip/hip_bf16.h>
#include <cstdint>

namespace {

typedef unsigned u32x4 __attribute__((ext_vector_type(4)));
typedef unsigned u32x2 __attribute__((ext_vector_type(2)));
typedef float f32x4 __attribute__((ext_vector_type(4)));
typedef float f32x2 __attribute__((ext_vector_type(2)));
typedef __bf16 bf16x8 __attribute__((ext_vector_type(8)));
typedef __bf16 bf16x2 __attribute__((ext_vector_type(2)));
typedef __amdgpu_buffer_rsrc_t rsrc_t;

constexpr int D = 448, SK = 65536, DH = 256, FF = 1792, NQ = 4096;
constexpr int NWG = 256, NTHR = 512;
#define CH_W 7
#define NI 5
#define PRETOUCH 1
#define HK_PRIO 2
#define ST_PRIO 0
#define CP_V 48
#define CP_WAIT 300
#define CP_GAIN 16
#define CP_OFF 30
#define LASTFAST 0
#define WAVEBAL 1
#define Q_FIRST 300
#define Q_SLEEP 0
#define GU_AT 2600
#define CPR 16
constexpr int NRED = D / CPR;
constexpr unsigned TILE = 8192;
constexpr unsigned OOB = 0x40000000u;
constexpr float C2 = 0.0625f * 1.4426950408889634f;
constexpr unsigned SPIN_CAP = 1u << 22;

constexpr unsigned WS_EPOCH = 0;
constexpr unsigned LSTR = 4352;
constexpr unsigned WS_PO = 4096;
constexpr unsigned WS_YA = WS_PO + 8 * 32 * 32 * 16 * 8;
constexpr unsigned WS_GU = WS_YA + 256 * D * 8;
constexpr unsigned WS_QG = WS_GU + 224 * 4 * 128;
constexpr unsigned WS_ML = WS_QG + 128 * LSTR;
constexpr unsigned WS_END = WS_ML + 256 * LSTR;

constexpr int L_RED = 0;
constexpr int L_CTL = 1024;
constexpr int C_PART = L_CTL, C_QRDY = L_CTL + 4, C_TAG = L_CTL + 12, C_LANDED = L_CTL + 16, C_KHELP = L_CTL + 20, C_VDONE = L_CTL + 24, C_FAST = L_CTL + 28,
              C_HRDY = L_CTL + 32, C_HOP1 = L_CTL + 36;
constexpr int L_ML = 1088;
constexpr int L_SC = 1152;
constexpr int L_H = 1184;
constexpr int L_YA = 1216;
constexpr int L_Q = 1792;
constexpr int L_XN = 2816;
constexpr int L_ACC = 4864;
constexpr int L_P = L_ACC + 4 * 2048;
constexpr int LDS_BYTES = 84 * 1024;

constexpr int AUX_NT = 2, AUX_SC1 = 16;
#define AUX_K 2
#define AUX_V 2
#define SB() __builtin_amdgcn_sched_barrier(0)
#define FENCE() asm volatile("" ::: "memory")

__device__ __forceinline__ float bf_lo(unsigned u) { return __builtin_bit_cast(float, u << 16); }
__device__ __forceinline__ float bf_hi(unsigned u) { return __builtin_bit_cast(float, u & 0xffff0000u); }
__device__ __forceinline__ unsigned pk_bf16(float lo, float hi) {
  bf16x2 v = {(__bf16)lo, (__bf16)hi};
  return __builtin_bit_cast(unsigned, v);
}
__device__ __forceinline__ float rnd_bf16(float x) { return (float)(__bf16)x; }
__device__ __forceinline__ float dot2(unsigned a, unsigned b, float acc) {
  return __builtin_amdgcn_fdot2_f32_bf16(__builtin_bit_cast(bf16x2, a), __builtin_bit_cast(bf16x2, b), acc, false);
}
template <int CTRL> __device__ __forceinline__ float dpp(float x) {
  return __builtin_bit_cast(float, __builtin_amdgcn_mov_dpp(__builtin_bit_cast(int, x), CTRL, 0xf, 0xf, true));
}
constexpr int XOR1 = 0xB1, XOR2 = 0x4E, XOR7 = 0x141, XOR15 = 0x140, XOR8 = 0x128, ROR4 = 0x124;
__device__ __forceinline__ float x16_max(float x) {
  auto s = __builtin_amdgcn_permlane16_swap(__float_as_uint(x), __float_as_uint(x), false, false);
  return fmaxf(__uint_as_float(s[0]), __uint_as_float(s[1]));
}
__device__ __forceinline__ float x32_max(float x) {
  auto s = __builtin_amdgcn_permlane32_swap(__float_as_uint(x), __float_as_uint(x), false, false);
  return fmaxf(__uint_as_float(s[0]), __uint_as_float(s[1]));
}
__device__ __forceinline__ float x16_sum(float x) {
  auto s = __builtin_amdgcn_permlane16_swap(__float_as_uint(x), __float_as_uint(x), false, false);
  return __uint_as_float(s[0]) + __uint_as_float(s[1]);
}
__device__ __forceinline__ float x32_sum(float x) {
  auto s = __builtin_amdgcn_permlane32_swap(__float_as_uint(x), __float_as_uint(x), false, false);
  return __uint_as_float(s[0]) + __uint_as_float(s[1]);
}
__device__ __forceinline__ float wave_sum(float x) {
  x += dpp<XOR1>(x); x += dpp<XOR2>(x); x += dpp<XOR7>(x); x += dpp<XOR15>(x);
  return x32_sum(x16_sum(x));
}
__device__ __forceinline__ float parity_sum(float x) {
  x += dpp<XOR2>(x); x += dpp<ROR4>(x); x += dpp<XOR8>(x);
  return x32_sum(x16_sum(x));
}
__device__ __forceinline__ float parity_max(float x) {
  x = fmaxf(x, dpp<XOR2>(x)); x = fmaxf(x, dpp<ROR4>(x)); x = fmaxf(x, dpp<XOR8>(x));
  return x32_max(x16_max(x));
}
__device__ __forceinline__ void lds_barrier() {
  asm volatile("s_waitcnt lgkmcnt(0)" ::: "memory");
  __builtin_amdgcn_s_barrier();
  FENCE();
}
__device__ __forceinline__ rsrc_t mk_rsrc(const void* p, unsigned bytes) {
  return __builtin_amdgcn_make_buffer_rsrc(const_cast<void*>(p), 0, bytes, 0x00020000);
}
__device__ __forceinline__ unsigned lds_ld(unsigned char* lds, int off) {
  return __builtin_amdgcn_readfirstlane(__hip_atomic_load(reinterpret_cast<unsigned*>(lds + off), __ATOMIC_RELAXED, __HIP_MEMORY_SCOPE_WORKGROUP));
}
template <int SLEEP = 1>
__device__ __forceinline__ void lds_wait_ge(unsigned char* lds, int off, unsigned want) {
#pragma nounroll
  for (unsigned spin = 0; spin < SPIN_CAP * 8u; ++spin) {
    if (lds_ld(lds, off) >= want) break;
    __builtin_amdgcn_s_sleep(SLEEP);
  }
  FENCE();
}
__device__ __forceinline__ void lds_arrive(unsigned char* lds, int off, int lane) {
  asm volatile("s_waitcnt lgkmcnt(0)" ::: "memory");
  if (lane == 0) __hip_atomic_fetch_add(reinterpret_cast<unsigned*>(lds + off), 1u, __ATOMIC_RELAXED, __HIP_MEMORY_SCOPE_WORKGROUP);
}
__device__ __forceinline__ unsigned now() { return (unsigned)__builtin_amdgcn_s_memrealtime(); }
template <int SLEEP>
__device__ __forceinline__ void sleep_until(unsigned t0, unsigned ticks) {
#pragma nounroll
  for (unsigned spin = 0; spin < SPIN_CAP && now() - t0 < ticks; ++spin) __builtin_amdgcn_s_sleep(SLEEP);
}
__device__ __forceinline__ void chain_wait(unsigned char* lds, int idx) {
#pragma nounroll
  for (unsigned spin = 0; spin < SPIN_CAP * 8u; ++spin) {
    if ((int)lds_ld(lds, C_LANDED) >= idx + 1 - CH_W || lds_ld(lds, C_QRDY) != 0u) break;
    __builtin_amdgcn_s_sleep(1);
  }
  FENCE();
}
template <int N, int SLEEP>
__device__ __forceinline__ bool gather(u32x4 (&v)[N], rsrc_t rs, const unsigned (&voff)[N], unsigned soff, unsigned tag) {
  bool have[N];
#pragma unroll
  for (int k = 0; k < N; ++k) { have[k] = voff[k] == OOB; v[k] = u32x4{0u, 0u, 0u, 0u}; }
#pragma nounroll
  for (unsigned spin = 0; spin < SPIN_CAP; ++spin) {
    FENCE();
    u32x4 t[N];
#pragma unroll
    for (int k = 0; k < N; ++k) t[k] = __builtin_amdgcn_raw_buffer_load_b128(rs, have[k] ? OOB : voff[k], soff, AUX_SC1);
    bool all = true;
#pragma unroll
    for (int k = 0; k < N; ++k) {
      const bool now = !have[k] && t[k][1] == tag && t[k][3] == tag;
      v[k][0] = now ? t[k][0] : v[k][0]; v[k][2] = now ? t[k][2] : v[k][2];
      have[k] = have[k] || now; all = all && have[k];
    }
    if (__builtin_amdgcn_ballot_w64(!all) == 0) return true;
    if (SLEEP > 0) __builtin_amdgcn_s_sleep(SLEEP);
  }
  return false;
}
#define STAMP(slot) do {} while (0)
#define PROG() do {} while (0)
#define PROG_OUT() do {} while (0)
#define PROGL() do {} while (0)

struct Args {
  const unsigned short* x;
  const unsigned char* k;
  const unsigned char* v;
  const unsigned char* wq;
  const unsigned char* wo;
  const unsigned char* wg;
  const unsigned char* wu;
  const unsigned char* wd;
  unsigned short* out;
  unsigned char* ws;
};

__host__ __device__ constexpr int piece(int e, int n) { return n < 2 ? 2 * e + n : 2 * n + e; }
template <int AUX>
__device__ __forceinline__ void issue(u32x4 (&r)[8], rsrc_t rs, unsigned v0, unsigned v1, unsigned soff) {
#pragma unroll
  for (int i = 0; i < 8; ++i) { r[i] = __builtin_amdgcn_raw_buffer_load_b128(rs, ((i & 1) ? v1 : v0) + (i >> 1) * 512, soff, AUX); SB(); }
}
__device__ __forceinline__ f32x4 mfma(u32x4 x, u32x4 y, f32x4 acc) {
  return __builtin_amdgcn_mfma_f32_16x16x32_bf16(__builtin_bit_cast(bf16x8, x), __builtin_bit_cast(bf16x8, y), acc, 0, 0, 0);
}
__device__ __forceinline__ void qk_mfma(const u32x4 (&r)[8], const u32x4 (&qf)[2], f32x4 (&d)[4]) {
#pragma unroll
  for (int j = 0; j < 4; ++j) d[j] = mfma(qf[1], r[2 * j + 1], mfma(qf[0], r[2 * j], f32x4{0.f, 0.f, 0.f, 0.f}));
  SB();
}
__device__ __forceinline__ unsigned short qk_score(const f32x4 (&d)[4], bool b0, bool b1) {
  float v[4];
#pragma unroll
  for (int j = 0; j < 4; ++j) {
    float e0 = d[j][0], e1 = d[j][1], e2 = d[j][2], e3 = d[j][3];
    asm("" : "+v"(e0), "+v"(e2));
    const float lo = b0 ? e1 : e0, hi = b0 ? e3 : e2;
    v[j] = b1 ? hi : lo;
  }
  const float u0 = (b0 ? v[1] : v[0]) + dpp<XOR1>(b0 ? v[0] : v[1]);
  const float u1 = (b0 ? v[3] : v[2]) + dpp<XOR1>(b0 ? v[2] : v[3]);
  const float w = (b1 ? u1 : u0) + dpp<XOR2>(b1 ? u0 : u1);
  return (unsigned short)(pk_bf16(w, w) & 0xffffu);
}
__device__ __forceinline__ void pv_mfma(const u32x4 (&vr)[8], u32x2 pk, int msel, f32x4& acc0, f32x4& acc1) {
#pragma unroll
  for (int q = 0; q < 4; ++q) {
    const u32x4 pa = {msel == 2 * q ? pk[0] : 0u, msel == 2 * q ? pk[1] : 0u, msel == 2 * q + 1 ? pk[0] : 0u, msel == 2 * q + 1 ? pk[1] : 0u};
    const unsigned sel = (q & 1) ? 0x07060302u : 0x05040100u;
    const int d0 = q >> 1, d1 = 2 + (q >> 1);
    const u32x4 b0 = {__builtin_amdgcn_perm(vr[2][d0], vr[0][d0], sel), __builtin_amdgcn_perm(vr[6][d0], vr[4][d0], sel),
                      __builtin_amdgcn_perm(vr[3][d0], vr[1][d0], sel), __builtin_amdgcn_perm(vr[7][d0], vr[5][d0], sel)};
    const u32x4 b1 = {__builtin_amdgcn_perm(vr[2][d1], vr[0][d1], sel), __builtin_amdgcn_perm(vr[6][d1], vr[4][d1], sel),
                      __builtin_amdgcn_perm(vr[3][d1], vr[1][d1], sel), __builtin_amdgcn_perm(vr[7][d1], vr[5][d1], sel)};
    acc0 = mfma(pa, b0, acc0);
    acc1 = mfma(pa, b1, acc1);
  }
  SB();
}
__device__ __forceinline__ void fma8(float (&s)[8], float xv, u32x4 w) {
#pragma unroll
  for (int i = 0; i < 4; ++i) { s[2 * i] = fmaf(xv, bf_lo(w[i]), s[2 * i]); s[2 * i + 1] = fmaf(xv, bf_hi(w[i]), s[2 * i + 1]); }
}
template <bool WHOLE>
__device__ __forceinline__ void load_qf(u32x4 (&qf)[2], const unsigned char* lds, int c, int g) {
#pragma unroll
  for (int e = 0; e < 2; ++e) qf[e] = *reinterpret_cast<const u32x4*>(lds + L_Q + ((c >> 2) & 1) * 512 + ((WHOLE ? 4 * e + g : piece(e, g)) * 4 + (c & 3)) * 16);
}

__global__ void __launch_bounds__(NTHR, 2) fused_decode(Args a) {
  extern __shared__ __attribute__((aligned(16))) unsigned char lds[];
  const int tid = threadIdx.x, lane = tid & 63, wave = __builtin_amdgcn_readfirstlane(tid >> 6);
  const int wg = blockIdx.x, h = wg >> 5, s = wg & 31;
  const int c = lane & 15, g = lane >> 4;
  STAMP(0);
  const rsrc_t r_ws = mk_rsrc(a.ws, WS_END);
  const rsrc_t r_k = mk_rsrc(a.k, 8u * SK * DH * 2);
  const unsigned wg_off = (unsigned)wg * (128 * TILE);
  const unsigned vw0 = (c >> 2) * 2048 + g * 64 + (c & 3) * 16, vw1 = vw0 + 256;
  const bool b0 = (c & 1) != 0, b1 = (c & 2) != 0;
  const int sc_lane = (g & 1) * 32 + c * 2;
  bool timed_out = false;
  unsigned tag;
  unsigned wop0[8], wop1[8];
  float y0 = 0.f, y1 = 0.f;

  if (wave < 4) {
    __builtin_amdgcn_s_setprio(3);
    const unsigned t_start = now();
    const bool qwg = (s & 8) == 0;
    const rsrc_t r_x = mk_rsrc(a.x, D * 2);
    const rsrc_t r_wq = mk_rsrc(a.wq, qwg ? D * NQ * 2 : 0);
    const int qsl = (s & 7) * 2 + (s >> 4);
    const u32x4 xv = __builtin_amdgcn_raw_buffer_load_b128(r_x, lane * 16, 0, 0);
    const unsigned epoch = __builtin_amdgcn_raw_buffer_load_b32(r_ws, 0, WS_EPOCH, AUX_SC1);
    const int qrow = (lane >> 2) + 16 * wave;
    const unsigned wq_off = (unsigned)qrow * (NQ * 2) + (unsigned)(h * 512 + qsl * 32) * 2 + (lane & 3) * 16;
    u32x4 wv[7];
#pragma unroll
    for (int i = 0; i < 7; ++i) wv[i] = __builtin_amdgcn_raw_buffer_load_b128(r_wq, wq_off, i * 64 * NQ * 2, AUX_NT);
    SB();
    if (tid < 16) *reinterpret_cast<unsigned*>(lds + L_CTL + tid * 4) = 0u;
    lds_barrier();

    {
      float xs[8];
#pragma unroll
      for (int i = 0; i < 4; ++i) { xs[2 * i] = bf_lo(xv[i]); xs[2 * i + 1] = bf_hi(xv[i]); }
      float sm = 0.f;
#pragma unroll
      for (int i = 0; i < 8; ++i) sm += xs[i];
      const float mean = wave_sum(sm) * (1.0f / D);
      float sq = 0.f;
#pragma unroll
      for (int i = 0; i < 8; ++i) { const float d = xs[i] - mean; sq = fmaf(d, d, sq); }
      sq = lane < D / 8 ? sq : 0.f;
      const float var = wave_sum(sq) * (1.0f / D);
      const float mean_b = rnd_bf16(mean);
      const float den_b = rnd_bf16(sqrtf(rnd_bf16(rnd_bf16(var) + 1e-5f)));
#pragma unroll
      for (int i = 0; i < 8; ++i) xs[i] = rnd_bf16(rnd_bf16(xs[i] - mean_b) / den_b);
      f32x4* xp = reinterpret_cast<f32x4*>(lds + L_XN + lane * 32);
      xp[0] = f32x4{xs[0], xs[1], xs[2], xs[3]}; xp[1] = f32x4{xs[4], xs[5], xs[6], xs[7]};
      FENCE();
    }
    tag = __builtin_amdgcn_readfirstlane(epoch) + 1u;
    if (qwg) {
      float sacc[8] = {0.f, 0.f, 0.f, 0.f, 0.f, 0.f, 0.f, 0.f};
#pragma unroll
      for (int i = 0; i < 7; ++i) fma8(sacc, *reinterpret_cast<const float*>(lds + L_XN + (qrow + 64 * i) * 4), wv[i]);
#pragma unroll
      for (int i = 0; i < 8; ++i) { float t = sacc[i]; t += dpp<ROR4>(t); t += dpp<XOR8>(t); sacc[i] = x32_sum(x16_sum(t)); }
      if (lane < 4) {
        f32x4* p = reinterpret_cast<f32x4*>(lds + L_RED + wave * 256 + lane * 32);
        p[0] = f32x4{sacc[0], sacc[1], sacc[2], sacc[3]}; p[1] = f32x4{sacc[4], sacc[5], sacc[6], sacc[7]};
      }
      lds_arrive(lds, C_PART, lane);
      if (wave == 0) {
        lds_wait_ge(lds, C_PART, 4u);
        if (lane < 16) {
          float q0 = 0.f, q1 = 0.f;
#pragma unroll
          for (int w = 0; w < 4; ++w) { const f32x2 t = *reinterpret_cast<const f32x2*>(lds + L_RED + w * 256 + lane * 8); q0 += t[0]; q1 += t[1]; }
          __builtin_amdgcn_raw_buffer_store_b64(u32x2{pk_bf16(q0, q1), tag}, r_ws, lane * 8, WS_QG + (h * 16 + qsl) * LSTR, AUX_SC1);
        }
        STAMP(1);
      }
    }
    if (wave == 0) {
      u32x4 qg[2];
      const unsigned qo[2] = {(lane >> 2) * LSTR + (lane & 3) * 32, (lane >> 2) * LSTR + (lane & 3) * 32 + 16};
      sleep_until<1>(t_start, Q_FIRST);
      timed_out = !gather<2, Q_SLEEP>(qg, r_ws, qo, WS_QG + h * 16 * LSTR, tag);
      *reinterpret_cast<u32x4*>(lds + L_Q + lane * 16) = u32x4{qg[0][0], qg[0][2], qg[1][0], qg[1][2]};
      if (lane == 0) *reinterpret_cast<unsigned*>(lds + C_TAG) = tag;
      lds_arrive(lds, C_QRDY, lane);
    }
    if (wave != 0) {
      __builtin_amdgcn_s_setprio(HK_PRIO);
      const int slot = NI + wave - 1;
      u32x4 kt[4][8];
#pragma unroll
      for (int i = 0; i < 4; ++i) { chain_wait(lds, 4 * slot + i); issue<AUX_K>(kt[i], r_k, vw0, vw1, wg_off + (4 * slot + i) * TILE); }
      asm volatile("s_waitcnt vmcnt(24)" ::: "memory"); lds_arrive(lds, C_LANDED, lane); PROGL();
      asm volatile("s_waitcnt vmcnt(16)" ::: "memory"); lds_arrive(lds, C_LANDED, lane); PROGL();
      asm volatile("s_waitcnt vmcnt(8)" ::: "memory"); lds_arrive(lds, C_LANDED, lane); PROGL();
      asm volatile("s_waitcnt vmcnt(0)" ::: "memory"); lds_arrive(lds, C_LANDED, lane); PROGL();
      lds_wait_ge(lds, C_QRDY, 1u);
      u32x4 qf[2];
      load_qf<true>(qf, lds, c, g);
#pragma unroll
      for (int i = 0; i < 4; ++i) {
        f32x4 d[4];
        qk_mfma(kt[i], qf, d);
        *reinterpret_cast<unsigned short*>(lds + L_P + i * 2048 + slot * 64 + sc_lane) = qk_score(d, b0, b1);
        PROG();
      }
      lds_arrive(lds, C_KHELP, lane);
      __builtin_amdgcn_s_setprio(3);
    }
    STAMP(3);
    const int fb = h * 16 + (s & 7) + ((s >> 4) << 3);
    if (!qwg && fb < 112) {
      const rsrc_t r_gu = mk_rsrc((wave & 1) ? a.wu : a.wg, D * FF * 2);
      const int frow = (fb / 28) * 112 + (wave >> 1) * 56 + (lane >> 3);
      const unsigned goff = (unsigned)frow * (FF * 2) + (fb % 28) * 128 + (lane & 7) * 16;
#pragma unroll
      for (int i = 0; i < 7; ++i) wv[i] = __builtin_amdgcn_raw_buffer_load_b128(r_gu, goff, i * 8 * FF * 2, AUX_NT);
      float sg[8] = {0.f, 0.f, 0.f, 0.f, 0.f, 0.f, 0.f, 0.f};
#pragma unroll
      for (int i = 0; i < 7; ++i) fma8(sg, *reinterpret_cast<const float*>(lds + L_XN + (frow + 8 * i) * 4), wv[i]);
#pragma unroll
      for (int i = 0; i < 8; ++i) { float t = sg[i]; t += dpp<XOR8>(t); sg[i] = x32_sum(x16_sum(t)); }
      if (lane < 8) {
        f32x4* p = reinterpret_cast<f32x4*>(lds + L_RED + wave * 256 + lane * 32);
        p[0] = f32x4{sg[0], sg[1], sg[2], sg[3]}; p[1] = f32x4{sg[4], sg[5], sg[6], sg[7]};
      }
      lds_arrive(lds, C_PART, lane);
      if (wave == 1) {
        lds_wait_ge(lds, C_PART, 4u);
        const int o = ((lane >> 2) & 1) * 256 + (lane >> 3) * 32 + (lane & 3) * 8;
        const f32x2 t0 = *reinterpret_cast<const f32x2*>(lds + L_RED + o), t1 = *reinterpret_cast<const f32x2*>(lds + L_RED + 512 + o);
        __builtin_amdgcn_raw_buffer_store_b128(u32x4{__float_as_uint(t0[0] + t1[0]), tag, __float_as_uint(t0[1] + t1[1]), tag}, r_ws,
                                               (lane >> 3) * 512 + (lane & 7) * 16, WS_GU + (fb % 28) * 4096 + (fb / 28) * 128, AUX_SC1);
      }
    }
    {
      const rsrc_t r_wo = mk_rsrc(a.wo, NQ * D * 2);
      const unsigned coff = tid < D / 2 ? tid * 4 : OOB;
      const bool ffn = wg < FF / 8;
      const rsrc_t r_d = mk_rsrc(a.wd, ffn ? FF * D * 2 : 0);
      unsigned draw[8], wraw[16];
#pragma unroll
      for (int j = 0; j < 8; ++j) draw[j] = __builtin_amdgcn_raw_buffer_load_b32(r_d, coff, (wg * 8 + j) * (D * 2), AUX_NT);
#pragma unroll
      for (int j = 0; j < 16; ++j) wraw[j] = __builtin_amdgcn_raw_buffer_load_b32(r_wo, coff, (wg * 16 + j) * (D * 2), AUX_NT);
#pragma unroll
      for (int j = 0; j < 8; ++j) { wop0[j] = __builtin_amdgcn_perm(wraw[2 * j + 1], wraw[2 * j], 0x05040100u); wop1[j] = __builtin_amdgcn_perm(wraw[2 * j + 1], wraw[2 * j], 0x07060302u); }
      if (wave == 3) {
        sleep_until<32>(t_start, GU_AT);
        u32x4 gu[1];
        const unsigned go[1] = {(ffn && lane < 32) ? (unsigned)lane * 16 : OOB};
        if (!gather<1, 64>(gu, r_ws, go, WS_GU + wg * 512, tag)) timed_out = true;
        float g0 = __uint_as_float(gu[0][0]), g1 = __uint_as_float(gu[0][2]);
        g0 += dpp<XOR8>(g0); g1 += dpp<XOR8>(g1);
        g0 = rnd_bf16(x16_sum(g0)); g1 = rnd_bf16(x16_sum(g1));
        const float u0 = dpp<ROR4>(g0), u1 = dpp<ROR4>(g1);
        const float h0 = rnd_bf16((g0 > 0.f ? g0 : g0 * 0.f) * u0), h1 = rnd_bf16((g1 > 0.f ? g1 : g1 * 0.f) * u1);
        if (lane < 4) *reinterpret_cast<f32x2*>(lds + L_H + lane * 8) = f32x2{h0, h1};
        lds_arrive(lds, C_HRDY, lane);
      }
      lds_wait_ge<32>(lds, C_HRDY, 1u);
#pragma unroll
      for (int j = 0; j < 8; ++j) {
        const float hv = *reinterpret_cast<const float*>(lds + L_H + j * 4);
        y0 = fmaf(hv, bf_lo(draw[j]), y0); y1 = fmaf(hv, bf_hi(draw[j]), y1);
      }
      asm volatile("" : "+v"(y0), "+v"(y1), "+v"(wop0[0]), "+v"(wop0[1]), "+v"(wop0[2]), "+v"(wop0[3]), "+v"(wop0[4]), "+v"(wop0[5]), "+v"(wop0[6]), "+v"(wop0[7]),
                        "+v"(wop1[0]), "+v"(wop1[1]), "+v"(wop1[2]), "+v"(wop1[3]), "+v"(wop1[4]), "+v"(wop1[5]), "+v"(wop1[6]), "+v"(wop1[7]));
    }
    STAMP(11);
    if (wave == 2) {
      const rsrc_t r_v = mk_rsrc(a.v, 8u * SK * DH * 2), r_o = mk_rsrc(a.out, D * 2);
      const unsigned tv = __builtin_amdgcn_raw_buffer_load_b32(r_v, lane < 16 ? lane * 65536 : OOB, wg_off, AUX_NT);
      const unsigned to = __builtin_amdgcn_raw_buffer_load_b32(r_o, lane < 1 ? 0 : OOB, 0, AUX_NT);
      asm volatile("" :: "v"(tv), "v"(to));
    }
    if (wave == 1) {
      lds_wait_ge<8>(lds, C_VDONE, 0x10000u * 4u + CP_V);
      const unsigned t_cp = now();
      const int hd = lane & 1;
      float mw[4], lw[4];
#pragma unroll
      for (int w = 0; w < 4; ++w) { const f32x2 t = *reinterpret_cast<const f32x2*>(lds + L_ML + w * 16 + hd * 8); mw[w] = t[0]; lw[w] = t[1]; }
      const float M = fmaxf(fmaxf(mw[0], mw[1]), fmaxf(mw[2], mw[3]));
      float L = 0.f;
#pragma unroll
      for (int w = 0; w < 4; ++w) L = fmaf(__builtin_amdgcn_exp2f((mw[w] - M) * C2), lw[w], L);
      __builtin_amdgcn_raw_buffer_store_b128(u32x4{(lane & 2) ? t_cp : __float_as_uint(M), tag, (lane & 2) ? t_cp : __float_as_uint(L), tag}, r_ws,
                                             lane < 8 ? lane * 16 : OOB, WS_ML + wg * LSTR, AUX_SC1);
      sleep_until<16>(t_cp, CP_WAIT);
      u32x4 ml[2];
      const unsigned mo[2] = {(lane >> 2) * LSTR + (lane & 3) * 16, (16 + (lane >> 2)) * LSTR + (lane & 3) * 16};
      if (!gather<2, 32>(ml, r_ws, mo, WS_ML + h * 32 * LSTR, tag)) timed_out = true;
      const float Ma = __uint_as_float(ml[0][0]), Mb = __uint_as_float(ml[1][0]);
      float Mg = fmaxf(Ma, Mb);
      Mg = fmaxf(Mg, dpp<ROR4>(Mg)); Mg = fmaxf(Mg, dpp<XOR8>(Mg)); Mg = x32_max(x16_max(Mg));
      float Lg = __builtin_amdgcn_exp2f((Ma - Mg) * C2) * __uint_as_float(ml[0][2]) + __builtin_amdgcn_exp2f((Mb - Mg) * C2) * __uint_as_float(ml[1][2]);
      Lg = (lane & 2) ? (float)(int)(t_cp - ml[0][0]) + (float)(int)(t_cp - ml[1][0]) : Lg;
      Lg += dpp<ROR4>(Lg); Lg += dpp<XOR8>(Lg); Lg = x32_sum(x16_sum(Lg));
      if (lane < 2) {
#pragma unroll
        for (int w = 0; w < 4; ++w) *reinterpret_cast<float*>(lds + L_SC + w * 8 + lane * 4) = timed_out ? __builtin_nanf("") : __builtin_amdgcn_exp2f((mw[w] - Mg) * C2) / Lg;
      }
      if (lane == 2) {
        const float nf = fminf((Lg * (1.0f / 32) + (float)CP_OFF) * (0.01f * CP_GAIN), 128.f);
        if (nf >= 1.f) *reinterpret_cast<unsigned*>(lds + C_FAST) = (*reinterpret_cast<const unsigned*>(lds + C_VDONE) & 0xffffu) + 8u + (unsigned)nf;
      }
      STAMP(14);
    }
  } else {
    const int w = wave - 4;
    const rsrc_t r_v = mk_rsrc(a.v, 8u * SK * DH * 2);
    const unsigned vv0 = g * 2048 + piece(0, c >> 2) * 64 + (c & 3) * 16, vv1 = g * 2048 + piece(1, c >> 2) * 64 + (c & 3) * 16;
    const unsigned vk0 = (c >> 2) * 2048 + piece(0, g) * 64 + (c & 3) * 16, vk1 = (c >> 2) * 2048 + piece(1, g) * 64 + (c & 3) * 16;
    const unsigned t0 = wg_off + w * TILE;
    u32x4 ra[8], rb[8];
    __builtin_amdgcn_s_setprio(ST_PRIO);
    u32x4 rx[NI - 2][8];
    if (s & 8) issue<AUX_K>(ra, r_k, vw0, vw1, t0);
    lds_barrier();
    if (!(s & 8)) issue<AUX_K>(ra, r_k, vw0, vw1, t0);
#define CHAIN_STEP(R, SLOT) do { \
      constexpr bool first_ = CH_W < 5;     \
      if (first_ && lds_ld(lds, C_QRDY) == 0u) { asm volatile("s_waitcnt vmcnt(0)" ::: "memory"); lds_arrive(lds, C_LANDED, lane); PROGL(); } \
      chain_wait(lds, 4 * (SLOT) + w); \
      issue<AUX_K>(R, r_k, vw0, vw1, t0 + 4 * (SLOT) * TILE); \
      if (!first_ && lds_ld(lds, C_QRDY) == 0u) { asm volatile("s_waitcnt vmcnt(8)" ::: "memory"); lds_arrive(lds, C_LANDED, lane); PROGL(); } } while (0)
    CHAIN_STEP(rb, 1);
#pragma unroll
    for (int i = 2; i < NI; ++i) CHAIN_STEP(rx[i - 2], i);
#undef CHAIN_STEP
    if (lds_ld(lds, C_QRDY) == 0u) { asm volatile("s_waitcnt vmcnt(0)" ::: "memory"); lds_arrive(lds, C_LANDED, lane); PROGL(); }
    lds_wait_ge(lds, C_QRDY, 1u);
    tag = lds_ld(lds, C_TAG);
    STAMP(2);
    u32x4 qf[2];
    load_qf<true>(qf, lds, c, g);
    unsigned char* const pl = lds + L_P + w * 2048;
    {
      f32x4 d[4];
      unsigned short* sw = reinterpret_cast<unsigned short*>(pl + sc_lane);
      unsigned toff = t0 + 4 * (NI + 3) * TILE;
#define K_STEP(R, ISSUE_NEXT, SLOT) do { qk_mfma(R, qf, d); PROG(); ISSUE_NEXT; sw[(SLOT) * 32] = qk_score(d, b0, b1); } while (0)
      K_STEP(ra, issue<AUX_K>(ra, r_k, vk0, vk1, toff), 0);
      K_STEP(rb, issue<AUX_K>(rb, r_k, vk0, vk1, toff + 4 * TILE), 1);
#pragma unroll
      for (int i = 2; i < NI; ++i) K_STEP(rx[i - 2], (void)0, i);
      load_qf<false>(qf, lds, c, g);
      sw += (NI + 3) * 32;
      for (int it = 1; it < (29 - NI) / 2; ++it) {
        toff += 8 * TILE;
        K_STEP(ra, issue<AUX_K>(ra, r_k, vk0, vk1, toff), 0);
        K_STEP(rb, issue<AUX_K>(rb, r_k, vk0, vk1, toff + 4 * TILE), 1);
        sw += 2 * 32;
      }
      K_STEP(ra, issue<AUX_V>(ra, r_v, vv0, vv1, t0), 0);
      K_STEP(rb, issue<AUX_V>(rb, r_v, vv0, vv1, t0 + 4 * TILE), 1);
#undef K_STEP
    }
    STAMP(12);
    lds_wait_ge(lds, C_KHELP, 3u);
    {
      u32x4* pp = reinterpret_cast<u32x4*>(pl + lane * 32);
      u32x4 t[2] = {pp[0], pp[1]};
      float mx = bf_lo(t[0][0]);
#pragma unroll
      for (int j = 0; j < 2; ++j)
#pragma unroll
        for (int i = 0; i < 4; ++i) mx = fmaxf(mx, fmaxf(bf_lo(t[j][i]), bf_hi(t[j][i])));
      mx = parity_max(mx);
      const float mc = mx * C2;
      float lp = 0.f;
#pragma unroll
      for (int j = 0; j < 2; ++j) {
#pragma unroll
        for (int i = 0; i < 4; ++i) {
          const float p0 = __builtin_amdgcn_exp2f(fmaf(bf_lo(t[j][i]), C2, -mc)), p1 = __builtin_amdgcn_exp2f(fmaf(bf_hi(t[j][i]), C2, -mc));
          lp += p0 + p1; t[j][i] = pk_bf16(p0, p1);
        }
        pp[j] = t[j];
      }
      lp = parity_sum(lp);
      if (lane < 2) *reinterpret_cast<f32x2*>(lds + L_ML + w * 16 + lane * 8) = f32x2{mx, lp};
      asm volatile("s_waitcnt lgkmcnt(0)" ::: "memory");
      if (lane == 0) __hip_atomic_fetch_add(reinterpret_cast<unsigned*>(lds + C_VDONE), 0x10000u, __ATOMIC_RELAXED, __HIP_MEMORY_SCOPE_WORKGROUP);
    }
    f32x4 acc0 = {0.f, 0.f, 0.f, 0.f}, acc1 = {0.f, 0.f, 0.f, 0.f};
    {
      const int msel = c >> 1;
      const unsigned char* pr = pl + (c & 1) * 32 + g * 8;
      const unsigned vf0 = g * 2048 + c * 16;
      float* const rec = reinterpret_cast<float*>(lds + L_ACC + w * 2048) + 8 * (c & 3) + g;
      auto put = [&](bool whole) {
        const int e0 = (whole ? c >> 2 : piece(0, c >> 2)) * 32, e1 = (whole ? 4 + (c >> 2) : piece(1, c >> 2)) * 32;
#pragma unroll
        for (int k = 0; k < 4; ++k) { float* r = rec + (k & 1) * 256 + ((k >> 1) ? e1 : e0); r[0] = acc0[k]; r[4] = acc1[k]; }
      };
      auto get = [&](bool whole) {
        const int e0 = (whole ? c >> 2 : piece(0, c >> 2)) * 32, e1 = (whole ? 4 + (c >> 2) : piece(1, c >> 2)) * 32;
#pragma unroll
        for (int k = 0; k < 4; ++k) { const float* r = rec + (k & 1) * 256 + ((k >> 1) ? e1 : e0); acc0[k] = r[0]; acc1[k] = r[4]; }
      };
      unsigned toff = t0 + 8 * TILE;
      bool cur = false;
#define V_STEP(R, ISSUE_NEXT) do { const u32x2 pk_ = *reinterpret_cast<const u32x2*>(pr); pr += 64; pv_mfma(R, pk_, msel, acc0, acc1); PROG(); ISSUE_NEXT; } while (0)
      for (int it = 0; it < 15; ++it) {
        const u32x2 cw = *reinterpret_cast<const u32x2*>(lds + C_VDONE);
        const bool nxt = (unsigned)(8 * it + 8 + w) < __builtin_amdgcn_readfirstlane(cw[1]) || 8 * it + 8 >= 128 - LASTFAST;
        if ((unsigned)(8 * it + 4) < (__builtin_amdgcn_readfirstlane(cw[0]) & 0xffffu)) __builtin_amdgcn_s_setprio(1); else __builtin_amdgcn_s_setprio(0);
        const unsigned n0 = nxt ? vf0 : vv0, n1 = nxt ? vf0 + 256 : vv1;
        V_STEP(ra, issue<AUX_V>(ra, r_v, n0, n1, toff));
        V_STEP(rb, issue<AUX_V>(rb, r_v, n0, n1, toff + 4 * TILE));
        if (lane == 0) __hip_atomic_fetch_add(reinterpret_cast<unsigned*>(lds + C_VDONE), 2u, __ATOMIC_RELAXED, __HIP_MEMORY_SCOPE_WORKGROUP);
        if (nxt != cur) { put(cur); FENCE(); get(nxt); cur = nxt; }
        FENCE();
        toff += 8 * TILE;
      }
      V_STEP(ra, (void)0);
      V_STEP(rb, (void)0);
#undef V_STEP
      STAMP(3);
      put(cur);
    }
#pragma unroll
    for (int j = 0; j < 8; ++j) { wop0[j] = 0u; wop1[j] = 0u; }
  }
  PROG_OUT();
  STAMP(4);
  lds_barrier();
  STAMP(5);
  {
    float O = 0.f;
#pragma unroll
    for (int w = 0; w < 4; ++w)
      O = fmaf(*reinterpret_cast<const float*>(lds + L_SC + w * 8 + (tid >> 8) * 4), *reinterpret_cast<const float*>(lds + L_ACC + w * 2048 + tid * 4), O);
    __builtin_amdgcn_raw_buffer_store_b64(u32x2{__float_as_uint(O), tag}, r_ws, (tid >> 4) * 4096 + (tid & 15) * 8, WS_PO + h * (32 * 4096) + s * 128, AUX_SC1);
  }
  STAMP(6);
  if (wave < 4) {
    u32x4 po[4];
    const unsigned oo[4] = {(unsigned)lane * 16, (unsigned)lane * 16 + 1024, (unsigned)lane * 16 + 2048, (unsigned)lane * 16 + 3072};
    if (!gather<4, 0>(po, r_ws, oo, WS_PO + wg * 4096, tag)) timed_out = true;
    STAMP(7);
    float o0 = (__uint_as_float(po[0][0]) + __uint_as_float(po[1][0])) + (__uint_as_float(po[2][0]) + __uint_as_float(po[3][0]));
    float o1 = (__uint_as_float(po[0][2]) + __uint_as_float(po[1][2])) + (__uint_as_float(po[2][2]) + __uint_as_float(po[3][2]));
    o0 += dpp<XOR8>(o0); o1 += dpp<XOR8>(o1);
    o0 = x32_sum(x16_sum(o0)); o1 = x32_sum(x16_sum(o1));
    const unsigned opk = pk_bf16(o0, o1);
#pragma unroll
    for (int j = 0; j < 8; ++j) { const unsigned oj = __builtin_amdgcn_readlane(opk, j); y0 = dot2(wop0[j], oj, y0); y1 = dot2(wop1[j], oj, y1); }
    if (timed_out) y0 = __builtin_nanf("");
    const unsigned yoff = tid < D / 2 ? (unsigned)((2 * tid / CPR) * (256 * CPR) + (2 * tid % CPR)) * 8 : OOB;
    __builtin_amdgcn_raw_buffer_store_b128(u32x4{__float_as_uint(y0), tag, __float_as_uint(y1), tag}, r_ws, yoff, WS_YA + wg * (CPR * 8), AUX_SC1);
    STAMP(8);
    if (wave == 0) lds_arrive(lds, C_HOP1, lane);
  }
  if (wg >= NRED) return;
  lds_wait_ge(lds, C_HOP1, 1u);
  {
    constexpr int NLD = (256 * CPR / 2 + NTHR - 1) / NTHR;
    const float xo = tid < CPR ? bf_lo(a.x[wg * CPR + tid]) : 0.f;
    u32x4 t[NLD];
    unsigned yo[NLD];
#pragma unroll
    for (int k = 0; k < NLD; ++k) yo[k] = tid + NTHR * k < 256 * CPR / 2 ? (unsigned)(tid + NTHR * k) * 16 : OOB;
    if (!gather<NLD, 0>(t, r_ws, yo, WS_YA + wg * (256 * CPR * 8), tag)) timed_out = true;
    STAMP(9);
    float s0 = 0.f, s1 = 0.f;
#pragma unroll
    for (int k = 0; k < NLD; ++k) { s0 += __uint_as_float(t[k][0]); s1 += __uint_as_float(t[k][2]); }
    if (CPR / 2 <= 8) { s0 += dpp<XOR8>(s0); s1 += dpp<XOR8>(s1); }
    if (CPR / 2 <= 4) { s0 += dpp<ROR4>(s0); s1 += dpp<ROR4>(s1); }
    if (CPR / 2 <= 2) { s0 += dpp<XOR2>(s0); s1 += dpp<XOR2>(s1); }
    if (CPR / 2 <= 1) { s0 += dpp<XOR1>(s0); s1 += dpp<XOR1>(s1); }
    s0 = x32_sum(x16_sum(s0)); s1 = x32_sum(x16_sum(s1));
    if (timed_out) s0 = __builtin_nanf("");
    if (lane < CPR / 2) *reinterpret_cast<f32x2*>(lds + L_YA + wave * 64 + lane * 8) = f32x2{s0, s1};
    lds_barrier();
    if (tid < CPR) {
      float r = xo;
#pragma unroll
      for (int w = 0; w < 8; ++w) r += *reinterpret_cast<const float*>(lds + L_YA + w * 64 + tid * 4);
      a.out[wg * CPR + tid] = (unsigned short)(pk_bf16(r, r) & 0xffffu);
    }
    if (wg == 0 && tid == 64) __builtin_amdgcn_raw_buffer_store_b32(tag, r_ws, 0, WS_EPOCH, AUX_SC1);
    STAMP(10);
  }
}

}

extern "C" void kernel_launch(void* const* d_in, const int* in_sizes, int n_in, void* d_out, int out_size, void* d_ws, size_t ws_size,
                              hipStream_t stream) {
  (void)in_sizes; (void)n_in; (void)out_size; (void)ws_size;
  Args a;
  a.x = (const unsigned short*)d_in[0];
  a.k = (const unsigned char*)d_in[1];
  a.v = (const unsigned char*)d_in[2];
  a.wq = (const unsigned char*)d_in[3];
  a.wo = (const unsigned char*)d_in[4];
  a.wg = (const unsigned char*)d_in[6];
  a.wu = (const unsigned char*)d_in[7];
  a.wd = (const unsigned char*)d_in[8];
  a.out = (unsigned short*)d_out;
  a.ws = (unsigned char*)d_ws;
  (void)hipFuncSetAttribute((const void*)fused_decode, hipFuncAttributeMaxDynamicSharedMemorySize, LDS_BYTES);
  hipLaunchKernelGGL(fused_decode, dim3(NWG), dim3(NTHR), LDS_BYTES, stream, a);
}
```

```cpp
#include <hip/hip_runtime.h>
#include <hip/hip_bf16.h>
#include <cstdint>

namespace {

typedef unsigned u32x4 __attribute__((ext_vector_type(4)));
typedef unsigned u32x2 __attribute__((ext_vector_type(2)));
typedef float f32x4 __attribute__((ext_vector_type(4)));
typedef float f32x2 __attribute__((ext_vector_type(2)));
typedef __bf16 bf16x8 __attribute__((ext_vector_type(8)));
typedef __bf16 bf16x2 __attribute__((ext_vector_type(2)));
typedef __amdgpu_buffer_rsrc_t rsrc_t;

constexpr int D = 448, SK = 65536, DH = 256, FF = 1792, NQ = 4096;
constexpr int NWG = 256, NTHR = 512;
#define CH_W 7
#define NI 5
#define PRETOUCH 1
#define HK_PRIO 2
#define ST_PRIO 0
#define CP_V 48
#define CP_WAIT 300
#define CP_GAIN 16
#define CP_OFF 30
#define LASTFAST 0
#define WAVEBAL 1
#define Q_FIRST 300
#define Q_SLEEP 0
#define GU_AT 2600
#define CPR 8
constexpr int NRED = D / CPR;
constexpr unsigned TILE = 8192;
constexpr unsigned OOB = 0x40000000u;
constexpr float C2 = 0.0625f * 1.4426950408889634f;
constexpr unsigned SPIN_CAP = 1u << 22;

constexpr unsigned WS_EPOCH = 0;
constexpr unsigned LSTR = 4352;
constexpr unsigned WS_PO = 4096;
constexpr unsigned WS_YA = WS_PO + 8 * 32 * 32 * 16 * 8;
constexpr unsigned WS_GU = WS_YA + 256 * D * 8;
constexpr unsigned WS_QG = WS_GU + 224 * 4 * 128;
constexpr unsigned WS_ML = WS_QG + 128 * LSTR;
constexpr unsigned WS_END = WS_ML + 256 * LSTR;

constexpr int L_RED = 0;
constexpr int L_CTL = 1024;
constexpr int C_PART = L_CTL, C_QRDY = L_CTL + 4, C_TAG = L_CTL + 12, C_LANDED = L_CTL + 16, C_KHELP = L_CTL + 20, C_VDONE = L_CTL + 24, C_FAST = L_CTL + 28,
              C_HRDY = L_CTL + 32, C_HOP1 = L_CTL + 36;
constexpr int L_ML = 1088;
constexpr int L_SC = 1152;
constexpr int L_H = 1184;
constexpr int L_YA = 1216;
constexpr int L_Q = 1792;
constexpr int L_XN = 2816;
constexpr int L_ACC = 4864;
constexpr int L_P = L_ACC + 4 * 2048;
constexpr int LDS_BYTES = 84 * 1024;

constexpr int AUX_NT = 2, AUX_SC1 = 16;
#define SB() __builtin_amdgcn_sched_barrier(0)
#define FENCE() asm volatile("" ::: "memory")

__device__ __forceinline__ float bf_lo(unsigned u) { return __builtin_bit_cast(float, u << 16); }
__device__ __forceinline__ float bf_hi(unsigned u) { return __builtin_bit_cast(float, u & 0xffff0000u); }
__device__ __forceinline__ unsigned pk_bf16(float lo, float hi) {
  bf16x2 v = {(__bf16)lo, (__bf16)hi};
  return __builtin_bit_cast(unsigned, v);
}
__device__ __forceinline__ float rnd_bf16(float x) { return (float)(__bf16)x; }
__device__ __forceinline__ float dot2(unsigned a, unsigned b, float acc) {
  return __builtin_amdgcn_fdot2_f32_bf16(__builtin_bit_cast(bf16x2, a), __builtin_bit_cast(bf16x2, b), acc, false);
}
template <int CTRL> __device__ __forceinline__ float dpp(float x) {
  return __builtin_bit_cast(float, __builtin_amdgcn_mov_dpp(__builtin_bit_cast(int, x), CTRL, 0xf, 0xf, true));
}
constexpr int XOR1 = 0xB1, XOR2 = 0x4E, XOR7 = 0x141, XOR15 = 0x140, XOR8 = 0x128, ROR4 = 0x124;
__device__ __forceinline__ float x16_max(float x) {
  auto s = __builtin_amdgcn_permlane16_swap(__float_as_uint(x), __float_as_uint(x), false, false);
  return fmaxf(__uint_as_float(s[0]), __uint_as_float(s[1]));
}
__device__ __forceinline__ float x32_max(float x) {
  auto s = __builtin_amdgcn_permlane32_swap(__float_as_uint(x), __float_as_uint(x), false, false);
  return fmaxf(__uint_as_float(s[0]), __uint_as_float(s[1]));
}
__device__ __forceinline__ float x16_sum(float x) {
  auto s = __builtin_amdgcn_permlane16_swap(__float_as_uint(x), __float_as_uint(x), false, false);
  return __uint_as_float(s[0]) + __uint_as_float(s[1]);
}
__device__ __forceinline__ float x32_sum(float x) {
  auto s = __builtin_amdgcn_permlane32_swap(__float_as_uint(x), __float_as_uint(x), false, false);
  return __uint_as_float(s[0]) + __uint_as_float(s[1]);
}
__device__ __forceinline__ float wave_sum(float x) {
  x += dpp<XOR1>(x); x += dpp<XOR2>(x); x += dpp<XOR7>(x); x += dpp<XOR15>(x);
  return x32_sum(x16_sum(x));
}
__device__ __forceinline__ float parity_sum(float x) {
  x += dpp<XOR2>(x); x += dpp<ROR4>(x); x += dpp<XOR8>(x);
  return x32_sum(x16_sum(x));
}
__device__ __forceinline__ float parity_max(float x) {
  x = fmaxf(x, dpp<XOR2>(x)); x = fmaxf(x, dpp<ROR4>(x)); x = fmaxf(x, dpp<XOR8>(x));
  return x32_max(x16_max(x));
}
__device__ __forceinline__ void lds_barrier() {
  asm volatile("s_waitcnt lgkmcnt(0)" ::: "memory");
  __builtin_amdgcn_s_barrier();
  FENCE();
}
__device__ __forceinline__ rsrc_t mk_rsrc(const void* p, unsigned bytes) {
  return __builtin_amdgcn_make_buffer_rsrc(const_cast<void*>(p), 0, bytes, 0x00020000);
}
__device__ __forceinline__ unsigned lds_ld(unsigned char* lds, int off) {
  return __builtin_amdgcn_readfirstlane(__hip_atomic_load(reinterpret_cast<unsigned*>(lds + off), __ATOMIC_RELAXED, __HIP_MEMORY_SCOPE_WORKGROUP));
}
template <int SLEEP = 1>
__device__ __forceinline__ void lds_wait_ge(unsigned char* lds, int off, unsigned want) {
#pragma nounroll
  for (unsigned spin = 0; spin < SPIN_CAP * 8u; ++spin) {
    if (lds_ld(lds, off) >= want) break;
    __builtin_amdgcn_s_sleep(SLEEP);
  }
  FENCE();
}
__device__ __forceinline__ void lds_arrive(unsigned char* lds, int off, int lane) {
  asm volatile("s_waitcnt lgkmcnt(0)" ::: "memory");
  if (lane == 0) __hip_atomic_fetch_add(reinterpret_cast<unsigned*>(lds + off), 1u, __ATOMIC_RELAXED, __HIP_MEMORY_SCOPE_WORKGROUP);
}
__device__ __forceinline__ unsigned now() { return (unsigned)__builtin_amdgcn_s_memrealtime(); }
template <int SLEEP>
__device__ __forceinline__ void sleep_until(unsigned t0, unsigned ticks) {
#pragma nounroll
  for (unsigned spin = 0; spin < SPIN_CAP && now() - t0 < ticks; ++spin) __builtin_amdgcn_s_sleep(SLEEP);
}
__device__ __forceinline__ void chain_wait(unsigned char* lds, int idx) {
#pragma nounroll
  for (unsigned spin = 0; spin < SPIN_CAP * 8u; ++spin) {
    if ((int)lds_ld(lds, C_LANDED) >= idx + 1 - CH_W || lds_ld(lds, C_QRDY) != 0u) break;
    __builtin_amdgcn_s_sleep(1);
  }
  FENCE();
}
template <int N, int SLEEP>
__device__ __forceinline__ bool gather(u32x4 (&v)[N], rsrc_t rs, const unsigned (&voff)[N], unsigned soff, unsigned tag) {
  bool have[N];
#pragma unroll
  for (int k = 0; k < N; ++k) { have[k] = voff[k] == OOB; v[k] = u32x4{0u, 0u, 0u, 0u}; }
#pragma nounroll
  for (unsigned spin = 0; spin < SPIN_CAP; ++spin) {
    FENCE();
    u32x4 t[N];
#pragma unroll
    for (int k = 0; k < N; ++k) t[k] = __builtin_amdgcn_raw_buffer_load_b128(rs, have[k] ? OOB : voff[k], soff, AUX_SC1);
    bool all = true;
#pragma unroll
    for (int k = 0; k < N; ++k) {
      const bool now = !have[k] && t[k][1] == tag && t[k][3] == tag;
      v[k][0] = now ? t[k][0] : v[k][0]; v[k][2] = now ? t[k][2] : v[k][2];
      have[k] = have[k] || now; all = all && have[k];
    }
    if (__builtin_amdgcn_ballot_w64(!all) == 0) return true;
    if (SLEEP > 0) __builtin_amdgcn_s_sleep(SLEEP);
  }
  return false;
}
#define STAMP(slot) do {} while (0)
#define PROG() do {} while (0)
#define PROG_OUT() do {} while (0)
#define PROGL() do {} while (0)

struct Args {
  const unsigned short* x;
  const unsigned char* k;
  const unsigned char* v;
  const unsigned char* wq;
  const unsigned char* wo;
  const unsigned char* wg;
  const unsigned char* wu;
  const unsigned char* wd;
  unsigned short* out;
  unsigned char* ws;
};

__host__ __device__ constexpr int piece(int e, int n) { return n < 2 ? 2 * e + n : 2 * n + e; }
template <int AUX>
__device__ __forceinline__ void issue(u32x4 (&r)[8], rsrc_t rs, unsigned v0, unsigned v1, unsigned soff) {
#pragma unroll
  for (int i = 0; i < 8; ++i) { r[i] = __builtin_amdgcn_raw_buffer_load_b128(rs, ((i & 1) ? v1 : v0) + (i >> 1) * 512, soff, AUX); SB(); }
}
__device__ __forceinline__ f32x4 mfma(u32x4 x, u32x4 y, f32x4 acc) {
  return __builtin_amdgcn_mfma_f32_16x16x32_bf16(__builtin_bit_cast(bf16x8, x), __builtin_bit_cast(bf16x8, y), acc, 0, 0, 0);
}
__device__ __forceinline__ void qk_mfma(const u32x4 (&r)[8], const u32x4 (&qf)[2], f32x4 (&d)[4]) {
#pragma unroll
  for (int j = 0; j < 4; ++j) d[j] = mfma(qf[1], r[2 * j + 1], mfma(qf[0], r[2 * j], f32x4{0.f, 0.f, 0.f, 0.f}));
  SB();
}
__device__ __forceinline__ unsigned short qk_score(const f32x4 (&d)[4], bool b0, bool b1) {
  float v[4];
#pragma unroll
  for (int j = 0; j < 4; ++j) {
    float e0 = d[j][0], e1 = d[j][1], e2 = d[j][2], e3 = d[j][3];
    asm("" : "+v"(e0), "+v"(e2));
    const float lo = b0 ? e1 : e0, hi = b0 ? e3 : e2;
    v[j] = b1 ? hi : lo;
  }
  const float u0 = (b0 ? v[1] : v[0]) + dpp<XOR1>(b0 ? v[0] : v[1]);
  const float u1 = (b0 ? v[3] : v[2]) + dpp<XOR1>(b0 ? v[2] : v[3]);
  const float w = (b1 ? u1 : u0) + dpp<XOR2>(b1 ? u0 : u1);
  return (unsigned short)(pk_bf16(w, w) & 0xffffu);
}
__device__ __forceinline__ void pv_mfma(const u32x4 (&vr)[8], u32x2 pk, int msel, f32x4& acc0, f32x4& acc1) {
#pragma unroll
  for (int q = 0; q < 4; ++q) {
    const u32x4 pa = {msel == 2 * q ? pk[0] : 0u, msel == 2 * q ? pk[1] : 0u, msel == 2 * q + 1 ? pk[0] : 0u, msel == 2 * q + 1 ? pk[1] : 0u};
    const unsigned sel = (q & 1) ? 0x07060302u : 0x05040100u;
    const int d0 = q >> 1, d1 = 2 + (q >> 1);
    const u32x4 b0 = {__builtin_amdgcn_perm(vr[2][d0], vr[0][d0], sel), __builtin_amdgcn_perm(vr[6][d0], vr[4][d0], sel),
                      __builtin_amdgcn_perm(vr[3][d0], vr[1][d0], sel), __builtin_amdgcn_perm(vr[7][d0], vr[5][d0], sel)};
    const u32x4 b1 = {__builtin_amdgcn_perm(vr[2][d1], vr[0][d1], sel), __builtin_amdgcn_perm(vr[6][d1], vr[4][d1], sel),
                      __builtin_amdgcn_perm(vr[3][d1], vr[1][d1], sel), __builtin_amdgcn_perm(vr[7][d1], vr[5][d1], sel)};
    acc0 = mfma(pa, b0, acc0);
    acc1 = mfma(pa, b1, acc1);
  }
  SB();
}
__device__ __forceinline__ void fma8(float (&s)[8], float xv, u32x4 w) {
#pragma unroll
  for (int i = 0; i < 4; ++i) { s[2 * i] = fmaf(xv, bf_lo(w[i]), s[2 * i]); s[2 * i + 1] = fmaf(xv, bf_hi(w[i]), s[2 * i + 1]); }
}
template <bool WHOLE>
__device__ __forceinline__ void load_qf(u32x4 (&qf)[2], const unsigned char* lds, int c, int g) {
#pragma unroll
  for (int e = 0; e < 2; ++e) qf[e] = *reinterpret_cast<const u32x4*>(lds + L_Q + ((c >> 2) & 1) * 512 + ((WHOLE ? 4 * e + g : piece(e, g)) * 4 + (c & 3)) * 16);
}

template <int AUX_K, int AUX_V>
__device__ __forceinline__ void body(const Args& a) {
  extern __shared__ __attribute__((aligned(16))) unsigned char lds[];
  const int tid = threadIdx.x, lane = tid & 63, wave = __builtin_amdgcn_readfirstlane(tid >> 6);
  const int wg = blockIdx.x, h = wg >> 5, s = wg & 31;
  const int c = lane & 15, g = lane >> 4;
  STAMP(0);
  const rsrc_t r_ws = mk_rsrc(a.ws, WS_END);
  const rsrc_t r_k = mk_rsrc(a.k, 8u * SK * DH * 2);
  const unsigned wg_off = (unsigned)wg * (128 * TILE);
  const unsigned vw0 = (c >> 2) * 2048 + g * 64 + (c & 3) * 16, vw1 = vw0 + 256;
  const bool b0 = (c & 1) != 0, b1 = (c & 2) != 0;
  const int sc_lane = (g & 1) * 32 + c * 2;
  bool timed_out = false;
  unsigned tag;
  unsigned wop0[8], wop1[8];
  float y0 = 0.f, y1 = 0.f;

  if (wave < 4) {
    __builtin_amdgcn_s_setprio(3);
    const unsigned t_start = now();
    const bool qwg = (s & 8) == 0;
    const rsrc_t r_x = mk_rsrc(a.x, D * 2);
    const rsrc_t r_wq = mk_rsrc(a.wq, qwg ? D * NQ * 2 : 0);
    const int qsl = (s & 7) * 2 + (s >> 4);
    const u32x4 xv = __builtin_amdgcn_raw_buffer_load_b128(r_x, lane * 16, 0, 0);
    const unsigned epoch = __builtin_amdgcn_raw_buffer_load_b32(r_ws, 0, WS_EPOCH, AUX_SC1);
    const int qrow = (lane >> 2) + 16 * wave;
    const unsigned wq_off = (unsigned)qrow * (NQ * 2) + (unsigned)(h * 512 + qsl * 32) * 2 + (lane & 3) * 16;
    u32x4 wv[7];
#pragma unroll
    for (int i = 0; i < 7; ++i) wv[i] = __builtin_amdgcn_raw_buffer_load_b128(r_wq, wq_off, i * 64 * NQ * 2, AUX_NT);
    SB();
    if (tid < 16) *reinterpret_cast<unsigned*>(lds + L_CTL + tid * 4) = 0u;
    lds_barrier();

    {
      float xs[8];
#pragma unroll
      for (int i = 0; i < 4; ++i) { xs[2 * i] = bf_lo(xv[i]); xs[2 * i + 1] = bf_hi(xv[i]); }
      float sm = 0.f;
#pragma unroll
      for (int i = 0; i < 8; ++i) sm += xs[i];
      const float mean = wave_sum(sm) * (1.0f / D);
      float sq = 0.f;
#pragma unroll
      for (int i = 0; i < 8; ++i) { const float d = xs[i] - mean; sq = fmaf(d, d, sq); }
      sq = lane < D / 8 ? sq : 0.f;
      const float var = wave_sum(sq) * (1.0f / D);
      const float mean_b = rnd_bf16(mean);
      const float den_b = rnd_bf16(sqrtf(rnd_bf16(rnd_bf16(var) + 1e-5f)));
#pragma unroll
      for (int i = 0; i < 8; ++i) xs[i] = rnd_bf16(rnd_bf16(xs[i] - mean_b) / den_b);
      f32x4* xp = reinterpret_cast<f32x4*>(lds + L_XN + lane * 32);
      xp[0] = f32x4{xs[0], xs[1], xs[2], xs[3]}; xp[1] = f32x4{xs[4], xs[5], xs[6], xs[7]};
      FENCE();
    }
    tag = __builtin_amdgcn_readfirstlane(epoch) + 1u;
    if (qwg) {
      float sacc[8] = {0.f, 0.f, 0.f, 0.f, 0.f, 0.f, 0.f, 0.f};
#pragma unroll
      for (int i = 0; i < 7; ++i) fma8(sacc, *reinterpret_cast<const float*>(lds + L_XN + (qrow + 64 * i) * 4), wv[i]);
#pragma unroll
      for (int i = 0; i < 8; ++i) { float t = sacc[i]; t += dpp<ROR4>(t); t += dpp<XOR8>(t); sacc[i] = x32_sum(x16_sum(t)); }
      if (lane < 4) {
        f32x4* p = reinterpret_cast<f32x4*>(lds + L_RED + wave * 256 + lane * 32);
        p[0] = f32x4{sacc[0], sacc[1], sacc[2], sacc[3]}; p[1] = f32x4{sacc[4], sacc[5], sacc[6], sacc[7]};
      }
      lds_arrive(lds, C_PART, lane);
      if (wave == 0) {
        lds_wait_ge(lds, C_PART, 4u);
        if (lane < 16) {
          float q0 = 0.f, q1 = 0.f;
#pragma unroll
          for (int w = 0; w < 4; ++w) { const f32x2 t = *reinterpret_cast<const f32x2*>(lds + L_RED + w * 256 + lane * 8); q0 += t[0]; q1 += t[1]; }
          __builtin_amdgcn_raw_buffer_store_b64(u32x2{pk_bf16(q0, q1), tag}, r_ws, lane * 8, WS_QG + (h * 16 + qsl) * LSTR, AUX_SC1);
        }
        STAMP(1);
      }
    }
    if (wave == 0) {
      u32x4 qg[2];
      const unsigned qo[2] = {(lane >> 2) * LSTR + (lane & 3) * 32, (lane >> 2) * LSTR + (lane & 3) * 32 + 16};
      sleep_until<1>(t_start, Q_FIRST);
      timed_out = !gather<2, Q_SLEEP>(qg, r_ws, qo, WS_QG + h * 16 * LSTR, tag);
      *reinterpret_cast<u32x4*>(lds + L_Q + lane * 16) = u32x4{qg[0][0], qg[0][2], qg[1][0], qg[1][2]};
      if (lane == 0) *reinterpret_cast<unsigned*>(lds + C_TAG) = tag;
      lds_arrive(lds, C_QRDY, lane);
    }
    if (wave != 0) {
      __builtin_amdgcn_s_setprio(HK_PRIO);
      const int slot = NI + wave - 1;
      u32x4 kt[4][8];
#pragma unroll
      for (int i = 0; i < 4; ++i) { chain_wait(lds, 4 * slot + i); issue<AUX_K>(kt[i], r_k, vw0, vw1, wg_off + (4 * slot + i) * TILE); }
      asm volatile("s_waitcnt vmcnt(24)" ::: "memory"); lds_arrive(lds, C_LANDED, lane); PROGL();
      asm volatile("s_waitcnt vmcnt(16)" ::: "memory"); lds_arrive(lds, C_LANDED, lane); PROGL();
      asm volatile("s_waitcnt vmcnt(8)" ::: "memory"); lds_arrive(lds, C_LANDED, lane); PROGL();
      asm volatile("s_waitcnt vmcnt(0)" ::: "memory"); lds_arrive(lds, C_LANDED, lane); PROGL();
      lds_wait_ge(lds, C_QRDY, 1u);
      u32x4 qf[2];
      load_qf<true>(qf, lds, c, g);
#pragma unroll
      for (int i = 0; i < 4; ++i) {
        f32x4 d[4];
        qk_mfma(kt[i], qf, d);
        *reinterpret_cast<unsigned short*>(lds + L_P + i * 2048 + slot * 64 + sc_lane) = qk_score(d, b0, b1);
        PROG();
      }
      lds_arrive(lds, C_KHELP, lane);
      __builtin_amdgcn_s_setprio(3);
    }
    STAMP(3);
    const int fb = h * 16 + (s & 7) + ((s >> 4) << 3);
    if (!qwg && fb < 112) {
      const rsrc_t r_gu = mk_rsrc((wave & 1) ? a.wu : a.wg, D * FF * 2);
      const int frow = (fb / 28) * 112 + (wave >> 1) * 56 + (lane >> 3);
      const unsigned goff = (unsigned)frow * (FF * 2) + (fb % 28) * 128 + (lane & 7) * 16;
#pragma unroll
      for (int i = 0; i < 7; ++i) wv[i] = __builtin_amdgcn_raw_buffer_load_b128(r_gu, goff, i * 8 * FF * 2, AUX_NT);
      float sg[8] = {0.f, 0.f, 0.f, 0.f, 0.f, 0.f, 0.f, 0.f};
#pragma unroll
      for (int i = 0; i < 7; ++i) fma8(sg, *reinterpret_cast<const float*>(lds + L_XN + (frow + 8 * i) * 4), wv[i]);
#pragma unroll
      for (int i = 0; i < 8; ++i) { float t = sg[i]; t += dpp<XOR8>(t); sg[i] = x32_sum(x16_sum(t)); }
      if (lane < 8) {
        f32x4* p = reinterpret_cast<f32x4*>(lds + L_RED + wave * 256 + lane * 32);
        p[0] = f32x4{sg[0], sg[1], sg[2], sg[3]}; p[1] = f32x4{sg[4], sg[5], sg[6], sg[7]};
      }
      lds_arrive(lds, C_PART, lane);
      if (wave == 1) {
        lds_wait_ge(lds, C_PART, 4u);
        const int o = ((lane >> 2) & 1) * 256 + (lane >> 3) * 32 + (lane & 3) * 8;
        const f32x2 t0 = *reinterpret_cast<const f32x2*>(lds + L_RED + o), t1 = *reinterpret_cast<const f32x2*>(lds + L_RED + 512 + o);
        __builtin_amdgcn_raw_buffer_store_b128(u32x4{__float_as_uint(t0[0] + t1[0]), tag, __float_as_uint(t0[1] + t1[1]), tag}, r_ws,
                                               (lane >> 3) * 512 + (lane & 7) * 16, WS_GU + (fb % 28) * 4096 + (fb / 28) * 128, AUX_SC1);
      }
    }
    {
      const rsrc_t r_wo = mk_rsrc(a.wo, NQ * D * 2);
      const unsigned coff = tid < D / 2 ? tid * 4 : OOB;
      const bool ffn = wg < FF / 8;
      const rsrc_t r_d = mk_rsrc(a.wd, ffn ? FF * D * 2 : 0);
      unsigned draw[8], wraw[16];
#pragma unroll
      for (int j = 0; j < 8; ++j) draw[j] = __builtin_amdgcn_raw_buffer_load_b32(r_d, coff, (wg * 8 + j) * (D * 2), AUX_NT);
#pragma unroll
      for (int j = 0; j < 16; ++j) wraw[j] = __builtin_amdgcn_raw_buffer_load_b32(r_wo, coff, (wg * 16 + j) * (D * 2), AUX_NT);
#pragma unroll
      for (int j = 0; j < 8; ++j) { wop0[j] = __builtin_amdgcn_perm(wraw[2 * j + 1], wraw[2 * j], 0x05040100u); wop1[j] = __builtin_amdgcn_perm(wraw[2 * j + 1], wraw[2 * j], 0x07060302u); }
      if (wave == 3) {
        sleep_until<32>(t_start, GU_AT);
        u32x4 gu[1];
        const unsigned go[1] = {(ffn && lane < 32) ? (unsigned)lane * 16 : OOB};
        if (!gather<1, 64>(gu, r_ws, go, WS_GU + wg * 512, tag)) timed_out = true;
        float g0 = __uint_as_float(gu[0][0]), g1 = __uint_as_float(gu[0][2]);
        g0 += dpp<XOR8>(g0); g1 += dpp<XOR8>(g1);
        g0 = rnd_bf16(x16_sum(g0)); g1 = rnd_bf16(x16_sum(g1));
        const float u0 = dpp<ROR4>(g0), u1 = dpp<ROR4>(g1);
        const float h0 = rnd_bf16((g0 > 0.f ? g0 : g0 * 0.f) * u0), h1 = rnd_bf16((g1 > 0.f ? g1 : g1 * 0.f) * u1);
        if (lane < 4) *reinterpret_cast<f32x2*>(lds + L_H + lane * 8) = f32x2{h0, h1};
        lds_arrive(lds, C_HRDY, lane);
      }
      lds_wait_ge<32>(lds, C_HRDY, 1u);
#pragma unroll
      for (int j = 0; j < 8; ++j) {
        const float hv = *reinterpret_cast<const float*>(lds + L_H + j * 4);
        y0 = fmaf(hv, bf_lo(draw[j]), y0); y1 = fmaf(hv, bf_hi(draw[j]), y1);
      }
      asm volatile("" : "+v"(y0), "+v"(y1), "+v"(wop0[0]), "+v"(wop0[1]), "+v"(wop0[2]), "+v"(wop0[3]), "+v"(wop0[4]), "+v"(wop0[5]), "+v"(wop0[6]), "+v"(wop0[7]),
                        "+v"(wop1[0]), "+v"(wop1[1]), "+v"(wop1[2]), "+v"(wop1[3]), "+v"(wop1[4]), "+v"(wop1[5]), "+v"(wop1[6]), "+v"(wop1[7]));
    }
    STAMP(11);
    if (wave == 2) {
      const rsrc_t r_v = mk_rsrc(a.v, 8u * SK * DH * 2), r_o = mk_rsrc(a.out, D * 2);
      const unsigned tv = __builtin_amdgcn_raw_buffer_load_b32(r_v, lane < 16 ? lane * 65536 : OOB, wg_off, AUX_NT);
      const unsigned to = __builtin_amdgcn_raw_buffer_load_b32(r_o, lane < 1 ? 0 : OOB, 0, AUX_NT);
      asm volatile("" :: "v"(tv), "v"(to));
    }
    if (wave == 1) {
      lds_wait_ge<8>(lds, C_VDONE, 0x10000u * 4u + CP_V);
      const unsigned t_cp = now();
      const int hd = lane & 1;
      float mw[4], lw[4];
#pragma unroll
      for (int w = 0; w < 4; ++w) { const f32x2 t = *reinterpret_cast<const f32x2*>(lds + L_ML + w * 16 + hd * 8); mw[w] = t[0]; lw[w] = t[1]; }
      const float M = fmaxf(fmaxf(mw[0], mw[1]), fmaxf(mw[2], mw[3]));
      float L = 0.f;
#pragma unroll
      for (int w = 0; w < 4; ++w) L = fmaf(__builtin_amdgcn_exp2f((mw[w] - M) * C2), lw[w], L);
      __builtin_amdgcn_raw_buffer_store_b128(u32x4{(lane & 2) ? t_cp : __float_as_uint(M), tag, (lane & 2) ? t_cp : __float_as_uint(L), tag}, r_ws,
                                             lane < 8 ? lane * 16 : OOB, WS_ML + wg * LSTR, AUX_SC1);
      sleep_until<16>(t_cp, CP_WAIT);
      u32x4 ml[2];
      const unsigned mo[2] = {(lane >> 2) * LSTR + (lane & 3) * 16, (16 + (lane >> 2)) * LSTR + (lane & 3) * 16};
      if (!gather<2, 32>(ml, r_ws, mo, WS_ML + h * 32 * LSTR, tag)) timed_out = true;
      const float Ma = __uint_as_float(ml[0][0]), Mb = __uint_as_float(ml[1][0]);
      float Mg = fmaxf(Ma, Mb);
      Mg = fmaxf(Mg, dpp<ROR4>(Mg)); Mg = fmaxf(Mg, dpp<XOR8>(Mg)); Mg = x32_max(x16_max(Mg));
      float Lg = __builtin_amdgcn_exp2f((Ma - Mg) * C2) * __uint_as_float(ml[0][2]) + __builtin_amdgcn_exp2f((Mb - Mg) * C2) * __uint_as_float(ml[1][2]);
      Lg = (lane & 2) ? (float)(int)(t_cp - ml[0][0]) + (float)(int)(t_cp - ml[1][0]) : Lg;
      Lg += dpp<ROR4>(Lg); Lg += dpp<XOR8>(Lg); Lg = x32_sum(x16_sum(Lg));
      if (lane < 2) {
#pragma unroll
        for (int w = 0; w < 4; ++w) *reinterpret_cast<float*>(lds + L_SC + w * 8 + lane * 4) = timed_out ? __builtin_nanf("") : __builtin_amdgcn_exp2f((mw[w] - Mg) * C2) / Lg;
      }
      if (lane == 2) {
        const float nf = fminf((Lg * (1.0f / 32) + (float)CP_OFF) * (0.01f * CP_GAIN), 128.f);
        if (nf >= 1.f) *reinterpret_cast<unsigned*>(lds + C_FAST) = (*reinterpret_cast<const unsigned*>(lds + C_VDONE) & 0xffffu) + 8u + (unsigned)nf;
      }
      STAMP(14);
    }
  } else {
    const int w = wave - 4;
    const rsrc_t r_v = mk_rsrc(a.v, 8u * SK * DH * 2);
    const unsigned vv0 = g * 2048 + piece(0, c >> 2) * 64 + (c & 3) * 16, vv1 = g * 2048 + piece(1, c >> 2) * 64 + (c & 3) * 16;
    const unsigned vk0 = (c >> 2) * 2048 + piece(0, g) * 64 + (c & 3) * 16, vk1 = (c >> 2) * 2048 + piece(1, g) * 64 + (c & 3) * 16;
    const unsigned t0 = wg_off + w * TILE;
    u32x4 ra[8], rb[8];
    __builtin_amdgcn_s_setprio(ST_PRIO);
    u32x4 rx[NI - 2][8];
    if (s & 8) issue<AUX_K>(ra, r_k, vw0, vw1, t0);
    lds_barrier();
    if (!(s & 8)) issue<AUX_K>(ra, r_k, vw0, vw1, t0);
#define CHAIN_STEP(R, SLOT) do { \
      constexpr bool first_ = CH_W < 5;     \
      if (first_ && lds_ld(lds, C_QRDY) == 0u) { asm volatile("s_waitcnt vmcnt(0)" ::: "memory"); lds_arrive(lds, C_LANDED, lane); PROGL(); } \
      chain_wait(lds, 4 * (SLOT) + w); \
      issue<AUX_K>(R, r_k, vw0, vw1, t0 + 4 * (SLOT) * TILE); \
      if (!first_ && lds_ld(lds, C_QRDY) == 0u) { asm volatile("s_waitcnt vmcnt(8)" ::: "memory"); lds_arrive(lds, C_LANDED, lane); PROGL(); } } while (0)
    CHAIN_STEP(rb, 1);
#pragma unroll
    for (int i = 2; i < NI; ++i) CHAIN_STEP(rx[i - 2], i);
#undef CHAIN_STEP
    if (lds_ld(lds, C_QRDY) == 0u) { asm volatile("s_waitcnt vmcnt(0)" ::: "memory"); lds_arrive(lds, C_LANDED, lane); PROGL(); }
    lds_wait_ge(lds, C_QRDY, 1u);
    tag = lds_ld(lds, C_TAG);
    STAMP(2);
    u32x4 qf[2];
    load_qf<true>(qf, lds, c, g);
    unsigned char* const pl = lds + L_P + w * 2048;
    {
      f32x4 d[4];
      unsigned short* sw = reinterpret_cast<unsigned short*>(pl + sc_lane);
      unsigned toff = t0 + 4 * (NI + 3) * TILE;
#define K_STEP(R, ISSUE_NEXT, SLOT) do { qk_mfma(R, qf, d); PROG(); ISSUE_NEXT; sw[(SLOT) * 32] = qk_score(d, b0, b1); } while (0)
      K_STEP(ra, issue<AUX_K>(ra, r_k, vk0, vk1, toff), 0);
      K_STEP(rb, issue<AUX_K>(rb, r_k, vk0, vk1, toff + 4 * TILE), 1);
#pragma unroll
      for (int i = 2; i < NI; ++i) K_STEP(rx[i - 2], (void)0, i);
      load_qf<false>(qf, lds, c, g);
      sw += (NI + 3) * 32;
      for (int it = 1; it < (29 - NI) / 2; ++it) {
        toff += 8 * TILE;
        K_STEP(ra, issue<AUX_K>(ra, r_k, vk0, vk1, toff), 0);
        K_STEP(rb, issue<AUX_K>(rb, r_k, vk0, vk1, toff + 4 * TILE), 1);
        sw += 2 * 32;
      }
      K_STEP(ra, issue<AUX_V>(ra, r_v, vv0, vv1, t0), 0);
      K_STEP(rb, issue<AUX_V>(rb, r_v, vv0, vv1, t0 + 4 * TILE), 1);
#undef K_STEP
    }
    STAMP(12);
    lds_wait_ge(lds, C_KHELP, 3u);
    {
      u32x4* pp = reinterpret_cast<u32x4*>(pl + lane * 32);
      u32x4 t[2] = {pp[0], pp[1]};
      float mx = bf_lo(t[0][0]);
#pragma unroll
      for (int j = 0; j < 2; ++j)
#pragma unroll
        for (int i = 0; i < 4; ++i) mx = fmaxf(mx, fmaxf(bf_lo(t[j][i]), bf_hi(t[j][i])));
      mx = parity_max(mx);
      const float mc = mx * C2;
      float lp = 0.f;
#pragma unroll
      for (int j = 0; j < 2; ++j) {
#pragma unroll
        for (int i = 0; i < 4; ++i) {
          const float p0 = __builtin_amdgcn_exp2f(fmaf(bf_lo(t[j][i]), C2, -mc)), p1 = __builtin_amdgcn_exp2f(fmaf(bf_hi(t[j][i]), C2, -mc));
          lp += p0 + p1; t[j][i] = pk_bf16(p0, p1);
        }
        pp[j] = t[j];
      }
      lp = parity_sum(lp);
      if (lane < 2) *reinterpret_cast<f32x2*>(lds + L_ML + w * 16 + lane * 8) = f32x2{mx, lp};
      asm volatile("s_waitcnt lgkmcnt(0)" ::: "memory");
      if (lane == 0) __hip_atomic_fetch_add(reinterpret_cast<unsigned*>(lds + C_VDONE), 0x10000u, __ATOMIC_RELAXED, __HIP_MEMORY_SCOPE_WORKGROUP);
    }
    f32x4 acc0 = {0.f, 0.f, 0.f, 0.f}, acc1 = {0.f, 0.f, 0.f, 0.f};
    {
      const int msel = c >> 1;
      const unsigned char* pr = pl + (c & 1) * 32 + g * 8;
      const unsigned vf0 = g * 2048 + c * 16;
      float* const rec = reinterpret_cast<float*>(lds + L_ACC + w * 2048) + 8 * (c & 3) + g;
      auto put = [&](bool whole) {
        const int e0 = (whole ? c >> 2 : piece(0, c >> 2)) * 32, e1 = (whole ? 4 + (c >> 2) : piece(1, c >> 2)) * 32;
#pragma unroll
        for (int k = 0; k < 4; ++k) { float* r = rec + (k & 1) * 256 + ((k >> 1) ? e1 : e0); r[0] = acc0[k]; r[4] = acc1[k]; }
      };
      auto get = [&](bool whole) {
        const int e0 = (whole ? c >> 2 : piece(0, c >> 2)) * 32, e1 = (whole ? 4 + (c >> 2) : piece(1, c >> 2)) * 32;
#pragma unroll
        for (int k = 0; k < 4; ++k) { const float* r = rec + (k & 1) * 256 + ((k >> 1) ? e1 : e0); acc0[k] = r[0]; acc1[k] = r[4]; }
      };
      unsigned toff = t0 + 8 * TILE;
      bool cur = false;
#define V_STEP(R, ISSUE_NEXT) do { const u32x2 pk_ = *reinterpret_cast<const u32x2*>(pr); pr += 64; pv_mfma(R, pk_, msel, acc0, acc1); PROG(); ISSUE_NEXT; } while (0)
      for (int it = 0; it < 15; ++it) {
        const u32x2 cw = *reinterpret_cast<const u32x2*>(lds + C_VDONE);
        const bool nxt = (unsigned)(8 * it + 8 + w) < __builtin_amdgcn_readfirstlane(cw[1]) || 8 * it + 8 >= 128 - LASTFAST;
        if ((unsigned)(8 * it + 4) < (__builtin_amdgcn_readfirstlane(cw[0]) & 0xffffu)) __builtin_amdgcn_s_setprio(1); else __builtin_amdgcn_s_setprio(0);
        const unsigned n0 = nxt ? vf0 : vv0, n1 = nxt ? vf0 + 256 : vv1;
        V_STEP(ra, issue<AUX_V>(ra, r_v, n0, n1, toff));
        V_STEP(rb, issue<AUX_V>(rb, r_v, n0, n1, toff + 4 * TILE));
        if (lane == 0) __hip_atomic_fetch_add(reinterpret_cast<unsigned*>(lds + C_VDONE), 2u, __ATOMIC_RELAXED, __HIP_MEMORY_SCOPE_WORKGROUP);
        if (nxt != cur) { put(cur); FENCE(); get(nxt); cur = nxt; }
        FENCE();
        toff += 8 * TILE;
      }
      V_STEP(ra, (void)0);
      V_STEP(rb, (void)0);
#undef V_STEP
      STAMP(3);
      put(cur);
    }
#pragma unroll
    for (int j = 0; j < 8; ++j) { wop0[j] = 0u; wop1[j] = 0u; }
  }
  PROG_OUT();
  STAMP(4);
  lds_barrier();
  STAMP(5);
  {
    float O = 0.f;
#pragma unroll
    for (int w = 0; w < 4; ++w)
      O = fmaf(*reinterpret_cast<const float*>(lds + L_SC + w * 8 + (tid >> 8) * 4), *reinterpret_cast<const float*>(lds + L_ACC + w * 2048 + tid * 4), O);
    __builtin_amdgcn_raw_buffer_store_b64(u32x2{__float_as_uint(O), tag}, r_ws, (tid >> 4) * 4096 + (tid & 15) * 8, WS_PO + h * (32 * 4096) + s * 128, AUX_SC1);
  }
  STAMP(6);
  if (wave < 4) {
    u32x4 po[4];
    const unsigned oo[4] = {(unsigned)lane * 16, (unsigned)lane * 16 + 1024, (unsigned)lane * 16 + 2048, (unsigned)lane * 16 + 3072};
    if (!gather<4, 0>(po, r_ws, oo, WS_PO + wg * 4096, tag)) timed_out = true;
    STAMP(7);
    float o0 = (__uint_as_float(po[0][0]) + __uint_as_float(po[1][0])) + (__uint_as_float(po[2][0]) + __uint_as_float(po[3][0]));
    float o1 = (__uint_as_float(po[0][2]) + __uint_as_float(po[1][2])) + (__uint_as_float(po[2][2]) + __uint_as_float(po[3][2]));
    o0 += dpp<XOR8>(o0); o1 += dpp<XOR8>(o1);
    o0 = x32_sum(x16_sum(o0)); o1 = x32_sum(x16_sum(o1));
    const unsigned opk = pk_bf16(o0, o1);
#pragma unroll
    for (int j = 0; j < 8; ++j) { const unsigned oj = __builtin_amdgcn_readlane(opk, j); y0 = dot2(wop0[j], oj, y0); y1 = dot2(wop1[j], oj, y1); }
    if (timed_out) y0 = __builtin_nanf("");
    const unsigned yoff = tid < D / 2 ? (unsigned)((2 * tid / CPR) * (256 * CPR) + (2 * tid % CPR)) * 8 : OOB;
    __builtin_amdgcn_raw_buffer_store_b128(u32x4{__float_as_uint(y0), tag, __float_as_uint(y1), tag}, r_ws, yoff, WS_YA + wg * (CPR * 8), AUX_SC1);
    STAMP(8);
    if (wave == 0) lds_arrive(lds, C_HOP1, lane);
  }
  if (wg >= NRED) return;
  lds_wait_ge(lds, C_HOP1, 1u);
  {
    constexpr int NLD = (256 * CPR / 2 + NTHR - 1) / NTHR;
    const float xo = tid < CPR ? bf_lo(a.x[wg * CPR + tid]) : 0.f;
    u32x4 t[NLD];
    unsigned yo[NLD];
#pragma unroll
    for (int k = 0; k < NLD; ++k) yo[k] = tid + NTHR * k < 256 * CPR / 2 ? (unsigned)(tid + NTHR * k) * 16 : OOB;
    if (!gather<NLD, 0>(t, r_ws, yo, WS_YA + wg * (256 * CPR * 8), tag)) timed_out = true;
    STAMP(9);
    float s0 = 0.f, s1 = 0.f;
#pragma unroll
    for (int k = 0; k < NLD; ++k) { s0 += __uint_as_float(t[k][0]); s1 += __uint_as_float(t[k][2]); }
    if (CPR / 2 <= 8) { s0 += dpp<XOR8>(s0); s1 += dpp<XOR8>(s1); }
    if (CPR / 2 <= 4) { s0 += dpp<ROR4>(s0); s1 += dpp<ROR4>(s1); }
    if (CPR / 2 <= 2) { s0 += dpp<XOR2>(s0); s1 += dpp<XOR2>(s1); }
    if (CPR / 2 <= 1) { s0 += dpp<XOR1>(s0); s1 += dpp<XOR1>(s1); }
    s0 = x32_sum(x16_sum(s0)); s1 = x32_sum(x16_sum(s1));
    if (timed_out) s0 = __builtin_nanf("");
    if (lane < CPR / 2) *reinterpret_cast<f32x2*>(lds + L_YA + wave * 64 + lane * 8) = f32x2{s0, s1};
    lds_barrier();
    if (tid < CPR) {
      float r = xo;
#pragma unroll
      for (int w = 0; w < 8; ++w) r += *reinterpret_cast<const float*>(lds + L_YA + w * 64 + tid * 4);
      a.out[wg * CPR + tid] = (unsigned short)(pk_bf16(r, r) & 0xffffu);
    }
    if (wg == 0 && tid == 64) __builtin_amdgcn_raw_buffer_store_b32(tag, r_ws, 0, WS_EPOCH, AUX_SC1);
    STAMP(10);
  }
}

__global__ void __launch_bounds__(NTHR, 2) fused_decode(Args a) {
  if (blockIdx.x < 128) body<2, 0>(a); else body<2, 2>(a);
}

}

extern "C" void kernel_launch(void* const* d_in, const int* in_sizes, int n_in, void* d_out, int out_size, void* d_ws, size_t ws_size,
                              hipStream_t stream) {
  (void)in_sizes; (void)n_in; (void)out_size; (void)ws_size;
  Args a;
  a.x = (const unsigned short*)d_in[0];
  a.k = (const unsigned char*)d_in[1];
  a.v = (const unsigned char*)d_in[2];
  a.wq = (const unsigned char*)d_in[3];
  a.wo = (const unsigned char*)d_in[4];
  a.wg = (const unsigned char*)d_in[6];
  a.wu = (const unsigned char*)d_in[7];
  a.wd = (const unsigned char*)d_in[8];
  a.out = (unsigned short*)d_out;
  a.ws = (unsigned char*)d_ws;
  (void)hipFuncSetAttribute((const void*)fused_decode, hipFuncAttributeMaxDynamicSharedMemorySize, LDS_BYTES);
  hipLaunchKernelGGL(fused_decode, dim3(NWG), dim3(NTHR), LDS_BYTES, stream, a);
}
```

```cpp
#define PROBE 0
#define PROBE_WAVE 0
#include <hip/hip_runtime.h>
#include <hip/hip_bf16.h>
#include <cstdint>

namespace {

typedef unsigned u32x4 __attribute__((ext_vector_type(4)));
typedef unsigned u32x2 __attribute__((ext_vector_type(2)));
typedef float f32x4 __attribute__((ext_vector_type(4)));
typedef float f32x2 __attribute__((ext_vector_type(2)));
typedef __bf16 bf16x8 __attribute__((ext_vector_type(8)));
typedef __bf16 bf16x2 __attribute__((ext_vector_type(2)));
typedef __amdgpu_buffer_rsrc_t rsrc_t;

constexpr int D = 448, SK = 65536, DH = 256, FF = 1792, NQ = 4096;
constexpr int NWG = 256, NTHR = 512;
#define CH_W 7
#define NI 5
#define PRETOUCH 1
#define HK_PRIO 2
#define ST_PRIO 0
#define CP_V 48
#define CP_WAIT 300
#define CP_GAIN 16
#define CP_OFF 30
#define LASTFAST 0
#define WAVEBAL 1
#define Q_FIRST 300
#define Q_SLEEP 0
#define GU_AT 2600
#define CPR 8
constexpr int NRED = D / CPR;
constexpr unsigned TILE = 8192;
constexpr unsigned OOB = 0x40000000u;
constexpr float C2 = 0.0625f * 1.4426950408889634f;
constexpr unsigned SPIN_CAP = 1u << 22;

constexpr unsigned WS_EPOCH = 0;
constexpr unsigned LSTR = 4352;
constexpr unsigned WS_PO = 4096;
constexpr unsigned WS_YA = WS_PO + 8 * 32 * 32 * 16 * 8;
constexpr unsigned WS_GU = WS_YA + 256 * D * 8;
constexpr unsigned WS_QG = WS_GU + 224 * 4 * 128;
constexpr unsigned WS_ML = WS_QG + 128 * LSTR;
constexpr unsigned WS_END = WS_ML + 256 * LSTR;

constexpr int L_RED = 0;
constexpr int L_CTL = 1024;
constexpr int C_PART = L_CTL, C_QRDY = L_CTL + 4, C_TAG = L_CTL + 12, C_LANDED = L_CTL + 16, C_KHELP = L_CTL + 20, C_VDONE = L_CTL + 24, C_FAST = L_CTL + 28,
              C_HRDY = L_CTL + 32, C_HOP1 = L_CTL + 36;
constexpr int L_ML = 1088;
constexpr int L_SC = 1152;
constexpr int L_H = 1184;
constexpr int L_YA = 1216;
constexpr int L_Q = 1792;
constexpr int L_XN = 2816;
constexpr int L_ACC = 4864;
constexpr int L_P = L_ACC + 4 * 2048;
constexpr int LDS_BYTES = 84 * 1024;

constexpr int AUX_NT = 2, AUX_SC1 = 16;
#define AUX_K 2
#define AUX_V 2
#define SB() __builtin_amdgcn_sched_barrier(0)
#define FENCE() asm volatile("" ::: "memory")

__device__ __forceinline__ float bf_lo(unsigned u) { return __builtin_bit_cast(float, u << 16); }
__device__ __forceinline__ float bf_hi(unsigned u) { return __builtin_bit_cast(float, u & 0xffff0000u); }
__device__ __forceinline__ unsigned pk_bf16(float lo, float hi) {
  bf16x2 v = {(__bf16)lo, (__bf16)hi};
  return __builtin_bit_cast(unsigned, v);
}
__device__ __forceinline__ float rnd_bf16(float x) { return (float)(__bf16)x; }
__device__ __forceinline__ float dot2(unsigned a, unsigned b, float acc) {
  return __builtin_amdgcn_fdot2_f32_bf16(__builtin_bit_cast(bf16x2, a), __builtin_bit_cast(bf16x2, b), acc, false);
}
template <int CTRL> __device__ __forceinline__ float dpp(float x) {
  return __builtin_bit_cast(float, __builtin_amdgcn_mov_dpp(__builtin_bit_cast(int, x), CTRL, 0xf, 0xf, true));
}
constexpr int XOR1 = 0xB1, XOR2 = 0x4E, XOR7 = 0x141, XOR15 = 0x140, XOR8 = 0x128, ROR4 = 0x124;
__device__ __forceinline__ float x16_max(float x) {
  auto s = __builtin_amdgcn_permlane16_swap(__float_as_uint(x), __float_as_uint(x), false, false);
  return fmaxf(__uint_as_float(s[0]), __uint_as_float(s[1]));
}
__device__ __forceinline__ float x32_max(float x) {
  auto s = __builtin_amdgcn_permlane32_swap(__float_as_uint(x), __float_as_uint(x), false, false);
  return fmaxf(__uint_as_float(s[0]), __uint_as_float(s[1]));
}
__device__ __forceinline__ float x16_sum(float x) {
  auto s = __builtin_amdgcn_permlane16_swap(__float_as_uint(x), __float_as_uint(x), false, false);
  return __uint_as_float(s[0]) + __uint_as_float(s[1]);
}
__device__ __forceinline__ float x32_sum(float x) {
  auto s = __builtin_amdgcn_permlane32_swap(__float_as_uint(x), __float_as_uint(x), false, false);
  return __uint_as_float(s[0]) + __uint_as_float(s[1]);
}
__device__ __forceinline__ float wave_sum(float x) {
  x += dpp<XOR1>(x); x += dpp<XOR2>(x); x += dpp<XOR7>(x); x += dpp<XOR15>(x);
  return x32_sum(x16_sum(x));
}
__device__ __forceinline__ float parity_sum(float x) {
  x += dpp<XOR2>(x); x += dpp<ROR4>(x); x += dpp<XOR8>(x);
  return x32_sum(x16_sum(x));
}
__device__ __forceinline__ float parity_max(float x) {
  x = fmaxf(x, dpp<XOR2>(x)); x = fmaxf(x, dpp<ROR4>(x)); x = fmaxf(x, dpp<XOR8>(x));
  return x32_max(x16_max(x));
}
__device__ __forceinline__ void lds_barrier() {
  asm volatile("s_waitcnt lgkmcnt(0)" ::: "memory");
  __builtin_amdgcn_s_barrier();
  FENCE();
}
__device__ __forceinline__ rsrc_t mk_rsrc(const void* p, unsigned bytes) {
  return __builtin_amdgcn_make_buffer_rsrc(const_cast<void*>(p), 0, bytes, 0x00020000);
}
__device__ __forceinline__ unsigned lds_ld(unsigned char* lds, int off) {
  return __builtin_amdgcn_readfirstlane(__hip_atomic_load(reinterpret_cast<unsigned*>(lds + off), __ATOMIC_RELAXED, __HIP_MEMORY_SCOPE_WORKGROUP));
}
template <int SLEEP = 1>
__device__ __forceinline__ void lds_wait_ge(unsigned char* lds, int off, unsigned want) {
#pragma nounroll
  for (unsigned spin = 0; spin < SPIN_CAP * 8u; ++spin) {
    if (lds_ld(lds, off) >= want) break;
    __builtin_amdgcn_s_sleep(SLEEP);
  }
  FENCE();
}
__device__ __forceinline__ void lds_arrive(unsigned char* lds, int off, int lane) {
  asm volatile("s_waitcnt lgkmcnt(0)" ::: "memory");
  if (lane == 0) __hip_atomic_fetch_add(reinterpret_cast<unsigned*>(lds + off), 1u, __ATOMIC_RELAXED, __HIP_MEMORY_SCOPE_WORKGROUP);
}
__device__ __forceinline__ unsigned now() { return (unsigned)__builtin_amdgcn_s_memrealtime(); }
template <int SLEEP>
__device__ __forceinline__ void sleep_until(unsigned t0, unsigned ticks) {
#pragma nounroll
  for (unsigned spin = 0; spin < SPIN_CAP && now() - t0 < ticks; ++spin) __builtin_amdgcn_s_sleep(SLEEP);
}
__device__ __forceinline__ void chain_wait(unsigned char* lds, int idx) {
#pragma nounroll
  for (unsigned spin = 0; spin < SPIN_CAP * 8u; ++spin) {
    if ((int)lds_ld(lds, C_LANDED) >= idx + 1 - CH_W || lds_ld(lds, C_QRDY) != 0u) break;
    __builtin_amdgcn_s_sleep(1);
  }
  FENCE();
}
template <int N, int SLEEP>
__device__ __forceinline__ bool gather(u32x4 (&v)[N], rsrc_t rs, const unsigned (&voff)[N], unsigned soff, unsigned tag) {
  bool have[N];
#pragma unroll
  for (int k = 0; k < N; ++k) { have[k] = voff[k] == OOB; v[k] = u32x4{0u, 0u, 0u, 0u}; }
#pragma nounroll
  for (unsigned spin = 0; spin < SPIN_CAP; ++spin) {
    FENCE();
    u32x4 t[N];
#pragma unroll
    for (int k = 0; k < N; ++k) t[k] = __builtin_amdgcn_raw_buffer_load_b128(rs, have[k] ? OOB : voff[k], soff, AUX_SC1);
    bool all = true;
#pragma unroll
    for (int k = 0; k < N; ++k) {
      const bool now = !have[k] && t[k][1] == tag && t[k][3] == tag;
      v[k][0] = now ? t[k][0] : v[k][0]; v[k][2] = now ? t[k][2] : v[k][2];
      have[k] = have[k] || now; all = all && have[k];
    }
    if (__builtin_amdgcn_ballot_w64(!all) == 0) return true;
    if (SLEEP > 0) __builtin_amdgcn_s_sleep(SLEEP);
  }
  return false;
}
#define STAMP(slot) do { if (lane == 0) __hip_atomic_store(reinterpret_cast<unsigned*>(lds + 40960) + wave * 16 + (slot), now(), __ATOMIC_RELAXED, __HIP_MEMORY_SCOPE_WORKGROUP); } while (0)
#define PROG() do {} while (0)
#define PROG_OUT() do {} while (0)
#define PROGL() do {} while (0)

struct Args {
  const unsigned short* x;
  const unsigned char* k;
  const unsigned char* v;
  const unsigned char* wq;
  const unsigned char* wo;
  const unsigned char* wg;
  const unsigned char* wu;
  const unsigned char* wd;
  unsigned short* out;
  unsigned char* ws;
};

__host__ __device__ constexpr int piece(int e, int n) { return n < 2 ? 2 * e + n : 2 * n + e; }
template <int AUX>
__device__ __forceinline__ void issue(u32x4 (&r)[8], rsrc_t rs, unsigned v0, unsigned v1, unsigned soff) {
#pragma unroll
  for (int i = 0; i < 8; ++i) { r[i] = __builtin_amdgcn_raw_buffer_load_b128(rs, ((i & 1) ? v1 : v0) + (i >> 1) * 512, soff, AUX); SB(); }
}
__device__ __forceinline__ f32x4 mfma(u32x4 x, u32x4 y, f32x4 acc) {
  return __builtin_amdgcn_mfma_f32_16x16x32_bf16(__builtin_bit_cast(bf16x8, x), __builtin_bit_cast(bf16x8, y), acc, 0, 0, 0);
}
__device__ __forceinline__ void qk_mfma(const u32x4 (&r)[8], const u32x4 (&qf)[2], f32x4 (&d)[4]) {
#pragma unroll
  for (int j = 0; j < 4; ++j) d[j] = mfma(qf[1], r[2 * j + 1], mfma(qf[0], r[2 * j], f32x4{0.f, 0.f, 0.f, 0.f}));
  SB();
}
__device__ __forceinline__ unsigned short qk_score(const f32x4 (&d)[4], bool b0, bool b1) {
  float v[4];
#pragma unroll
  for (int j = 0; j < 4; ++j) {
    float e0 = d[j][0], e1 = d[j][1], e2 = d[j][2], e3 = d[j][3];
    asm("" : "+v"(e0), "+v"(e2));
    const float lo = b0 ? e1 : e0, hi = b0 ? e3 : e2;
    v[j] = b1 ? hi : lo;
  }
  const float u0 = (b0 ? v[1] : v[0]) + dpp<XOR1>(b0 ? v[0] : v[1]);
  const float u1 = (b0 ? v[3] : v[2]) + dpp<XOR1>(b0 ? v[2] : v[3]);
  const float w = (b1 ? u1 : u0) + dpp<XOR2>(b1 ? u0 : u1);
  return (unsigned short)(pk_bf16(w, w) & 0xffffu);
}
__device__ __forceinline__ void pv_mfma(const u32x4 (&vr)[8], u32x2 pk, int msel, f32x4& acc0, f32x4& acc1) {
#pragma unroll
  for (int q = 0; q < 4; ++q) {
    const u32x4 pa = {msel == 2 * q ? pk[0] : 0u, msel == 2 * q ? pk[1] : 0u, msel == 2 * q + 1 ? pk[0] : 0u, msel == 2 * q + 1 ? pk[1] : 0u};
    const unsigned sel = (q & 1) ? 0x07060302u : 0x05040100u;
    const int d0 = q >> 1, d1 = 2 + (q >> 1);
    const u32x4 b0 = {__builtin_amdgcn_perm(vr[2][d0], vr[0][d0], sel), __builtin_amdgcn_perm(vr[6][d0], vr[4][d0], sel),
                      __builtin_amdgcn_perm(vr[3][d0], vr[1][d0], sel), __builtin_amdgcn_perm(vr[7][d0], vr[5][d0], sel)};
    const u32x4 b1 = {__builtin_amdgcn_perm(vr[2][d1], vr[0][d1], sel), __builtin_amdgcn_perm(vr[6][d1], vr[4][d1], sel),
                      __builtin_amdgcn_perm(vr[3][d1], vr[1][d1], sel), __builtin_amdgcn_perm(vr[7][d1], vr[5][d1], sel)};
    acc0 = mfma(pa, b0, acc0);
    acc1 = mfma(pa, b1, acc1);
  }
  SB();
}
__device__ __forceinline__ void fma8(float (&s)[8], float xv, u32x4 w) {
#pragma unroll
  for (int i = 0; i < 4; ++i) { s[2 * i] = fmaf(xv, bf_lo(w[i]), s[2 * i]); s[2 * i + 1] = fmaf(xv, bf_hi(w[i]), s[2 * i + 1]); }
}
template <bool WHOLE>
__device__ __forceinline__ void load_qf(u32x4 (&qf)[2], const unsigned char* lds, int c, int g) {
#pragma unroll
  for (int e = 0; e < 2; ++e) qf[e] = *reinterpret_cast<const u32x4*>(lds + L_Q + ((c >> 2) & 1) * 512 + ((WHOLE ? 4 * e + g : piece(e, g)) * 4 + (c & 3)) * 16);
}

__global__ void __launch_bounds__(NTHR, 2) fused_decode(Args a) {
  extern __shared__ __attribute__((aligned(16))) unsigned char lds[];
  const int tid = threadIdx.x, lane = tid & 63, wave = __builtin_amdgcn_readfirstlane(tid >> 6);
  const int wg = blockIdx.x, h = wg >> 5, s = wg & 31;
  const int c = lane & 15, g = lane >> 4;
  STAMP(0);
  const rsrc_t r_ws = mk_rsrc(a.ws, WS_END);
  const rsrc_t r_k = mk_rsrc(a.k, 8u * SK * DH * 2);
  const unsigned wg_off = (unsigned)wg * (128 * TILE);
  const unsigned vw0 = (c >> 2) * 2048 + g * 64 + (c & 3) * 16, vw1 = vw0 + 256;
  const bool b0 = (c & 1) != 0, b1 = (c & 2) != 0;
  const int sc_lane = (g & 1) * 32 + c * 2;
  bool timed_out = false;
  unsigned tag;
  unsigned wop0[8], wop1[8];
  float y0 = 0.f, y1 = 0.f;

  if (wave < 4) {
    __builtin_amdgcn_s_setprio(3);
    const unsigned t_start = now();
    const bool qwg = (s & 8) == 0;
    const rsrc_t r_x = mk_rsrc(a.x, D * 2);
    const rsrc_t r_wq = mk_rsrc(a.wq, qwg ? D * NQ * 2 : 0);
    const int qsl = (s & 7) * 2 + (s >> 4);
    const u32x4 xv = __builtin_amdgcn_raw_buffer_load_b128(r_x, lane * 16, 0, 0);
    const unsigned epoch = __builtin_amdgcn_raw_buffer_load_b32(r_ws, 0, WS_EPOCH, AUX_SC1);
    const int qrow = (lane >> 2) + 16 * wave;
    const unsigned wq_off = (unsigned)qrow * (NQ * 2) + (unsigned)(h * 512 + qsl * 32) * 2 + (lane & 3) * 16;
    u32x4 wv[7];
#pragma unroll
    for (int i = 0; i < 7; ++i) wv[i] = __builtin_amdgcn_raw_buffer_load_b128(r_wq, wq_off, i * 64 * NQ * 2, AUX_NT);
    SB();
    if (tid < 16) *reinterpret_cast<unsigned*>(lds + L_CTL + tid * 4) = 0u;
    lds_barrier();

    {
      float xs[8];
#pragma unroll
      for (int i = 0; i < 4; ++i) { xs[2 * i] = bf_lo(xv[i]); xs[2 * i + 1] = bf_hi(xv[i]); }
      float sm = 0.f;
#pragma unroll
      for (int i = 0; i < 8; ++i) sm += xs[i];
      const float mean = wave_sum(sm) * (1.0f / D);
      float sq = 0.f;
#pragma unroll
      for (int i = 0; i < 8; ++i) { const float d = xs[i] - mean; sq = fmaf(d, d, sq); }
      sq = lane < D / 8 ? sq : 0.f;
      const float var = wave_sum(sq) * (1.0f / D);
      const float mean_b = rnd_bf16(mean);
      const float den_b = rnd_bf16(sqrtf(rnd_bf16(rnd_bf16(var) + 1e-5f)));
#pragma unroll
      for (int i = 0; i < 8; ++i) xs[i] = rnd_bf16(rnd_bf16(xs[i] - mean_b) / den_b);
      f32x4* xp = reinterpret_cast<f32x4*>(lds + L_XN + lane * 32);
      xp[0] = f32x4{xs[0], xs[1], xs[2], xs[3]}; xp[1] = f32x4{xs[4], xs[5], xs[6], xs[7]};
      FENCE();
    }
    tag = __builtin_amdgcn_readfirstlane(epoch) + 1u;
    if (qwg) {
      float sacc[8] = {0.f, 0.f, 0.f, 0.f, 0.f, 0.f, 0.f, 0.f};
#pragma unroll
      for (int i = 0; i < 7; ++i) fma8(sacc, *reinterpret_cast<const float*>(lds + L_XN + (qrow + 64 * i) * 4), wv[i]);
#pragma unroll
      for (int i = 0; i < 8; ++i) { float t = sacc[i]; t += dpp<ROR4>(t); t += dpp<XOR8>(t); sacc[i] = x32_sum(x16_sum(t)); }
      if (lane < 4) {
        f32x4* p = reinterpret_cast<f32x4*>(lds + L_RED + wave * 256 + lane * 32);
        p[0] = f32x4{sacc[0], sacc[1], sacc[2], sacc[3]}; p[1] = f32x4{sacc[4], sacc[5], sacc[6], sacc[7]};
      }
      lds_arrive(lds, C_PART, lane);
      if (wave == 0) {
        lds_wait_ge(lds, C_PART, 4u);
        if (lane < 16) {
          float q0 = 0.f, q1 = 0.f;
#pragma unroll
          for (int w = 0; w < 4; ++w) { const f32x2 t = *reinterpret_cast<const f32x2*>(lds + L_RED + w * 256 + lane * 8); q0 += t[0]; q1 += t[1]; }
          __builtin_amdgcn_raw_buffer_store_b64(u32x2{pk_bf16(q0, q1), tag}, r_ws, lane * 8, WS_QG + (h * 16 + qsl) * LSTR, AUX_SC1);
        }
        STAMP(1);
      }
    }
    if (wave == 0) {
      u32x4 qg[2];
      const unsigned qo[2] = {(lane >> 2) * LSTR + (lane & 3) * 32, (lane >> 2) * LSTR + (lane & 3) * 32 + 16};
      sleep_until<1>(t_start, Q_FIRST);
      timed_out = !gather<2, Q_SLEEP>(qg, r_ws, qo, WS_QG + h * 16 * LSTR, tag);
      *reinterpret_cast<u32x4*>(lds + L_Q + lane * 16) = u32x4{qg[0][0], qg[0][2], qg[1][0], qg[1][2]};
      if (lane == 0) *reinterpret_cast<unsigned*>(lds + C_TAG) = tag;
      lds_arrive(lds, C_QRDY, lane);
    }
    if (wave != 0) {
      __builtin_amdgcn_s_setprio(HK_PRIO);
      const int slot = NI + wave - 1;
      u32x4 kt[4][8];
#pragma unroll
      for (int i = 0; i < 4; ++i) { chain_wait(lds, 4 * slot + i); issue<AUX_K>(kt[i], r_k, vw0, vw1, wg_off + (4 * slot + i) * TILE); }
      asm volatile("s_waitcnt vmcnt(24)" ::: "memory"); lds_arrive(lds, C_LANDED, lane); PROGL();
      asm volatile("s_waitcnt vmcnt(16)" ::: "memory"); lds_arrive(lds, C_LANDED, lane); PROGL();
      asm volatile("s_waitcnt vmcnt(8)" ::: "memory"); lds_arrive(lds, C_LANDED, lane); PROGL();
      asm volatile("s_waitcnt vmcnt(0)" ::: "memory"); lds_arrive(lds, C_LANDED, lane); PROGL();
      lds_wait_ge(lds, C_QRDY, 1u);
      u32x4 qf[2];
      load_qf<true>(qf, lds, c, g);
#pragma unroll
      for (int i = 0; i < 4; ++i) {
        f32x4 d[4];
        qk_mfma(kt[i], qf, d);
        *reinterpret_cast<unsigned short*>(lds + L_P + i * 2048 + slot * 64 + sc_lane) = qk_score(d, b0, b1);
        PROG();
      }
      lds_arrive(lds, C_KHELP, lane);
      __builtin_amdgcn_s_setprio(3);
    }
    STAMP(3);
    const int fb = h * 16 + (s & 7) + ((s >> 4) << 3);
    if (!qwg && fb < 112) {
      const rsrc_t r_gu = mk_rsrc((wave & 1) ? a.wu : a.wg, D * FF * 2);
      const int frow = (fb / 28) * 112 + (wave >> 1) * 56 + (lane >> 3);
      const unsigned goff = (unsigned)frow * (FF * 2) + (fb % 28) * 128 + (lane & 7) * 16;
#pragma unroll
      for (int i = 0; i < 7; ++i) wv[i] = __builtin_amdgcn_raw_buffer_load_b128(r_gu, goff, i * 8 * FF * 2, AUX_NT);
      float sg[8] = {0.f, 0.f, 0.f, 0.f, 0.f, 0.f, 0.f, 0.f};
#pragma unroll
      for (int i = 0; i < 7; ++i) fma8(sg, *reinterpret_cast<const float*>(lds + L_XN + (frow + 8 * i) * 4), wv[i]);
#pragma unroll
      for (int i = 0; i < 8; ++i) { float t = sg[i]; t += dpp<XOR8>(t); sg[i] = x32_sum(x16_sum(t)); }
      if (lane < 8) {
        f32x4* p = reinterpret_cast<f32x4*>(lds + L_RED + wave * 256 + lane * 32);
        p[0] = f32x4{sg[0], sg[1], sg[2], sg[3]}; p[1] = f32x4{sg[4], sg[5], sg[6], sg[7]};
      }
      lds_arrive(lds, C_PART, lane);
      if (wave == 1) {
        lds_wait_ge(lds, C_PART, 4u);
        const int o = ((lane >> 2) & 1) * 256 + (lane >> 3) * 32 + (lane & 3) * 8;
        const f32x2 t0 = *reinterpret_cast<const f32x2*>(lds + L_RED + o), t1 = *reinterpret_cast<const f32x2*>(lds + L_RED + 512 + o);
        __builtin_amdgcn_raw_buffer_store_b128(u32x4{__float_as_uint(t0[0] + t1[0]), tag, __float_as_uint(t0[1] + t1[1]), tag}, r_ws,
                                               (lane >> 3) * 512 + (lane & 7) * 16, WS_GU + (fb % 28) * 4096 + (fb / 28) * 128, AUX_SC1);
      }
    }
    {
      const rsrc_t r_wo = mk_rsrc(a.wo, NQ * D * 2);
      const unsigned coff = tid < D / 2 ? tid * 4 : OOB;
      const bool ffn = wg < FF / 8;
      const rsrc_t r_d = mk_rsrc(a.wd, ffn ? FF * D * 2 : 0);
      unsigned draw[8], wraw[16];
#pragma unroll
      for (int j = 0; j < 8; ++j) draw[j] = __builtin_amdgcn_raw_buffer_load_b32(r_d, coff, (wg * 8 + j) * (D * 2), AUX_NT);
#pragma unroll
      for (int j = 0; j < 16; ++j) wraw[j] = __builtin_amdgcn_raw_buffer_load_b32(r_wo, coff, (wg * 16 + j) * (D * 2), AUX_NT);
#pragma unroll
      for (int j = 0; j < 8; ++j) { wop0[j] = __builtin_amdgcn_perm(wraw[2 * j + 1], wraw[2 * j], 0x05040100u); wop1[j] = __builtin_amdgcn_perm(wraw[2 * j + 1], wraw[2 * j], 0x07060302u); }
      if (wave == 3) {
        sleep_until<32>(t_start, GU_AT);
        u32x4 gu[1];
        const unsigned go[1] = {(ffn && lane < 32) ? (unsigned)lane * 16 : OOB};
        if (!gather<1, 64>(gu, r_ws, go, WS_GU + wg * 512, tag)) timed_out = true;
        float g0 = __uint_as_float(gu[0][0]), g1 = __uint_as_float(gu[0][2]);
        g0 += dpp<XOR8>(g0); g1 += dpp<XOR8>(g1);
        g0 = rnd_bf16(x16_sum(g0)); g1 = rnd_bf16(x16_sum(g1));
        const float u0 = dpp<ROR4>(g0), u1 = dpp<ROR4>(g1);
        const float h0 = rnd_bf16((g0 > 0.f ? g0 : g0 * 0.f) * u0), h1 = rnd_bf16((g1 > 0.f ? g1 : g1 * 0.f) * u1);
        if (lane < 4) *reinterpret_cast<f32x2*>(lds + L_H + lane * 8) = f32x2{h0, h1};
        lds_arrive(lds, C_HRDY, lane);
      }
      lds_wait_ge<32>(lds, C_HRDY, 1u);
#pragma unroll
      for (int j = 0; j < 8; ++j) {
        const float hv = *reinterpret_cast<const float*>(lds + L_H + j * 4);
        y0 = fmaf(hv, bf_lo(draw[j]), y0); y1 = fmaf(hv, bf_hi(draw[j]), y1);
      }
      asm volatile("" : "+v"(y0), "+v"(y1), "+v"(wop0[0]), "+v"(wop0[1]), "+v"(wop0[2]), "+v"(wop0[3]), "+v"(wop0[4]), "+v"(wop0[5]), "+v"(wop0[6]), "+v"(wop0[7]),
                        "+v"(wop1[0]), "+v"(wop1[1]), "+v"(wop1[2]), "+v"(wop1[3]), "+v"(wop1[4]), "+v"(wop1[5]), "+v"(wop1[6]), "+v"(wop1[7]));
    }
    STAMP(11);
    if (wave == 2) {
      const rsrc_t r_v = mk_rsrc(a.v, 8u * SK * DH * 2), r_o = mk_rsrc(a.out, D * 2);
      const unsigned tv = __builtin_amdgcn_raw_buffer_load_b32(r_v, lane < 16 ? lane * 65536 : OOB, wg_off, AUX_NT);
      const unsigned to = __builtin_amdgcn_raw_buffer_load_b32(r_o, lane < 1 ? 0 : OOB, 0, AUX_NT);
      asm volatile("" :: "v"(tv), "v"(to));
    }
    if (wave == 1) {
      lds_wait_ge<8>(lds, C_VDONE, 0x10000u * 4u + CP_V);
      const unsigned t_cp = now();
      const int hd = lane & 1;
      float mw[4], lw[4];
#pragma unroll
      for (int w = 0; w < 4; ++w) { const f32x2 t = *reinterpret_cast<const f32x2*>(lds + L_ML + w * 16 + hd * 8); mw[w] = t[0]; lw[w] = t[1]; }
      const float M = fmaxf(fmaxf(mw[0], mw[1]), fmaxf(mw[2], mw[3]));
      float L = 0.f;
#pragma unroll
      for (int w = 0; w < 4; ++w) L = fmaf(__builtin_amdgcn_exp2f((mw[w] - M) * C2), lw[w], L);
      __builtin_amdgcn_raw_buffer_store_b128(u32x4{(lane & 2) ? t_cp : __float_as_uint(M), tag, (lane & 2) ? t_cp : __float_as_uint(L), tag}, r_ws,
                                             lane < 8 ? lane * 16 : OOB, WS_ML + wg * LSTR, AUX_SC1);
      sleep_until<16>(t_cp, CP_WAIT);
      u32x4 ml[2];
      const unsigned mo[2] = {(lane >> 2) * LSTR + (lane & 3) * 16, (16 + (lane >> 2)) * LSTR + (lane & 3) * 16};
      if (!gather<2, 32>(ml, r_ws, mo, WS_ML + h * 32 * LSTR, tag)) timed_out = true;
      const float Ma = __uint_as_float(ml[0][0]), Mb = __uint_as_float(ml[1][0]);
      float Mg = fmaxf(Ma, Mb);
      Mg = fmaxf(Mg, dpp<ROR4>(Mg)); Mg = fmaxf(Mg, dpp<XOR8>(Mg)); Mg = x32_max(x16_max(Mg));
      float Lg = __builtin_amdgcn_exp2f((Ma - Mg) * C2) * __uint_as_float(ml[0][2]) + __builtin_amdgcn_exp2f((Mb - Mg) * C2) * __uint_as_float(ml[1][2]);
      Lg = (lane & 2) ? (float)(int)(t_cp - ml[0][0]) + (float)(int)(t_cp - ml[1][0]) : Lg;
      Lg += dpp<ROR4>(Lg); Lg += dpp<XOR8>(Lg); Lg = x32_sum(x16_sum(Lg));
      if (lane < 2) {
#pragma unroll
        for (int w = 0; w < 4; ++w) *reinterpret_cast<float*>(lds + L_SC + w * 8 + lane * 4) = timed_out ? __builtin_nanf("") : __builtin_amdgcn_exp2f((mw[w] - Mg) * C2) / Lg;
      }
      if (lane == 2) {
        const float nf = fminf((Lg * (1.0f / 32) + (float)CP_OFF) * (0.01f * CP_GAIN), 128.f);
        if (nf >= 1.f) *reinterpret_cast<unsigned*>(lds + C_FAST) = (*reinterpret_cast<const unsigned*>(lds + C_VDONE) & 0xffffu) + 8u + (unsigned)nf;
      }
      STAMP(14);
    }
  } else {
    const int w = wave - 4;
    const rsrc_t r_v = mk_rsrc(a.v, 8u * SK * DH * 2);
    const unsigned vv0 = g * 2048 + piece(0, c >> 2) * 64 + (c & 3) * 16, vv1 = g * 2048 + piece(1, c >> 2) * 64 + (c & 3) * 16;
    const unsigned vk0 = (c >> 2) * 2048 + piece(0, g) * 64 + (c & 3) * 16, vk1 = (c >> 2) * 2048 + piece(1, g) * 64 + (c & 3) * 16;
    const unsigned t0 = wg_off + w * TILE;
    u32x4 ra[8], rb[8];
    __builtin_amdgcn_s_setprio(ST_PRIO);
    u32x4 rx[NI - 2][8];
    if (s & 8) issue<AUX_K>(ra, r_k, vw0, vw1, t0);
    lds_barrier();
    if (!(s & 8)) issue<AUX_K>(ra, r_k, vw0, vw1, t0);
#define CHAIN_STEP(R, SLOT) do { \
      constexpr bool first_ = CH_W < 5;     \
      if (first_ && lds_ld(lds, C_QRDY) == 0u) { asm volatile("s_waitcnt vmcnt(0)" ::: "memory"); lds_arrive(lds, C_LANDED, lane); PROGL(); } \
      chain_wait(lds, 4 * (SLOT) + w); \
      issue<AUX_K>(R, r_k, vw0, vw1, t0 + 4 * (SLOT) * TILE); \
      if (!first_ && lds_ld(lds, C_QRDY) == 0u) { asm volatile("s_waitcnt vmcnt(8)" ::: "memory"); lds_arrive(lds, C_LANDED, lane); PROGL(); } } while (0)
    CHAIN_STEP(rb, 1);
#pragma unroll
    for (int i = 2; i < NI; ++i) CHAIN_STEP(rx[i - 2], i);
#undef CHAIN_STEP
    if (lds_ld(lds, C_QRDY) == 0u) { asm volatile("s_waitcnt vmcnt(0)" ::: "memory"); lds_arrive(lds, C_LANDED, lane); PROGL(); }
    lds_wait_ge(lds, C_QRDY, 1u);
    tag = lds_ld(lds, C_TAG);
    STAMP(2);
    u32x4 qf[2];
    load_qf<true>(qf, lds, c, g);
    unsigned char* const pl = lds + L_P + w * 2048;
    {
      f32x4 d[4];
      unsigned short* sw = reinterpret_cast<unsigned short*>(pl + sc_lane);
      unsigned toff = t0 + 4 * (NI + 3) * TILE;
#define K_STEP(R, ISSUE_NEXT, SLOT) do { qk_mfma(R, qf, d); PROG(); ISSUE_NEXT; sw[(SLOT) * 32] = qk_score(d, b0, b1); } while (0)
      K_STEP(ra, issue<AUX_K>(ra, r_k, vk0, vk1, toff), 0);
      K_STEP(rb, issue<AUX_K>(rb, r_k, vk0, vk1, toff + 4 * TILE), 1);
#pragma unroll
      for (int i = 2; i < NI; ++i) K_STEP(rx[i - 2], (void)0, i);
      load_qf<false>(qf, lds, c, g);
      sw += (NI + 3) * 32;
      for (int it = 1; it < (29 - NI) / 2; ++it) {
        toff += 8 * TILE;
        K_STEP(ra, issue<AUX_K>(ra, r_k, vk0, vk1, toff), 0);
        K_STEP(rb, issue<AUX_K>(rb, r_k, vk0, vk1, toff + 4 * TILE), 1);
        sw += 2 * 32;
      }
      K_STEP(ra, issue<AUX_V>(ra, r_v, vv0, vv1, t0), 0);
      K_STEP(rb, issue<AUX_V>(rb, r_v, vv0, vv1, t0 + 4 * TILE), 1);
#undef K_STEP
    }
    STAMP(12);
    lds_wait_ge(lds, C_KHELP, 3u);
    {
      u32x4* pp = reinterpret_cast<u32x4*>(pl + lane * 32);
      u32x4 t[2] = {pp[0], pp[1]};
      float mx = bf_lo(t[0][0]);
#pragma unroll
      for (int j = 0; j < 2; ++j)
#pragma unroll
        for (int i = 0; i < 4; ++i) mx = fmaxf(mx, fmaxf(bf_lo(t[j][i]), bf_hi(t[j][i])));
      mx = parity_max(mx);
      const float mc = mx * C2;
      float lp = 0.f;
#pragma unroll
      for (int j = 0; j < 2; ++j) {
#pragma unroll
        for (int i = 0; i < 4; ++i) {
          const float p0 = __builtin_amdgcn_exp2f(fmaf(bf_lo(t[j][i]), C2, -mc)), p1 = __builtin_amdgcn_exp2f(fmaf(bf_hi(t[j][i]), C2, -mc));
          lp += p0 + p1; t[j][i] = pk_bf16(p0, p1);
        }
        pp[j] = t[j];
      }
      lp = parity_sum(lp);
      if (lane < 2) *reinterpret_cast<f32x2*>(lds + L_ML + w * 16 + lane * 8) = f32x2{mx, lp};
      asm volatile("s_waitcnt lgkmcnt(0)" ::: "memory");
      if (lane == 0) __hip_atomic_fetch_add(reinterpret_cast<unsigned*>(lds + C_VDONE), 0x10000u, __ATOMIC_RELAXED, __HIP_MEMORY_SCOPE_WORKGROUP);
    }
    f32x4 acc0 = {0.f, 0.f, 0.f, 0.f}, acc1 = {0.f, 0.f, 0.f, 0.f};
    {
      const int msel = c >> 1;
      const unsigned char* pr = pl + (c & 1) * 32 + g * 8;
      const unsigned vf0 = g * 2048 + c * 16;
      float* const rec = reinterpret_cast<float*>(lds + L_ACC + w * 2048) + 8 * (c & 3) + g;
      auto put = [&](bool whole) {
        const int e0 = (whole ? c >> 2 : piece(0, c >> 2)) * 32, e1 = (whole ? 4 + (c >> 2) : piece(1, c >> 2)) * 32;
#pragma unroll
        for (int k = 0; k < 4; ++k) { float* r = rec + (k & 1) * 256 + ((k >> 1) ? e1 : e0); r[0] = acc0[k]; r[4] = acc1[k]; }
      };
      auto get = [&](bool whole) {
        const int e0 = (whole ? c >> 2 : piece(0, c >> 2)) * 32, e1 = (whole ? 4 + (c >> 2) : piece(1, c >> 2)) * 32;
#pragma unroll
        for (int k = 0; k < 4; ++k) { const float* r = rec + (k & 1) * 256 + ((k >> 1) ? e1 : e0); acc0[k] = r[0]; acc1[k] = r[4]; }
      };
      unsigned toff = t0 + 8 * TILE;
      bool cur = false;
#define V_STEP(R, ISSUE_NEXT) do { const u32x2 pk_ = *reinterpret_cast<const u32x2*>(pr); pr += 64; pv_mfma(R, pk_, msel, acc0, acc1); PROG(); ISSUE_NEXT; } while (0)
      for (int it = 0; it < 15; ++it) {
        const u32x2 cw = *reinterpret_cast<const u32x2*>(lds + C_VDONE);
        const bool nxt = (unsigned)(8 * it + 8 + w) < __builtin_amdgcn_readfirstlane(cw[1]) || 8 * it + 8 >= 128 - LASTFAST;
        if ((unsigned)(8 * it + 4) < (__builtin_amdgcn_readfirstlane(cw[0]) & 0xffffu)) __builtin_amdgcn_s_setprio(1); else __builtin_amdgcn_s_setprio(0);
        const unsigned n0 = nxt ? vf0 : vv0, n1 = nxt ? vf0 + 256 : vv1;
        V_STEP(ra, issue<AUX_V>(ra, r_v, n0, n1, toff));
        V_STEP(rb, issue<AUX_V>(rb, r_v, n0, n1, toff + 4 * TILE));
        if (lane == 0) __hip_atomic_fetch_add(reinterpret_cast<unsigned*>(lds + C_VDONE), 2u, __ATOMIC_RELAXED, __HIP_MEMORY_SCOPE_WORKGROUP);
        if (nxt != cur) { put(cur); FENCE(); get(nxt); cur = nxt; }
        FENCE();
        toff += 8 * TILE;
      }
      V_STEP(ra, (void)0);
      V_STEP(rb, (void)0);
#undef V_STEP
      STAMP(3);
      put(cur);
    }
#pragma unroll
    for (int j = 0; j < 8; ++j) { wop0[j] = 0u; wop1[j] = 0u; }
  }
  PROG_OUT();
  STAMP(4);
  lds_barrier();
  STAMP(5);
  {
    float O = 0.f;
#pragma unroll
    for (int w = 0; w < 4; ++w)
      O = fmaf(*reinterpret_cast<const float*>(lds + L_SC + w * 8 + (tid >> 8) * 4), *reinterpret_cast<const float*>(lds + L_ACC + w * 2048 + tid * 4), O);
    __builtin_amdgcn_raw_buffer_store_b64(u32x2{__float_as_uint(O), tag}, r_ws, (tid >> 4) * 4096 + (tid & 15) * 8, WS_PO + h * (32 * 4096) + s * 128, AUX_SC1);
  }
  STAMP(6);
  if (wave < 4) {
    u32x4 po[4];
    const unsigned oo[4] = {(unsigned)lane * 16, (unsigned)lane * 16 + 1024, (unsigned)lane * 16 + 2048, (unsigned)lane * 16 + 3072};
    if (!gather<4, 0>(po, r_ws, oo, WS_PO + wg * 4096, tag)) timed_out = true;
    STAMP(7);
    float o0 = (__uint_as_float(po[0][0]) + __uint_as_float(po[1][0])) + (__uint_as_float(po[2][0]) + __uint_as_float(po[3][0]));
    float o1 = (__uint_as_float(po[0][2]) + __uint_as_float(po[1][2])) + (__uint_as_float(po[2][2]) + __uint_as_float(po[3][2]));
    o0 += dpp<XOR8>(o0); o1 += dpp<XOR8>(o1);
    o0 = x32_sum(x16_sum(o0)); o1 = x32_sum(x16_sum(o1));
    const unsigned opk = pk_bf16(o0, o1);
#pragma unroll
    for (int j = 0; j < 8; ++j) { const unsigned oj = __builtin_amdgcn_readlane(opk, j); y0 = dot2(wop0[j], oj, y0); y1 = dot2(wop1[j], oj, y1); }
    if (timed_out) y0 = __builtin_nanf("");
    const unsigned yoff = tid < D / 2 ? (unsigned)((2 * tid / CPR) * (256 * CPR) + (2 * tid % CPR)) * 8 : OOB;
    __builtin_amdgcn_raw_buffer_store_b128(u32x4{__float_as_uint(y0), tag, __float_as_uint(y1), tag}, r_ws, yoff, WS_YA + wg * (CPR * 8), AUX_SC1);
    STAMP(8);
    if (wave == 0) lds_arrive(lds, C_HOP1, lane);
  }
  if (wg >= NRED) return;
  lds_wait_ge(lds, C_HOP1, 1u);
  {
    constexpr int NLD = (256 * CPR / 2 + NTHR - 1) / NTHR;
    const float xo = tid < CPR ? bf_lo(a.x[wg * CPR + tid]) : 0.f;
    u32x4 t[NLD];
    unsigned yo[NLD];
#pragma unroll
    for (int k = 0; k < NLD; ++k) yo[k] = tid + NTHR * k < 256 * CPR / 2 ? (unsigned)(tid + NTHR * k) * 16 : OOB;
    if (!gather<NLD, 0>(t, r_ws, yo, WS_YA + wg * (256 * CPR * 8), tag)) timed_out = true;
    STAMP(9);
    float s0 = 0.f, s1 = 0.f;
#pragma unroll
    for (int k = 0; k < NLD; ++k) { s0 += __uint_as_float(t[k][0]); s1 += __uint_as_float(t[k][2]); }
    if (CPR / 2 <= 8) { s0 += dpp<XOR8>(s0); s1 += dpp<XOR8>(s1); }
    if (CPR / 2 <= 4) { s0 += dpp<ROR4>(s0); s1 += dpp<ROR4>(s1); }
    if (CPR / 2 <= 2) { s0 += dpp<XOR2>(s0); s1 += dpp<XOR2>(s1); }
    if (CPR / 2 <= 1) { s0 += dpp<XOR1>(s0); s1 += dpp<XOR1>(s1); }
    s0 = x32_sum(x16_sum(s0)); s1 = x32_sum(x16_sum(s1));
    if (timed_out) s0 = __builtin_nanf("");
    if (lane < CPR / 2) *reinterpret_cast<f32x2*>(lds + L_YA + wave * 64 + lane * 8) = f32x2{s0, s1};
    lds_barrier();
    if (tid < CPR) {
      float r = xo;
#pragma unroll
      for (int w = 0; w < 8; ++w) r += *reinterpret_cast<const float*>(lds + L_YA + w * 64 + tid * 4);
      a.out[wg * CPR + tid] = (unsigned short)(pk_bf16(r, r) & 0xffffu);
    }
    if (wg == 0 && tid == 64) __builtin_amdgcn_raw_buffer_store_b32(tag, r_ws, 0, WS_EPOCH, AUX_SC1);
    STAMP(10);
    if (wg == 0 && wave == 0) {
      const unsigned ts_ = lds_ld(lds, 40960), tx_ = lds_ld(lds, 40960 + (PROBE_WAVE * 16 + PROBE) * 4);
      sleep_until<1>(ts_, 10000u + (tx_ - ts_));
    }
  }
}

}

extern "C" void kernel_launch(void* const* d_in, const int* in_sizes, int n_in, void* d_out, int out_size, void* d_ws, size_t ws_size,
                              hipStream_t stream) {
  (void)in_sizes; (void)n_in; (void)out_size; (void)ws_size;
  Args a;
  a.x = (const unsigned short*)d_in[0];
  a.k = (const unsigned char*)d_in[1];
  a.v = (const unsigned char*)d_in[2];
  a.wq = (const unsigned char*)d_in[3];
  a.wo = (const unsigned char*)d_in[4];
  a.wg = (const unsigned char*)d_in[6];
  a.wu = (const unsigned char*)d_in[7];
  a.wd = (const unsigned char*)d_in[8];
  a.out = (unsigned short*)d_out;
  a.ws = (unsigned char*)d_ws;
  (void)hipFuncSetAttribute((const void*)fused_decode, hipFuncAttributeMaxDynamicSharedMemorySize, LDS_BYTES);
  hipLaunchKernelGGL(fused_decode, dim3(NWG), dim3(NTHR), LDS_BYTES, stream, a);
}
```

```cpp
#define AUX_OUT 17
#define LDS_KB 24
#include <hip/hip_runtime.h>
#include <hip/hip_bf16.h>
#include <cstdint>

namespace {

typedef unsigned u32x4 __attribute__((ext_vector_type(4)));
typedef unsigned u32x2 __attribute__((ext_vector_type(2)));
typedef float f32x4 __attribute__((ext_vector_type(4)));
typedef float f32x2 __attribute__((ext_vector_type(2)));
typedef __bf16 bf16x8 __attribute__((ext_vector_type(8)));
typedef __bf16 bf16x2 __attribute__((ext_vector_type(2)));
typedef __amdgpu_buffer_rsrc_t rsrc_t;

constexpr int D = 448, SK = 65536, DH = 256, FF = 1792, NQ = 4096;
constexpr int NWG = 256, NTHR = 512;
#define CH_W 7
#define NI 5
#define PRETOUCH 1
#define HK_PRIO 2
#define ST_PRIO 0
#define CP_V 48
#define CP_WAIT 300
#define CP_GAIN 16
#define CP_OFF 30
#define LASTFAST 0
#define WAVEBAL 1
#define Q_FIRST 300
#define Q_SLEEP 0
#define GU_AT 2600
#define CPR 8
constexpr int NRED = D / CPR;
constexpr unsigned TILE = 8192;
constexpr unsigned OOB = 0x40000000u;
constexpr float C2 = 0.0625f * 1.4426950408889634f;
constexpr unsigned SPIN_CAP = 1u << 22;

constexpr unsigned WS_EPOCH = 0;
constexpr unsigned LSTR = 4352;
constexpr unsigned WS_PO = 4096;
constexpr unsigned WS_YA = WS_PO + 8 * 32 * 32 * 16 * 8;
constexpr unsigned WS_GU = WS_YA + 256 * D * 8;
constexpr unsigned WS_QG = WS_GU + 224 * 4 * 128;
constexpr unsigned WS_ML = WS_QG + 128 * LSTR;
constexpr unsigned WS_END = WS_ML + 256 * LSTR;

constexpr int L_RED = 0;
constexpr int L_CTL = 1024;
constexpr int C_PART = L_CTL, C_QRDY = L_CTL + 4, C_TAG = L_CTL + 12, C_LANDED = L_CTL + 16, C_KHELP = L_CTL + 20, C_VDONE = L_CTL + 24, C_FAST = L_CTL + 28,
              C_HRDY = L_CTL + 32, C_HOP1 = L_CTL + 36;
constexpr int L_ML = 1088;
constexpr int L_SC = 1152;
constexpr int L_H = 1184;
constexpr int L_YA = 1216;
constexpr int L_Q = 1792;
constexpr int L_XN = 2816;
constexpr int L_ACC = 4864;
constexpr int L_P = L_ACC + 4 * 2048;
constexpr int LDS_BYTES = LDS_KB * 1024;

constexpr int AUX_NT = 2, AUX_SC1 = 16;
#define AUX_K 2
#define AUX_V 2
#define SB() __builtin_amdgcn_sched_barrier(0)
#define FENCE() asm volatile("" ::: "memory")

__device__ __forceinline__ float bf_lo(unsigned u) { return __builtin_bit_cast(float, u << 16); }
__device__ __forceinline__ float bf_hi(unsigned u) { return __builtin_bit_cast(float, u & 0xffff0000u); }
__device__ __forceinline__ unsigned pk_bf16(float lo, float hi) {
  bf16x2 v = {(__bf16)lo, (__bf16)hi};
  return __builtin_bit_cast(unsigned, v);
}
__device__ __forceinline__ float rnd_bf16(float x) { return (float)(__bf16)x; }
__device__ __forceinline__ float dot2(unsigned a, unsigned b, float acc) {
  return __builtin_amdgcn_fdot2_f32_bf16(__builtin_bit_cast(bf16x2, a), __builtin_bit_cast(bf16x2, b), acc, false);
}
template <int CTRL> __device__ __forceinline__ float dpp(float x) {
  return __builtin_bit_cast(float, __builtin_amdgcn_mov_dpp(__builtin_bit_cast(int, x), CTRL, 0xf, 0xf, true));
}
constexpr int XOR1 = 0xB1, XOR2 = 0x4E, XOR7 = 0x141, XOR15 = 0x140, XOR8 = 0x128, ROR4 = 0x124;
__device__ __forceinline__ float x16_max(float x) {
  auto s = __builtin_amdgcn_permlane16_swap(__float_as_uint(x), __float_as_uint(x), false, false);
  return fmaxf(__uint_as_float(s[0]), __uint_as_float(s[1]));
}
__device__ __forceinline__ float x32_max(float x) {
  auto s = __builtin_amdgcn_permlane32_swap(__float_as_uint(x), __float_as_uint(x), false, false);
  return fmaxf(__uint_as_float(s[0]), __uint_as_float(s[1]));
}
__device__ __forceinline__ float x16_sum(float x) {
  auto s = __builtin_amdgcn_permlane16_swap(__float_as_uint(x), __float_as_uint(x), false, false);
  return __uint_as_float(s[0]) + __uint_as_float(s[1]);
}
__device__ __forceinline__ float x32_sum(float x) {
  auto s = __builtin_amdgcn_permlane32_swap(__float_as_uint(x), __float_as_uint(x), false, false);
  return __uint_as_float(s[0]) + __uint_as_float(s[1]);
}
__device__ __forceinline__ float wave_sum(float x) {
  x += dpp<XOR1>(x); x += dpp<XOR2>(x); x += dpp<XOR7>(x); x += dpp<XOR15>(x);
  return x32_sum(x16_sum(x));
}
__device__ __forceinline__ float parity_sum(float x) {
  x += dpp<XOR2>(x); x += dpp<ROR4>(x); x += dpp<XOR8>(x);
  return x32_sum(x16_sum(x));
}
__device__ __forceinline__ float parity_max(float x) {
  x = fmaxf(x, dpp<XOR2>(x)); x = fmaxf(x, dpp<ROR4>(x)); x = fmaxf(x, dpp<XOR8>(x));
  return x32_max(x16_max(x));
}
__device__ __forceinline__ void lds_barrier() {
  asm volatile("s_waitcnt lgkmcnt(0)" ::: "memory");
  __builtin_amdgcn_s_barrier();
  FENCE();
}
__device__ __forceinline__ rsrc_t mk_rsrc(const void* p, unsigned bytes) {
  return __builtin_amdgcn_make_buffer_rsrc(const_cast<void*>(p), 0, bytes, 0x00020000);
}
__device__ __forceinline__ unsigned lds_ld(unsigned char* lds, int off) {
  return __builtin_amdgcn_readfirstlane(__hip_atomic_load(reinterpret_cast<unsigned*>(lds + off), __ATOMIC_RELAXED, __HIP_MEMORY_SCOPE_WORKGROUP));
}
template <int SLEEP = 1>
__device__ __forceinline__ void lds_wait_ge(unsigned char* lds, int off, unsigned want) {
#pragma nounroll
  for (unsigned spin = 0; spin < SPIN_CAP * 8u; ++spin) {
    if (lds_ld(lds, off) >= want) break;
    __builtin_amdgcn_s_sleep(SLEEP);
  }
  FENCE();
}
__device__ __forceinline__ void lds_arrive(unsigned char* lds, int off, int lane) {
  asm volatile("s_waitcnt lgkmcnt(0)" ::: "memory");
  if (lane == 0) __hip_atomic_fetch_add(reinterpret_cast<unsigned*>(lds + off), 1u, __ATOMIC_RELAXED, __HIP_MEMORY_SCOPE_WORKGROUP);
}
__device__ __forceinline__ unsigned now() { return (unsigned)__builtin_amdgcn_s_memrealtime(); }
template <int SLEEP>
__device__ __forceinline__ void sleep_until(unsigned t0, unsigned ticks) {
#pragma nounroll
  for (unsigned spin = 0; spin < SPIN_CAP && now() - t0 < ticks; ++spin) __builtin_amdgcn_s_sleep(SLEEP);
}
__device__ __forceinline__ void chain_wait(unsigned char* lds, int idx) {
#pragma nounroll
  for (unsigned spin = 0; spin < SPIN_CAP * 8u; ++spin) {
    if ((int)lds_ld(lds, C_LANDED) >= idx + 1 - CH_W || lds_ld(lds, C_QRDY) != 0u) break;
    __builtin_amdgcn_s_sleep(1);
  }
  FENCE();
}
template <int N, int SLEEP>
__device__ __forceinline__ bool gather(u32x4 (&v)[N], rsrc_t rs, const unsigned (&voff)[N], unsigned soff, unsigned tag) {
  bool have[N];
#pragma unroll
  for (int k = 0; k < N; ++k) { have[k] = voff[k] == OOB; v[k] = u32x4{0u, 0u, 0u, 0u}; }
#pragma nounroll
  for (unsigned spin = 0; spin < SPIN_CAP; ++spin) {
    FENCE();
    u32x4 t[N];
#pragma unroll
    for (int k = 0; k < N; ++k) t[k] = __builtin_amdgcn_raw_buffer_load_b128(rs, have[k] ? OOB : voff[k], soff, AUX_SC1);
    bool all = true;
#pragma unroll
    for (int k = 0; k < N; ++k) {
      const bool now = !have[k] && t[k][1] == tag && t[k][3] == tag;
      v[k][0] = now ? t[k][0] : v[k][0]; v[k][2] = now ? t[k][2] : v[k][2];
      have[k] = have[k] || now; all = all && have[k];
    }
    if (__builtin_amdgcn_ballot_w64(!all) == 0) return true;
    if (SLEEP > 0) __builtin_amdgcn_s_sleep(SLEEP);
  }
  return false;
}
#define STAMP(slot) do {} while (0)
#define PROG() do {} while (0)
#define PROG_OUT() do {} while (0)
#define PROGL() do {} while (0)

struct Args {
  const unsigned short* x;
  const unsigned char* k;
  const unsigned char* v;
  const unsigned char* wq;
  const unsigned char* wo;
  const unsigned char* wg;
  const unsigned char* wu;
  const unsigned char* wd;
  unsigned short* out;
  unsigned char* ws;
};

__host__ __device__ constexpr int piece(int e, int n) { return n < 2 ? 2 * e + n : 2 * n + e; }
template <int AUX>
__device__ __forceinline__ void issue(u32x4 (&r)[8], rsrc_t rs, unsigned v0, unsigned v1, unsigned soff) {
#pragma unroll
  for (int i = 0; i < 8; ++i) { r[i] = __builtin_amdgcn_raw_buffer_load_b128(rs, ((i & 1) ? v1 : v0) + (i >> 1) * 512, soff, AUX); SB(); }
}
__device__ __forceinline__ f32x4 mfma(u32x4 x, u32x4 y, f32x4 acc) {
  return __builtin_amdgcn_mfma_f32_16x16x32_bf16(__builtin_bit_cast(bf16x8, x), __builtin_bit_cast(bf16x8, y), acc, 0, 0, 0);
}
__device__ __forceinline__ void qk_mfma(const u32x4 (&r)[8], const u32x4 (&qf)[2], f32x4 (&d)[4]) {
#pragma unroll
  for (int j = 0; j < 4; ++j) d[j] = mfma(qf[1], r[2 * j + 1], mfma(qf[0], r[2 * j], f32x4{0.f, 0.f, 0.f, 0.f}));
  SB();
}
__device__ __forceinline__ unsigned short qk_score(const f32x4 (&d)[4], bool b0, bool b1) {
  float v[4];
#pragma unroll
  for (int j = 0; j < 4; ++j) {
    float e0 = d[j][0], e1 = d[j][1], e2 = d[j][2], e3 = d[j][3];
    asm("" : "+v"(e0), "+v"(e2));
    const float lo = b0 ? e1 : e0, hi = b0 ? e3 : e2;
    v[j] = b1 ? hi : lo;
  }
  const float u0 = (b0 ? v[1] : v[0]) + dpp<XOR1>(b0 ? v[0] : v[1]);
  const float u1 = (b0 ? v[3] : v[2]) + dpp<XOR1>(b0 ? v[2] : v[3]);
  const float w = (b1 ? u1 : u0) + dpp<XOR2>(b1 ? u0 : u1);
  return (unsigned short)(pk_bf16(w, w) & 0xffffu);
}
__device__ __forceinline__ void pv_mfma(const u32x4 (&vr)[8], u32x2 pk, int msel, f32x4& acc0, f32x4& acc1) {
#pragma unroll
  for (int q = 0; q < 4; ++q) {
    const u32x4 pa = {msel == 2 * q ? pk[0] : 0u, msel == 2 * q ? pk[1] : 0u, msel == 2 * q + 1 ? pk[0] : 0u, msel == 2 * q + 1 ? pk[1] : 0u};
    const unsigned sel = (q & 1) ? 0x07060302u : 0x05040100u;
    const int d0 = q >> 1, d1 = 2 + (q >> 1);
    const u32x4 b0 = {__builtin_amdgcn_perm(vr[2][d0], vr[0][d0], sel), __builtin_amdgcn_perm(vr[6][d0], vr[4][d0], sel),
                      __builtin_amdgcn_perm(vr[3][d0], vr[1][d0], sel), __builtin_amdgcn_perm(vr[7][d0], vr[5][d0], sel)};
    const u32x4 b1 = {__builtin_amdgcn_perm(vr[2][d1], vr[0][d1], sel), __builtin_amdgcn_perm(vr[6][d1], vr[4][d1], sel),
                      __builtin_amdgcn_perm(vr[3][d1], vr[1][d1], sel), __builtin_amdgcn_perm(vr[7][d1], vr[5][d1], sel)};
    acc0 = mfma(pa, b0, acc0);
    acc1 = mfma(pa, b1, acc1);
  }
  SB();
}
__device__ __forceinline__ void fma8(float (&s)[8], float xv, u32x4 w) {
#pragma unroll
  for (int i = 0; i < 4; ++i) { s[2 * i] = fmaf(xv, bf_lo(w[i]), s[2 * i]); s[2 * i + 1] = fmaf(xv, bf_hi(w[i]), s[2 * i + 1]); }
}
template <bool WHOLE>
__device__ __forceinline__ void load_qf(u32x4 (&qf)[2], const unsigned char* lds, int c, int g) {
#pragma unroll
  for (int e = 0; e < 2; ++e) qf[e] = *reinterpret_cast<const u32x4*>(lds + L_Q + ((c >> 2) & 1) * 512 + ((WHOLE ? 4 * e + g : piece(e, g)) * 4 + (c & 3)) * 16);
}

__global__ void __launch_bounds__(NTHR, 2) fused_decode(Args a) {
  extern __shared__ __attribute__((aligned(16))) unsigned char lds[];
  const int tid = threadIdx.x, lane = tid & 63, wave = __builtin_amdgcn_readfirstlane(tid >> 6);
  const int wg = blockIdx.x, h = wg >> 5, s = wg & 31;
  const int c = lane & 15, g = lane >> 4;
  STAMP(0);
  const rsrc_t r_ws = mk_rsrc(a.ws, WS_END);
  const rsrc_t r_k = mk_rsrc(a.k, 8u * SK * DH * 2);
  const unsigned wg_off = (unsigned)wg * (128 * TILE);
  const unsigned vw0 = (c >> 2) * 2048 + g * 64 + (c & 3) * 16, vw1 = vw0 + 256;
  const bool b0 = (c & 1) != 0, b1 = (c & 2) != 0;
  const int sc_lane = (g & 1) * 32 + c * 2;
  bool timed_out = false;
  unsigned tag;
  unsigned wop0[8], wop1[8];
  float y0 = 0.f, y1 = 0.f;

  if (wave < 4) {
    __builtin_amdgcn_s_setprio(3);
    const unsigned t_start = now();
    const bool qwg = (s & 8) == 0;
    const rsrc_t r_x = mk_rsrc(a.x, D * 2);
    const rsrc_t r_wq = mk_rsrc(a.wq, qwg ? D * NQ * 2 : 0);
    const int qsl = (s & 7) * 2 + (s >> 4);
    const u32x4 xv = __builtin_amdgcn_raw_buffer_load_b128(r_x, lane * 16, 0, 0);
    const unsigned epoch = __builtin_amdgcn_raw_buffer_load_b32(r_ws, 0, WS_EPOCH, AUX_SC1);
    const int qrow = (lane >> 2) + 16 * wave;
    const unsigned wq_off = (unsigned)qrow * (NQ * 2) + (unsigned)(h * 512 + qsl * 32) * 2 + (lane & 3) * 16;
    u32x4 wv[7];
#pragma unroll
    for (int i = 0; i < 7; ++i) wv[i] = __builtin_amdgcn_raw_buffer_load_b128(r_wq, wq_off, i * 64 * NQ * 2, AUX_NT);
    SB();
    if (tid < 16) *reinterpret_cast<unsigned*>(lds + L_CTL + tid * 4) = 0u;
    lds_barrier();

    {
      float xs[8];
#pragma unroll
      for (int i = 0; i < 4; ++i) { xs[2 * i] = bf_lo(xv[i]); xs[2 * i + 1] = bf_hi(xv[i]); }
      float sm = 0.f;
#pragma unroll
      for (int i = 0; i < 8; ++i) sm += xs[i];
      const float mean = wave_sum(sm) * (1.0f / D);
      float sq = 0.f;
#pragma unroll
      for (int i = 0; i < 8; ++i) { const float d = xs[i] - mean; sq = fmaf(d, d, sq); }
      sq = lane < D / 8 ? sq : 0.f;
      const float var = wave_sum(sq) * (1.0f / D);
      const float mean_b = rnd_bf16(mean);
      const float den_b = rnd_bf16(sqrtf(rnd_bf16(rnd_bf16(var) + 1e-5f)));
#pragma unroll
      for (int i = 0; i < 8; ++i) xs[i] = rnd_bf16(rnd_bf16(xs[i] - mean_b) / den_b);
      f32x4* xp = reinterpret_cast<f32x4*>(lds + L_XN + lane * 32);
      xp[0] = f32x4{xs[0], xs[1], xs[2], xs[3]}; xp[1] = f32x4{xs[4], xs[5], xs[6], xs[7]};
      FENCE();
    }
    tag = __builtin_amdgcn_readfirstlane(epoch) + 1u;
    if (qwg) {
      float sacc[8] = {0.f, 0.f, 0.f, 0.f, 0.f, 0.f, 0.f, 0.f};
#pragma unroll
      for (int i = 0; i < 7; ++i) fma8(sacc, *reinterpret_cast<const float*>(lds + L_XN + (qrow + 64 * i) * 4), wv[i]);
#pragma unroll
      for (int i = 0; i < 8; ++i) { float t = sacc[i]; t += dpp<ROR4>(t); t += dpp<XOR8>(t); sacc[i] = x32_sum(x16_sum(t)); }
      if (lane < 4) {
        f32x4* p = reinterpret_cast<f32x4*>(lds + L_RED + wave * 256 + lane * 32);
        p[0] = f32x4{sacc[0], sacc[1], sacc[2], sacc[3]}; p[1] = f32x4{sacc[4], sacc[5], sacc[6], sacc[7]};
      }
      lds_arrive(lds, C_PART, lane);
      if (wave == 0) {
        lds_wait_ge(lds, C_PART, 4u);
        if (lane < 16) {
          float q0 = 0.f, q1 = 0.f;
#pragma unroll
          for (int w = 0; w < 4; ++w) { const f32x2 t = *reinterpret_cast<const f32x2*>(lds + L_RED + w * 256 + lane * 8); q0 += t[0]; q1 += t[1]; }
          __builtin_amdgcn_raw_buffer_store_b64(u32x2{pk_bf16(q0, q1), tag}, r_ws, lane * 8, WS_QG + (h * 16 + qsl) * LSTR, AUX_SC1);
        }
        STAMP(1);
      }
    }
    if (wave == 0) {
      u32x4 qg[2];
      const unsigned qo[2] = {(lane >> 2) * LSTR + (lane & 3) * 32, (lane >> 2) * LSTR + (lane & 3) * 32 + 16};
      sleep_until<1>(t_start, Q_FIRST);
      timed_out = !gather<2, Q_SLEEP>(qg, r_ws, qo, WS_QG + h * 16 * LSTR, tag);
      *reinterpret_cast<u32x4*>(lds + L_Q + lane * 16) = u32x4{qg[0][0], qg[0][2], qg[1][0], qg[1][2]};
      if (lane == 0) *reinterpret_cast<unsigned*>(lds + C_TAG) = tag;
      lds_arrive(lds, C_QRDY, lane);
    }
    if (wave != 0) {
      __builtin_amdgcn_s_setprio(HK_PRIO);
      const int slot = NI + wave - 1;
      u32x4 kt[4][8];
#pragma unroll
      for (int i = 0; i < 4; ++i) { chain_wait(lds, 4 * slot + i); issue<AUX_K>(kt[i], r_k, vw0, vw1, wg_off + (4 * slot + i) * TILE); }
      asm volatile("s_waitcnt vmcnt(24)" ::: "memory"); lds_arrive(lds, C_LANDED, lane); PROGL();
      asm volatile("s_waitcnt vmcnt(16)" ::: "memory"); lds_arrive(lds, C_LANDED, lane); PROGL();
      asm volatile("s_waitcnt vmcnt(8)" ::: "memory"); lds_arrive(lds, C_LANDED, lane); PROGL();
      asm volatile("s_waitcnt vmcnt(0)" ::: "memory"); lds_arrive(lds, C_LANDED, lane); PROGL();
      lds_wait_ge(lds, C_QRDY, 1u);
      u32x4 qf[2];
      load_qf<true>(qf, lds, c, g);
#pragma unroll
      for (int i = 0; i < 4; ++i) {
        f32x4 d[4];
        qk_mfma(kt[i], qf, d);
        *reinterpret_cast<unsigned short*>(lds + L_P + i * 2048 + slot * 64 + sc_lane) = qk_score(d, b0, b1);
        PROG();
      }
      lds_arrive(lds, C_KHELP, lane);
      __builtin_amdgcn_s_setprio(3);
    }
    STAMP(3);
    const int fb = h * 16 + (s & 7) + ((s >> 4) << 3);
    if (!qwg && fb < 112) {
      const rsrc_t r_gu = mk_rsrc((wave & 1) ? a.wu : a.wg, D * FF * 2);
      const int frow = (fb / 28) * 112 + (wave >> 1) * 56 + (lane >> 3);
      const unsigned goff = (unsigned)frow * (FF * 2) + (fb % 28) * 128 + (lane & 7) * 16;
#pragma unroll
      for (int i = 0; i < 7; ++i) wv[i] = __builtin_amdgcn_raw_buffer_load_b128(r_gu, goff, i * 8 * FF * 2, AUX_NT);
      float sg[8] = {0.f, 0.f, 0.f, 0.f, 0.f, 0.f, 0.f, 0.f};
#pragma unroll
      for (int i = 0; i < 7; ++i) fma8(sg, *reinterpret_cast<const float*>(lds + L_XN + (frow + 8 * i) * 4), wv[i]);
#pragma unroll
      for (int i = 0; i < 8; ++i) { float t = sg[i]; t += dpp<XOR8>(t); sg[i] = x32_sum(x16_sum(t)); }
      if (lane < 8) {
        f32x4* p = reinterpret_cast<f32x4*>(lds + L_RED + wave * 256 + lane * 32);
        p[0] = f32x4{sg[0], sg[1], sg[2], sg[3]}; p[1] = f32x4{sg[4], sg[5], sg[6], sg[7]};
      }
      lds_arrive(lds, C_PART, lane);
      if (wave == 1) {
        lds_wait_ge(lds, C_PART, 4u);
        const int o = ((lane >> 2) & 1) * 256 + (lane >> 3) * 32 + (lane & 3) * 8;
        const f32x2 t0 = *reinterpret_cast<const f32x2*>(lds + L_RED + o), t1 = *reinterpret_cast<const f32x2*>(lds + L_RED + 512 + o);
        __builtin_amdgcn_raw_buffer_store_b128(u32x4{__float_as_uint(t0[0] + t1[0]), tag, __float_as_uint(t0[1] + t1[1]), tag}, r_ws,
                                               (lane >> 3) * 512 + (lane & 7) * 16, WS_GU + (fb % 28) * 4096 + (fb / 28) * 128, AUX_SC1);
      }
    }
    {
      const rsrc_t r_wo = mk_rsrc(a.wo, NQ * D * 2);
      const unsigned coff = tid < D / 2 ? tid * 4 : OOB;
      const bool ffn = wg < FF / 8;
      const rsrc_t r_d = mk_rsrc(a.wd, ffn ? FF * D * 2 : 0);
      unsigned draw[8], wraw[16];
#pragma unroll
      for (int j = 0; j < 8; ++j) draw[j] = __builtin_amdgcn_raw_buffer_load_b32(r_d, coff, (wg * 8 + j) * (D * 2), AUX_NT);
#pragma unroll
      for (int j = 0; j < 16; ++j) wraw[j] = __builtin_amdgcn_raw_buffer_load_b32(r_wo, coff, (wg * 16 + j) * (D * 2), AUX_NT);
#pragma unroll
      for (int j = 0; j < 8; ++j) { wop0[j] = __builtin_amdgcn_perm(wraw[2 * j + 1], wraw[2 * j], 0x05040100u); wop1[j] = __builtin_amdgcn_perm(wraw[2 * j + 1], wraw[2 * j], 0x07060302u); }
      if (wave == 3) {
        sleep_until<32>(t_start, GU_AT);
        u32x4 gu[1];
        const unsigned go[1] = {(ffn && lane < 32) ? (unsigned)lane * 16 : OOB};
        if (!gather<1, 64>(gu, r_ws, go, WS_GU + wg * 512, tag)) timed_out = true;
        float g0 = __uint_as_float(gu[0][0]), g1 = __uint_as_float(gu[0][2]);
        g0 += dpp<XOR8>(g0); g1 += dpp<XOR8>(g1);
        g0 = rnd_bf16(x16_sum(g0)); g1 = rnd_bf16(x16_sum(g1));
        const float u0 = dpp<ROR4>(g0), u1 = dpp<ROR4>(g1);
        const float h0 = rnd_bf16((g0 > 0.f ? g0 : g0 * 0.f) * u0), h1 = rnd_bf16((g1 > 0.f ? g1 : g1 * 0.f) * u1);
        if (lane < 4) *reinterpret_cast<f32x2*>(lds + L_H + lane * 8) = f32x2{h0, h1};
        lds_arrive(lds, C_HRDY, lane);
      }
      lds_wait_ge<32>(lds, C_HRDY, 1u);
#pragma unroll
      for (int j = 0; j < 8; ++j) {
        const float hv = *reinterpret_cast<const float*>(lds + L_H + j * 4);
        y0 = fmaf(hv, bf_lo(draw[j]), y0); y1 = fmaf(hv, bf_hi(draw[j]), y1);
      }
      asm volatile("" : "+v"(y0), "+v"(y1), "+v"(wop0[0]), "+v"(wop0[1]), "+v"(wop0[2]), "+v"(wop0[3]), "+v"(wop0[4]), "+v"(wop0[5]), "+v"(wop0[6]), "+v"(wop0[7]),
                        "+v"(wop1[0]), "+v"(wop1[1]), "+v"(wop1[2]), "+v"(wop1[3]), "+v"(wop1[4]), "+v"(wop1[5]), "+v"(wop1[6]), "+v"(wop1[7]));
    }
    STAMP(11);
    if (wave == 2) {
      const rsrc_t r_v = mk_rsrc(a.v, 8u * SK * DH * 2), r_o = mk_rsrc(a.out, D * 2);
      const unsigned tv = __builtin_amdgcn_raw_buffer_load_b32(r_v, lane < 16 ? lane * 65536 : OOB, wg_off, AUX_NT);
      const unsigned to = __builtin_amdgcn_raw_buffer_load_b32(r_o, lane < 1 ? 0 : OOB, 0, AUX_NT);
      asm volatile("" :: "v"(tv), "v"(to));
    }
    if (wave == 1) {
      lds_wait_ge<8>(lds, C_VDONE, 0x10000u * 4u + CP_V);
      const unsigned t_cp = now();
      const int hd = lane & 1;
      float mw[4], lw[4];
#pragma unroll
      for (int w = 0; w < 4; ++w) { const f32x2 t = *reinterpret_cast<const f32x2*>(lds + L_ML + w * 16 + hd * 8); mw[w] = t[0]; lw[w] = t[1]; }
      const float M = fmaxf(fmaxf(mw[0], mw[1]), fmaxf(mw[2], mw[3]));
      float L = 0.f;
#pragma unroll
      for (int w = 0; w < 4; ++w) L = fmaf(__builtin_amdgcn_exp2f((mw[w] - M) * C2), lw[w], L);
      __builtin_amdgcn_raw_buffer_store_b128(u32x4{(lane & 2) ? t_cp : __float_as_uint(M), tag, (lane & 2) ? t_cp : __float_as_uint(L), tag}, r_ws,
                                             lane < 8 ? lane * 16 : OOB, WS_ML + wg * LSTR, AUX_SC1);
      sleep_until<16>(t_cp, CP_WAIT);
      u32x4 ml[2];
      const unsigned mo[2] = {(lane >> 2) * LSTR + (lane & 3) * 16, (16 + (lane >> 2)) * LSTR + (lane & 3) * 16};
      if (!gather<2, 32>(ml, r_ws, mo, WS_ML + h * 32 * LSTR, tag)) timed_out = true;
      const float Ma = __uint_as_float(ml[0][0]), Mb = __uint_as_float(ml[1][0]);
      float Mg = fmaxf(Ma, Mb);
      Mg = fmaxf(Mg, dpp<ROR4>(Mg)); Mg = fmaxf(Mg, dpp<XOR8>(Mg)); Mg = x32_max(x16_max(Mg));
      float Lg = __builtin_amdgcn_exp2f((Ma - Mg) * C2) * __uint_as_float(ml[0][2]) + __builtin_amdgcn_exp2f((Mb - Mg) * C2) * __uint_as_float(ml[1][2]);
      Lg = (lane & 2) ? (float)(int)(t_cp - ml[0][0]) + (float)(int)(t_cp - ml[1][0]) : Lg;
      Lg += dpp<ROR4>(Lg); Lg += dpp<XOR8>(Lg); Lg = x32_sum(x16_sum(Lg));
      if (lane < 2) {
#pragma unroll
        for (int w = 0; w < 4; ++w) *reinterpret_cast<float*>(lds + L_SC + w * 8 + lane * 4) = timed_out ? __builtin_nanf("") : __builtin_amdgcn_exp2f((mw[w] - Mg) * C2) / Lg;
      }
      if (lane == 2) {
        const float nf = fminf((Lg * (1.0f / 32) + (float)CP_OFF) * (0.01f * CP_GAIN), 128.f);
        if (nf >= 1.f) *reinterpret_cast<unsigned*>(lds + C_FAST) = (*reinterpret_cast<const unsigned*>(lds + C_VDONE) & 0xffffu) + 8u + (unsigned)nf;
      }
      STAMP(14);
    }
  } else {
    const int w = wave - 4;
    const rsrc_t r_v = mk_rsrc(a.v, 8u * SK * DH * 2);
    const unsigned vv0 = g * 2048 + piece(0, c >> 2) * 64 + (c & 3) * 16, vv1 = g * 2048 + piece(1, c >> 2) * 64 + (c & 3) * 16;
    const unsigned vk0 = (c >> 2) * 2048 + piece(0, g) * 64 + (c & 3) * 16, vk1 = (c >> 2) * 2048 + piece(1, g) * 64 + (c & 3) * 16;
    const unsigned t0 = wg_off + w * TILE;
    u32x4 ra[8], rb[8];
    __builtin_amdgcn_s_setprio(ST_PRIO);
    u32x4 rx[NI - 2][8];
    if (s & 8) issue<AUX_K>(ra, r_k, vw0, vw1, t0);
    lds_barrier();
    if (!(s & 8)) issue<AUX_K>(ra, r_k, vw0, vw1, t0);
#define CHAIN_STEP(R, SLOT) do { \
      constexpr bool first_ = CH_W < 5;     \
      if (first_ && lds_ld(lds, C_QRDY) == 0u) { asm volatile("s_waitcnt vmcnt(0)" ::: "memory"); lds_arrive(lds, C_LANDED, lane); PROGL(); } \
      chain_wait(lds, 4 * (SLOT) + w); \
      issue<AUX_K>(R, r_k, vw0, vw1, t0 + 4 * (SLOT) * TILE); \
      if (!first_ && lds_ld(lds, C_QRDY) == 0u) { asm volatile("s_waitcnt vmcnt(8)" ::: "memory"); lds_arrive(lds, C_LANDED, lane); PROGL(); } } while (0)
    CHAIN_STEP(rb, 1);
#pragma unroll
    for (int i = 2; i < NI; ++i) CHAIN_STEP(rx[i - 2], i);
#undef CHAIN_STEP
    if (lds_ld(lds, C_QRDY) == 0u) { asm volatile("s_waitcnt vmcnt(0)" ::: "memory"); lds_arrive(lds, C_LANDED, lane); PROGL(); }
    lds_wait_ge(lds, C_QRDY, 1u);
    tag = lds_ld(lds, C_TAG);
    STAMP(2);
    u32x4 qf[2];
    load_qf<true>(qf, lds, c, g);
    unsigned char* const pl = lds + L_P + w * 2048;
    {
      f32x4 d[4];
      unsigned short* sw = reinterpret_cast<unsigned short*>(pl + sc_lane);
      unsigned toff = t0 + 4 * (NI + 3) * TILE;
#define K_STEP(R, ISSUE_NEXT, SLOT) do { qk_mfma(R, qf, d); PROG(); ISSUE_NEXT; sw[(SLOT) * 32] = qk_score(d, b0, b1); } while (0)
      K_STEP(ra, issue<AUX_K>(ra, r_k, vk0, vk1, toff), 0);
      K_STEP(rb, issue<AUX_K>(rb, r_k, vk0, vk1, toff + 4 * TILE), 1);
#pragma unroll
      for (int i = 2; i < NI; ++i) K_STEP(rx[i - 2], (void)0, i);
      load_qf<false>(qf, lds, c, g);
      sw += (NI + 3) * 32;
      for (int it = 1; it < (29 - NI) / 2; ++it) {
        toff += 8 * TILE;
        K_STEP(ra, issue<AUX_K>(ra, r_k, vk0, vk1, toff), 0);
        K_STEP(rb, issue<AUX_K>(rb, r_k, vk0, vk1, toff + 4 * TILE), 1);
        sw += 2 * 32;
      }
      K_STEP(ra, issue<AUX_V>(ra, r_v, vv0, vv1, t0), 0);
      K_STEP(rb, issue<AUX_V>(rb, r_v, vv0, vv1, t0 + 4 * TILE), 1);
#undef K_STEP
    }
    STAMP(12);
    lds_wait_ge(lds, C_KHELP, 3u);
    {
      u32x4* pp = reinterpret_cast<u32x4*>(pl + lane * 32);
      u32x4 t[2] = {pp[0], pp[1]};
      float mx = bf_lo(t[0][0]);
#pragma unroll
      for (int j = 0; j < 2; ++j)
#pragma unroll
        for (int i = 0; i < 4; ++i) mx = fmaxf(mx, fmaxf(bf_lo(t[j][i]), bf_hi(t[j][i])));
      mx = parity_max(mx);
      const float mc = mx * C2;
      float lp = 0.f;
#pragma unroll
      for (int j = 0; j < 2; ++j) {
#pragma unroll
        for (int i = 0; i < 4; ++i) {
          const float p0 = __builtin_amdgcn_exp2f(fmaf(bf_lo(t[j][i]), C2, -mc)), p1 = __builtin_amdgcn_exp2f(fmaf(bf_hi(t[j][i]), C2, -mc));
          lp += p0 + p1; t[j][i] = pk_bf16(p0, p1);
        }
        pp[j] = t[j];
      }
      lp = parity_sum(lp);
      if (lane < 2) *reinterpret_cast<f32x2*>(lds + L_ML + w * 16 + lane * 8) = f32x2{mx, lp};
      asm volatile("s_waitcnt lgkmcnt(0)" ::: "memory");
      if (lane == 0) __hip_atomic_fetch_add(reinterpret_cast<unsigned*>(lds + C_VDONE), 0x10000u, __ATOMIC_RELAXED, __HIP_MEMORY_SCOPE_WORKGROUP);
    }
    f32x4 acc0 = {0.f, 0.f, 0.f, 0.f}, acc1 = {0.f, 0.f, 0.f, 0.f};
    {
      const int msel = c >> 1;
      const unsigned char* pr = pl + (c & 1) * 32 + g * 8;
      const unsigned vf0 = g * 2048 + c * 16;
      float* const rec = reinterpret_cast<float*>(lds + L_ACC + w * 2048) + 8 * (c & 3) + g;
      auto put = [&](bool whole) {
        const int e0 = (whole ? c >> 2 : piece(0, c >> 2)) * 32, e1 = (whole ? 4 + (c >> 2) : piece(1, c >> 2)) * 32;
#pragma unroll
        for (int k = 0; k < 4; ++k) { float* r = rec + (k & 1) * 256 + ((k >> 1) ? e1 : e0); r[0] = acc0[k]; r[4] = acc1[k]; }
      };
      auto get = [&](bool whole) {
        const int e0 = (whole ? c >> 2 : piece(0, c >> 2)) * 32, e1 = (whole ? 4 + (c >> 2) : piece(1, c >> 2)) * 32;
#pragma unroll
        for (int k = 0; k < 4; ++k) { const float* r = rec + (k & 1) * 256 + ((k >> 1) ? e1 : e0); acc0[k] = r[0]; acc1[k] = r[4]; }
      };
      unsigned toff = t0 + 8 * TILE;
      bool cur = false;
#define V_STEP(R, ISSUE_NEXT) do { const u32x2 pk_ = *reinterpret_cast<const u32x2*>(pr); pr += 64; pv_mfma(R, pk_, msel, acc0, acc1); PROG(); ISSUE_NEXT; } while (0)
      for (int it = 0; it < 15; ++it) {
        const u32x2 cw = *reinterpret_cast<const u32x2*>(lds + C_VDONE);
        const bool nxt = (unsigned)(8 * it + 8 + w) < __builtin_amdgcn_readfirstlane(cw[1]) || 8 * it + 8 >= 128 - LASTFAST;
        if ((unsigned)(8 * it + 4) < (__builtin_amdgcn_readfirstlane(cw[0]) & 0xffffu)) __builtin_amdgcn_s_setprio(1); else __builtin_amdgcn_s_setprio(0);
        const unsigned n0 = nxt ? vf0 : vv0, n1 = nxt ? vf0 + 256 : vv1;
        V_STEP(ra, issue<AUX_V>(ra, r_v, n0, n1, toff));
        V_STEP(rb, issue<AUX_V>(rb, r_v, n0, n1, toff + 4 * TILE));
        if (lane == 0) __hip_atomic_fetch_add(reinterpret_cast<unsigned*>(lds + C_VDONE), 2u, __ATOMIC_RELAXED, __HIP_MEMORY_SCOPE_WORKGROUP);
        if (nxt != cur) { put(cur); FENCE(); get(nxt); cur = nxt; }
        FENCE();
        toff += 8 * TILE;
      }
      V_STEP(ra, (void)0);
      V_STEP(rb, (void)0);
#undef V_STEP
      STAMP(3);
      put(cur);
    }
#pragma unroll
    for (int j = 0; j < 8; ++j) { wop0[j] = 0u; wop1[j] = 0u; }
  }
  PROG_OUT();
  STAMP(4);
  lds_barrier();
  STAMP(5);
  {
    float O = 0.f;
#pragma unroll
    for (int w = 0; w < 4; ++w)
      O = fmaf(*reinterpret_cast<const float*>(lds + L_SC + w * 8 + (tid >> 8) * 4), *reinterpret_cast<const float*>(lds + L_ACC + w * 2048 + tid * 4), O);
    __builtin_amdgcn_raw_buffer_store_b64(u32x2{__float_as_uint(O), tag}, r_ws, (tid >> 4) * 4096 + (tid & 15) * 8, WS_PO + h * (32 * 4096) + s * 128, AUX_SC1);
  }
  STAMP(6);
  if (wave < 4) {
    u32x4 po[4];
    const unsigned oo[4] = {(unsigned)lane * 16, (unsigned)lane * 16 + 1024, (unsigned)lane * 16 + 2048, (unsigned)lane * 16 + 3072};
    if (!gather<4, 0>(po, r_ws, oo, WS_PO + wg * 4096, tag)) timed_out = true;
    STAMP(7);
    float o0 = (__uint_as_float(po[0][0]) + __uint_as_float(po[1][0])) + (__uint_as_float(po[2][0]) + __uint_as_float(po[3][0]));
    float o1 = (__uint_as_float(po[0][2]) + __uint_as_float(po[1][2])) + (__uint_as_float(po[2][2]) + __uint_as_float(po[3][2]));
    o0 += dpp<XOR8>(o0); o1 += dpp<XOR8>(o1);
    o0 = x32_sum(x16_sum(o0)); o1 = x32_sum(x16_sum(o1));
    const unsigned opk = pk_bf16(o0, o1);
#pragma unroll
    for (int j = 0; j < 8; ++j) { const unsigned oj = __builtin_amdgcn_readlane(opk, j); y0 = dot2(wop0[j], oj, y0); y1 = dot2(wop1[j], oj, y1); }
    if (timed_out) y0 = __builtin_nanf("");
    const unsigned yoff = tid < D / 2 ? (unsigned)((2 * tid / CPR) * (256 * CPR) + (2 * tid % CPR)) * 8 : OOB;
    __builtin_amdgcn_raw_buffer_store_b128(u32x4{__float_as_uint(y0), tag, __float_as_uint(y1), tag}, r_ws, yoff, WS_YA + wg * (CPR * 8), AUX_SC1);
    STAMP(8);
    if (wave == 0) lds_arrive(lds, C_HOP1, lane);
  }
  if (wg >= NRED) return;
  lds_wait_ge(lds, C_HOP1, 1u);
  {
    constexpr int NLD = (256 * CPR / 2 + NTHR - 1) / NTHR;
    const float xo = tid < CPR ? bf_lo(a.x[wg * CPR + tid]) : 0.f;
    u32x4 t[NLD];
    unsigned yo[NLD];
#pragma unroll
    for (int k = 0; k < NLD; ++k) yo[k] = tid + NTHR * k < 256 * CPR / 2 ? (unsigned)(tid + NTHR * k) * 16 : OOB;
    if (!gather<NLD, 0>(t, r_ws, yo, WS_YA + wg * (256 * CPR * 8), tag)) timed_out = true;
    STAMP(9);
    float s0 = 0.f, s1 = 0.f;
#pragma unroll
    for (int k = 0; k < NLD; ++k) { s0 += __uint_as_float(t[k][0]); s1 += __uint_as_float(t[k][2]); }
    if (CPR / 2 <= 8) { s0 += dpp<XOR8>(s0); s1 += dpp<XOR8>(s1); }
    if (CPR / 2 <= 4) { s0 += dpp<ROR4>(s0); s1 += dpp<ROR4>(s1); }
    if (CPR / 2 <= 2) { s0 += dpp<XOR2>(s0); s1 += dpp<XOR2>(s1); }
    if (CPR / 2 <= 1) { s0 += dpp<XOR1>(s0); s1 += dpp<XOR1>(s1); }
    s0 = x32_sum(x16_sum(s0)); s1 = x32_sum(x16_sum(s1));
    if (timed_out) s0 = __builtin_nanf("");
    if (lane < CPR / 2) *reinterpret_cast<f32x2*>(lds + L_YA + wave * 64 + lane * 8) = f32x2{s0, s1};
    lds_barrier();
    if (tid < CPR) {
      float r = xo;
#pragma unroll
      for (int w = 0; w < 8; ++w) r += *reinterpret_cast<const float*>(lds + L_YA + w * 64 + tid * 4);
      __builtin_amdgcn_raw_buffer_store_b16((unsigned short)(pk_bf16(r, r) & 0xffffu), mk_rsrc(a.out, D * 2), (wg * CPR + tid) * 2, 0, AUX_OUT);
    }
    if (wg == 0 && tid == 64) __builtin_amdgcn_raw_buffer_store_b32(tag, r_ws, 0, WS_EPOCH, AUX_SC1);
    STAMP(10);
  }
}

}

extern "C" void kernel_launch(void* const* d_in, const int* in_sizes, int n_in, void* d_out, int out_size, void* d_ws, size_t ws_size,
                              hipStream_t stream) {
  (void)in_sizes; (void)n_in; (void)out_size; (void)ws_size;
  Args a;
  a.x = (const unsigned short*)d_in[0];
  a.k = (const unsigned char*)d_in[1];
  a.v = (const unsigned char*)d_in[2];
  a.wq = (const unsigned char*)d_in[3];
  a.wo = (const unsigned char*)d_in[4];
  a.wg = (const unsigned char*)d_in[6];
  a.wu = (const unsigned char*)d_in[7];
  a.wd = (const unsigned char*)d_in[8];
  a.out = (unsigned short*)d_out;
  a.ws = (unsigned char*)d_ws;
  (void)hipFuncSetAttribute((const void*)fused_decode, hipFuncAttributeMaxDynamicSharedMemorySize, LDS_BYTES);
  hipLaunchKernelGGL(fused_decode, dim3(NWG), dim3(NTHR), LDS_BYTES, stream, a);
}
```

```cpp
#include <hip/hip_runtime.h>
#include <hip/hip_bf16.h>
#include <cstdint>

namespace {

typedef unsigned u32x4 __attribute__((ext_vector_type(4)));
typedef unsigned u32x2 __attribute__((ext_vector_type(2)));
typedef float f32x4 __attribute__((ext_vector_type(4)));
typedef float f32x2 __attribute__((ext_vector_type(2)));
typedef __bf16 bf16x8 __attribute__((ext_vector_type(8)));
typedef __bf16 bf16x2 __attribute__((ext_vector_type(2)));
typedef __amdgpu_buffer_rsrc_t rsrc_t;

constexpr int D = 448, SK = 65536, DH = 256, FF = 1792, NQ = 4096;
constexpr int NWG = 256, NTHR = 512;
#define CH_W 8
#define NI 5
#define HK_PRIO 2
#define ST_PRIO 0
#define CP_REF 64
#define CP_AT 144
#define CP_WAIT 300
#define CP_OFF 30
#define CP_TAU 1.005f
#define CODE_PF 0
#define PREWARM 2
#define PREWARM_AT 240
#define AUX_ST 16
#define AUX_LD 16
#define SLACK 4
#define Q_FIRST 500
#define Q_SLEEP 0
#define GU_AT 2600
#define CPR 8
constexpr int NRED = D / CPR;
constexpr unsigned TILE = 8192;
constexpr unsigned OOB = 0x40000000u;
constexpr float C2 = 0.0625f * 1.4426950408889634f;
constexpr unsigned SPIN_CAP = 1u << 22;

constexpr unsigned WS_EPOCH = 0;
constexpr unsigned LSTR = 4352;
constexpr unsigned WS_PO = 4096;
constexpr unsigned WS_YA = WS_PO + 8 * 32 * 32 * 16 * 8;
constexpr unsigned WS_GU = WS_YA + 256 * D * 8;
constexpr unsigned WS_QG = WS_GU + 224 * 4 * 128;
constexpr unsigned WS_ML = WS_QG + 128 * LSTR;
constexpr unsigned WS_END = WS_ML + 256 * LSTR;

constexpr int L_RED = 0;
constexpr int L_CTL = 1024;
constexpr int C_PART = L_CTL, C_QRDY = L_CTL + 4, C_TAG = L_CTL + 12, C_LANDED = L_CTL + 16, C_KHELP = L_CTL + 20, C_HRDY = L_CTL + 24, C_HOP1 = L_CTL + 28,
              C_DONE = L_CTL + 32, C_BASE = L_CTL + 40;
constexpr int L_ML = 1088;
constexpr int L_SC = 1152;
constexpr int L_H = 1184;
constexpr int L_YA = 1216;
constexpr int L_Q = 1792;
constexpr int L_XN = 2816;
constexpr int L_ACC = 4864;
constexpr int L_P = L_ACC + 4 * 2048;
#define LDS_KB 44
constexpr int LDS_BYTES = LDS_KB * 1024;

constexpr int AUX_NT = 2, AUX_SC1 = 16;
#define AUX_K 2
#define AUX_V 2
#define SB() __builtin_amdgcn_sched_barrier(0)
#define FENCE() asm volatile("" ::: "memory")

__device__ __forceinline__ float bf_lo(unsigned u) { return __builtin_bit_cast(float, u << 16); }
__device__ __forceinline__ float bf_hi(unsigned u) { return __builtin_bit_cast(float, u & 0xffff0000u); }
__device__ __forceinline__ unsigned pk_bf16(float lo, float hi) {
  bf16x2 v = {(__bf16)lo, (__bf16)hi};
  return __builtin_bit_cast(unsigned, v);
}
__device__ __forceinline__ float rnd_bf16(float x) { return (float)(__bf16)x; }
__device__ __forceinline__ float dot2(unsigned a, unsigned b, float acc) {
  return __builtin_amdgcn_fdot2_f32_bf16(__builtin_bit_cast(bf16x2, a), __builtin_bit_cast(bf16x2, b), acc, false);
}
template <int CTRL> __device__ __forceinline__ float dpp(float x) {
  return __builtin_bit_cast(float, __builtin_amdgcn_mov_dpp(__builtin_bit_cast(int, x), CTRL, 0xf, 0xf, true));
}
constexpr int XOR1 = 0xB1, XOR2 = 0x4E, XOR7 = 0x141, XOR15 = 0x140, XOR8 = 0x128, ROR4 = 0x124;
__device__ __forceinline__ float x16_max(float x) {
  auto s = __builtin_amdgcn_permlane16_swap(__float_as_uint(x), __float_as_uint(x), false, false);
  return fmaxf(__uint_as_float(s[0]), __uint_as_float(s[1]));
}
__device__ __forceinline__ float x32_max(float x) {
  auto s = __builtin_amdgcn_permlane32_swap(__float_as_uint(x), __float_as_uint(x), false, false);
  return fmaxf(__uint_as_float(s[0]), __uint_as_float(s[1]));
}
__device__ __forceinline__ float x16_sum(float x) {
  auto s = __builtin_amdgcn_permlane16_swap(__float_as_uint(x), __float_as_uint(x), false, false);
  return __uint_as_float(s[0]) + __uint_as_float(s[1]);
}
__device__ __forceinline__ float x32_sum(float x) {
  auto s = __builtin_amdgcn_permlane32_swap(__float_as_uint(x), __float_as_uint(x), false, false);
  return __uint_as_float(s[0]) + __uint_as_float(s[1]);
}
__device__ __forceinline__ float wave_sum(float x) {
  x += dpp<XOR1>(x); x += dpp<XOR2>(x); x += dpp<XOR7>(x); x += dpp<XOR15>(x);
  return x32_sum(x16_sum(x));
}
__device__ __forceinline__ float parity_sum(float x) {
  x += dpp<XOR2>(x); x += dpp<ROR4>(x); x += dpp<XOR8>(x);
  return x32_sum(x16_sum(x));
}
__device__ __forceinline__ float parity_max(float x) {
  x = fmaxf(x, dpp<XOR2>(x)); x = fmaxf(x, dpp<ROR4>(x)); x = fmaxf(x, dpp<XOR8>(x));
  return x32_max(x16_max(x));
}
__device__ __forceinline__ void lds_barrier() {
  asm volatile("s_waitcnt lgkmcnt(0)" ::: "memory");
  __builtin_amdgcn_s_barrier();
  FENCE();
}
__device__ __forceinline__ rsrc_t mk_rsrc(const void* p, unsigned bytes) {
  return __builtin_amdgcn_make_buffer_rsrc(const_cast<void*>(p), 0, bytes, 0x00020000);
}
__device__ __forceinline__ unsigned lds_ld(unsigned char* lds, int off) {
  return __builtin_amdgcn_readfirstlane(__hip_atomic_load(reinterpret_cast<unsigned*>(lds + off), __ATOMIC_RELAXED, __HIP_MEMORY_SCOPE_WORKGROUP));
}
template <int SLEEP = 1>
__device__ __forceinline__ void lds_wait_ge(unsigned char* lds, int off, unsigned want) {
#pragma nounroll
  for (unsigned spin = 0; spin < SPIN_CAP * 8u; ++spin) {
    if (lds_ld(lds, off) >= want) break;
    __builtin_amdgcn_s_sleep(SLEEP);
  }
  FENCE();
}
__device__ __forceinline__ void lds_arrive(unsigned char* lds, int off, int lane) {
  asm volatile("s_waitcnt lgkmcnt(0)" ::: "memory");
  if (lane == 0) __hip_atomic_fetch_add(reinterpret_cast<unsigned*>(lds + off), 1u, __ATOMIC_RELAXED, __HIP_MEMORY_SCOPE_WORKGROUP);
}
__device__ __forceinline__ void lds_add(unsigned char* lds, int off, unsigned v, int lane) {
  if (lane == 0) __hip_atomic_fetch_add(reinterpret_cast<unsigned*>(lds + off), v, __ATOMIC_RELAXED, __HIP_MEMORY_SCOPE_WORKGROUP);
}
__device__ __forceinline__ unsigned now() { return (unsigned)__builtin_amdgcn_s_memrealtime(); }
template <int SLEEP>
__device__ __forceinline__ void sleep_until(unsigned t0, unsigned ticks) {
#pragma nounroll
  for (unsigned spin = 0; spin < SPIN_CAP && now() - t0 < ticks; ++spin) __builtin_amdgcn_s_sleep(SLEEP);
}
__device__ __forceinline__ void chain_wait(unsigned char* lds, int idx) {
#pragma nounroll
  for (unsigned spin = 0; spin < SPIN_CAP * 8u; ++spin) {
    if ((int)lds_ld(lds, C_LANDED) >= idx + 1 - CH_W || lds_ld(lds, C_QRDY) != 0u) break;
    __builtin_amdgcn_s_sleep(1);
  }
  FENCE();
}
template <int N, int SLEEP>
__device__ __forceinline__ bool gather(u32x4 (&v)[N], rsrc_t rs, const unsigned (&voff)[N], unsigned soff, unsigned tag) {
  bool have[N];
#pragma unroll
  for (int k = 0; k < N; ++k) { have[k] = voff[k] == OOB; v[k] = u32x4{0u, 0u, 0u, 0u}; }
#pragma nounroll
  for (unsigned spin = 0; spin < SPIN_CAP; ++spin) {
    FENCE();
    u32x4 t[N];
#pragma unroll
    for (int k = 0; k < N; ++k) t[k] = __builtin_amdgcn_raw_buffer_load_b128(rs, have[k] ? OOB : voff[k], soff, AUX_LD);
    bool all = true;
#pragma unroll
    for (int k = 0; k < N; ++k) {
      const bool now = !have[k] && t[k][1] == tag && t[k][3] == tag;
      v[k][0] = now ? t[k][0] : v[k][0]; v[k][2] = now ? t[k][2] : v[k][2];
      have[k] = have[k] || now; all = all && have[k];
    }
    if (__builtin_amdgcn_ballot_w64(!all) == 0) return true;
    if (SLEEP > 0) __builtin_amdgcn_s_sleep(SLEEP);
  }
  return false;
}
#define STAMP(slot) do {} while (0)
#define PROG() do {} while (0)
#define PROG_OUT() do {} while (0)
#define PROGL() do {} while (0)

struct Args {
  const unsigned short* x;
  const unsigned char* k;
  const unsigned char* v;
  const unsigned char* wq;
  const unsigned char* wo;
  const unsigned char* wg;
  const unsigned char* wu;
  const unsigned char* wd;
  unsigned short* out;
  unsigned char* ws;
};

__host__ __device__ constexpr int piece(int e, int n) { return n < 2 ? 2 * e + n : 2 * n + e; }
template <int AUX>
__device__ __forceinline__ void issue(u32x4 (&r)[8], rsrc_t rs, unsigned v0, unsigned v1, unsigned soff) {
#pragma unroll
  for (int i = 0; i < 8; ++i) { r[i] = __builtin_amdgcn_raw_buffer_load_b128(rs, ((i & 1) ? v1 : v0) + (i >> 1) * 512, soff, AUX); SB(); }
}
__device__ __forceinline__ f32x4 mfma(u32x4 x, u32x4 y, f32x4 acc) {
  return __builtin_amdgcn_mfma_f32_16x16x32_bf16(__builtin_bit_cast(bf16x8, x), __builtin_bit_cast(bf16x8, y), acc, 0, 0, 0);
}
__device__ __forceinline__ void qk_mfma(const u32x4 (&r)[8], const u32x4 (&qf)[2], f32x4 (&d)[4]) {
#pragma unroll
  for (int j = 0; j < 4; ++j) d[j] = mfma(qf[1], r[2 * j + 1], mfma(qf[0], r[2 * j], f32x4{0.f, 0.f, 0.f, 0.f}));
  SB();
}
__device__ __forceinline__ unsigned short qk_score(const f32x4 (&d)[4], bool b0, bool b1) {
  float v[4];
#pragma unroll
  for (int j = 0; j < 4; ++j) {
    float e0 = d[j][0], e1 = d[j][1], e2 = d[j][2], e3 = d[j][3];
    asm("" : "+v"(e0), "+v"(e2));
    const float lo = b0 ? e1 : e0, hi = b0 ? e3 : e2;
    v[j] = b1 ? hi : lo;
  }
  const float u0 = (b0 ? v[1] : v[0]) + dpp<XOR1>(b0 ? v[0] : v[1]);
  const float u1 = (b0 ? v[3] : v[2]) + dpp<XOR1>(b0 ? v[2] : v[3]);
  const float w = (b1 ? u1 : u0) + dpp<XOR2>(b1 ? u0 : u1);
  return (unsigned short)(pk_bf16(w, w) & 0xffffu);
}
__device__ __forceinline__ void pv_mfma(const u32x4 (&vr)[8], u32x2 pk, int msel, f32x4& acc0, f32x4& acc1) {
#pragma unroll
  for (int q = 0; q < 4; ++q) {
    const u32x4 pa = {msel == 2 * q ? pk[0] : 0u, msel == 2 * q ? pk[1] : 0u, msel == 2 * q + 1 ? pk[0] : 0u, msel == 2 * q + 1 ? pk[1] : 0u};
    const unsigned sel = (q & 1) ? 0x07060302u : 0x05040100u;
    const int d0 = q >> 1, d1 = 2 + (q >> 1);
    const u32x4 b0 = {__builtin_amdgcn_perm(vr[2][d0], vr[0][d0], sel), __builtin_amdgcn_perm(vr[6][d0], vr[4][d0], sel),
                      __builtin_amdgcn_perm(vr[3][d0], vr[1][d0], sel), __builtin_amdgcn_perm(vr[7][d0], vr[5][d0], sel)};
    const u32x4 b1 = {__builtin_amdgcn_perm(vr[2][d1], vr[0][d1], sel), __builtin_amdgcn_perm(vr[6][d1], vr[4][d1], sel),
                      __builtin_amdgcn_perm(vr[3][d1], vr[1][d1], sel), __builtin_amdgcn_perm(vr[7][d1], vr[5][d1], sel)};
    acc0 = mfma(pa, b0, acc0);
    acc1 = mfma(pa, b1, acc1);
  }
  SB();
}
__device__ __forceinline__ void fma8(float (&s)[8], float xv, u32x4 w) {
#pragma unroll
  for (int i = 0; i < 4; ++i) { s[2 * i] = fmaf(xv, bf_lo(w[i]), s[2 * i]); s[2 * i + 1] = fmaf(xv, bf_hi(w[i]), s[2 * i + 1]); }
}
template <bool WHOLE>
__device__ __forceinline__ void load_qf(u32x4 (&qf)[2], const unsigned char* lds, int c, int g) {
#pragma unroll
  for (int e = 0; e < 2; ++e) qf[e] = *reinterpret_cast<const u32x4*>(lds + L_Q + ((c >> 2) & 1) * 512 + ((WHOLE ? 4 * e + g : piece(e, g)) * 4 + (c & 3)) * 16);
}

__global__ void __launch_bounds__(NTHR, 2) fused_decode(Args a) {
  extern __shared__ __attribute__((aligned(16))) unsigned char lds[];
  const int tid = threadIdx.x, lane = tid & 63, wave = __builtin_amdgcn_readfirstlane(tid >> 6);
  const int wg = blockIdx.x, h = wg >> 5, s = wg & 31;
  const int c = lane & 15, g = lane >> 4;
  STAMP(0);
  const rsrc_t r_ws = mk_rsrc(a.ws, WS_END);
  const rsrc_t r_k = mk_rsrc(a.k, 8u * SK * DH * 2);
  const unsigned wg_off = (unsigned)wg * (128 * TILE);
  const unsigned vw0 = (c >> 2) * 2048 + g * 64 + (c & 3) * 16, vw1 = vw0 + 256;
  const bool b0 = (c & 1) != 0, b1 = (c & 2) != 0;
  const int sc_lane = (g & 1) * 32 + c * 2;
  bool timed_out = false;
  unsigned tag;
  unsigned wop0[8], wop1[8];
  float y0 = 0.f, y1 = 0.f;

  if (wave < 4) {
    __builtin_amdgcn_s_setprio(3);
    const unsigned t_start = now();
    unsigned t_ref = 0, n_ref = 0;
    const bool qwg = (s & 8) == 0;
    const rsrc_t r_x = mk_rsrc(a.x, D * 2);
    const rsrc_t r_wq = mk_rsrc(a.wq, qwg ? D * NQ * 2 : 0);
    const int qsl = (s & 7) * 2 + (s >> 4);
    const u32x4 xv = __builtin_amdgcn_raw_buffer_load_b128(r_x, lane * 16, 0, 0);
    const unsigned epoch = __builtin_amdgcn_raw_buffer_load_b32(r_ws, 0, WS_EPOCH, AUX_SC1);
    const int qrow = (lane >> 2) + 16 * wave;
    const unsigned wq_off = (unsigned)qrow * (NQ * 2) + (unsigned)(h * 512 + qsl * 32) * 2 + (lane & 3) * 16;
    u32x4 wv[7];
#pragma unroll
    for (int i = 0; i < 7; ++i) wv[i] = __builtin_amdgcn_raw_buffer_load_b128(r_wq, wq_off, i * 64 * NQ * 2, AUX_NT);
    SB();
    if (tid < 16) *reinterpret_cast<unsigned*>(lds + L_CTL + tid * 4) = 0u;
    lds_barrier();

    {
      float xs[8];
#pragma unroll
      for (int i = 0; i < 4; ++i) { xs[2 * i] = bf_lo(xv[i]); xs[2 * i + 1] = bf_hi(xv[i]); }
      float sm = 0.f;
#pragma unroll
      for (int i = 0; i < 8; ++i) sm += xs[i];
      const float mean = wave_sum(sm) * (1.0f / D);
      float sq = 0.f;
#pragma unroll
      for (int i = 0; i < 8; ++i) { const float d = xs[i] - mean; sq = fmaf(d, d, sq); }
      sq = lane < D / 8 ? sq : 0.f;
      const float var = wave_sum(sq) * (1.0f / D);
      const float mean_b = rnd_bf16(mean);
      const float den_b = rnd_bf16(sqrtf(rnd_bf16(rnd_bf16(var) + 1e-5f)));
#pragma unroll
      for (int i = 0; i < 8; ++i) xs[i] = rnd_bf16(rnd_bf16(xs[i] - mean_b) / den_b);
      f32x4* xp = reinterpret_cast<f32x4*>(lds + L_XN + lane * 32);
      xp[0] = f32x4{xs[0], xs[1], xs[2], xs[3]}; xp[1] = f32x4{xs[4], xs[5], xs[6], xs[7]};
      FENCE();
    }
    tag = __builtin_amdgcn_readfirstlane(epoch) + 1u;
    if (qwg) {
      float sacc[8] = {0.f, 0.f, 0.f, 0.f, 0.f, 0.f, 0.f, 0.f};
#pragma unroll
      for (int i = 0; i < 7; ++i) fma8(sacc, *reinterpret_cast<const float*>(lds + L_XN + (qrow + 64 * i) * 4), wv[i]);
#pragma unroll
      for (int i = 0; i < 8; ++i) { float t = sacc[i]; t += dpp<ROR4>(t); t += dpp<XOR8>(t); sacc[i] = x32_sum(x16_sum(t)); }
      if (lane < 4) {
        f32x4* p = reinterpret_cast<f32x4*>(lds + L_RED + wave * 256 + lane * 32);
        p[0] = f32x4{sacc[0], sacc[1], sacc[2], sacc[3]}; p[1] = f32x4{sacc[4], sacc[5], sacc[6], sacc[7]};
      }
      lds_arrive(lds, C_PART, lane);
      if (wave == 0) {
        lds_wait_ge(lds, C_PART, 4u);
        if (lane < 16) {
          float q0 = 0.f, q1 = 0.f;
#pragma unroll
          for (int w = 0; w < 4; ++w) { const f32x2 t = *reinterpret_cast<const f32x2*>(lds + L_RED + w * 256 + lane * 8); q0 += t[0]; q1 += t[1]; }
          __builtin_amdgcn_raw_buffer_store_b64(u32x2{pk_bf16(q0, q1), tag}, r_ws, lane * 8, WS_QG + (h * 16 + qsl) * LSTR, AUX_SC1);
        }
        STAMP(1);
      }
    }
    if (wave == 0) {
      u32x4 qg[2];
      const unsigned qo[2] = {(lane >> 2) * LSTR + (lane & 3) * 32, (lane >> 2) * LSTR + (lane & 3) * 32 + 16};
      sleep_until<1>(t_start, Q_FIRST);
      timed_out = !gather<2, Q_SLEEP>(qg, r_ws, qo, WS_QG + h * 16 * LSTR, tag);
      *reinterpret_cast<u32x4*>(lds + L_Q + lane * 16) = u32x4{qg[0][0], qg[0][2], qg[1][0], qg[1][2]};
      if (lane == 0) *reinterpret_cast<unsigned*>(lds + C_TAG) = tag;
      lds_arrive(lds, C_QRDY, lane);
    }
    if (wave != 0) {
      __builtin_amdgcn_s_setprio(HK_PRIO);
      const int slot = NI + wave - 1;
      u32x4 kt[4][8];
#pragma unroll
      for (int i = 0; i < 4; ++i) { chain_wait(lds, 4 * slot + i); issue<AUX_K>(kt[i], r_k, vw0, vw1, wg_off + (4 * slot + i) * TILE); }
      asm volatile("s_waitcnt vmcnt(24)" ::: "memory"); lds_arrive(lds, C_LANDED, lane); PROGL();
      asm volatile("s_waitcnt vmcnt(16)" ::: "memory"); lds_arrive(lds, C_LANDED, lane); PROGL();
      asm volatile("s_waitcnt vmcnt(8)" ::: "memory"); lds_arrive(lds, C_LANDED, lane); PROGL();
      asm volatile("s_waitcnt vmcnt(0)" ::: "memory"); lds_arrive(lds, C_LANDED, lane); PROGL();
      lds_wait_ge(lds, C_QRDY, 1u);
      u32x4 qf[2];
      load_qf<true>(qf, lds, c, g);
#pragma unroll
      for (int i = 0; i < 4; ++i) {
        f32x4 d[4];
        qk_mfma(kt[i], qf, d);
        *reinterpret_cast<unsigned short*>(lds + L_P + i * 2048 + slot * 64 + sc_lane) = qk_score(d, b0, b1);
        PROG();
      }
      lds_arrive(lds, C_KHELP, lane);
      lds_add(lds, C_DONE, 4u, lane);
      __builtin_amdgcn_s_setprio(3);
    }
    STAMP(3);
    const int fb = h * 16 + (s & 7) + ((s >> 4) << 3);
    if (!qwg && fb < 112) {
      const rsrc_t r_gu = mk_rsrc((wave & 1) ? a.wu : a.wg, D * FF * 2);
      const int frow = (fb / 28) * 112 + (wave >> 1) * 56 + (lane >> 3);
      const unsigned goff = (unsigned)frow * (FF * 2) + (fb % 28) * 128 + (lane & 7) * 16;
#pragma unroll
      for (int i = 0; i < 7; ++i) wv[i] = __builtin_amdgcn_raw_buffer_load_b128(r_gu, goff, i * 8 * FF * 2, AUX_NT);
      float sg[8] = {0.f, 0.f, 0.f, 0.f, 0.f, 0.f, 0.f, 0.f};
#pragma unroll
      for (int i = 0; i < 7; ++i) fma8(sg, *reinterpret_cast<const float*>(lds + L_XN + (frow + 8 * i) * 4), wv[i]);
#pragma unroll
      for (int i = 0; i < 8; ++i) { float t = sg[i]; t += dpp<XOR8>(t); sg[i] = x32_sum(x16_sum(t)); }
      if (lane < 8) {
        f32x4* p = reinterpret_cast<f32x4*>(lds + L_RED + wave * 256 + lane * 32);
        p[0] = f32x4{sg[0], sg[1], sg[2], sg[3]}; p[1] = f32x4{sg[4], sg[5], sg[6], sg[7]};
      }
      lds_arrive(lds, C_PART, lane);
      if (wave == 1) {
        lds_wait_ge(lds, C_PART, 4u);
        const int o = ((lane >> 2) & 1) * 256 + (lane >> 3) * 32 + (lane & 3) * 8;
        const f32x2 t0 = *reinterpret_cast<const f32x2*>(lds + L_RED + o), t1 = *reinterpret_cast<const f32x2*>(lds + L_RED + 512 + o);
        __builtin_amdgcn_raw_buffer_store_b128(u32x4{__float_as_uint(t0[0] + t1[0]), tag, __float_as_uint(t0[1] + t1[1]), tag}, r_ws,
                                               (lane >> 3) * 512 + (lane & 7) * 16, WS_GU + (fb % 28) * 4096 + (fb / 28) * 128, AUX_SC1);
      }
    }
    {
      const rsrc_t r_wo = mk_rsrc(a.wo, NQ * D * 2);
      const unsigned coff = tid < D / 2 ? tid * 4 : OOB;
      const bool ffn = wg < FF / 8;
      const rsrc_t r_d = mk_rsrc(a.wd, ffn ? FF * D * 2 : 0);
      unsigned draw[8], wraw[16];
#pragma unroll
      for (int j = 0; j < 8; ++j) draw[j] = __builtin_amdgcn_raw_buffer_load_b32(r_d, coff, (wg * 8 + j) * (D * 2), AUX_NT);
#pragma unroll
      for (int j = 0; j < 16; ++j) wraw[j] = __builtin_amdgcn_raw_buffer_load_b32(r_wo, coff, (wg * 16 + j) * (D * 2), AUX_NT);
#pragma unroll
      for (int j = 0; j < 8; ++j) { wop0[j] = __builtin_amdgcn_perm(wraw[2 * j + 1], wraw[2 * j], 0x05040100u); wop1[j] = __builtin_amdgcn_perm(wraw[2 * j + 1], wraw[2 * j], 0x07060302u); }
      if (wave == 3) {
        sleep_until<32>(t_start, GU_AT);
        u32x4 gu[1];
        const unsigned go[1] = {(ffn && lane < 32) ? (unsigned)lane * 16 : OOB};
        if (!gather<1, 64>(gu, r_ws, go, WS_GU + wg * 512, tag)) timed_out = true;
        float g0 = __uint_as_float(gu[0][0]), g1 = __uint_as_float(gu[0][2]);
        g0 += dpp<XOR8>(g0); g1 += dpp<XOR8>(g1);
        g0 = rnd_bf16(x16_sum(g0)); g1 = rnd_bf16(x16_sum(g1));
        const float u0 = dpp<ROR4>(g0), u1 = dpp<ROR4>(g1);
        const float h0 = rnd_bf16((g0 > 0.f ? g0 : g0 * 0.f) * u0), h1 = rnd_bf16((g1 > 0.f ? g1 : g1 * 0.f) * u1);
        if (lane < 4) *reinterpret_cast<f32x2*>(lds + L_H + lane * 8) = f32x2{h0, h1};
        lds_arrive(lds, C_HRDY, lane);
      }
      if (wave == 1) { lds_wait_ge<2>(lds, C_DONE, CP_REF); t_ref = now(); n_ref = lds_ld(lds, C_DONE) & 0xffffu; }
      lds_wait_ge<32>(lds, C_HRDY, 1u);
#pragma unroll
      for (int j = 0; j < 8; ++j) {
        const float hv = *reinterpret_cast<const float*>(lds + L_H + j * 4);
        y0 = fmaf(hv, bf_lo(draw[j]), y0); y1 = fmaf(hv, bf_hi(draw[j]), y1);
      }
      asm volatile("" : "+v"(y0), "+v"(y1), "+v"(wop0[0]), "+v"(wop0[1]), "+v"(wop0[2]), "+v"(wop0[3]), "+v"(wop0[4]), "+v"(wop0[5]), "+v"(wop0[6]), "+v"(wop0[7]),
                        "+v"(wop1[0]), "+v"(wop1[1]), "+v"(wop1[2]), "+v"(wop1[3]), "+v"(wop1[4]), "+v"(wop1[5]), "+v"(wop1[6]), "+v"(wop1[7]));
    }
    STAMP(11);
    if (wave == 2) {
      lds_wait_ge<32>(lds, C_DONE, 0x10000u * 4u + PREWARM_AT);
      unsigned t = 0;
      if (PREWARM & 1) t ^= __builtin_amdgcn_raw_buffer_load_b32(r_ws, lane < 32 ? lane * 128 : OOB, WS_PO + wg * 4096, AUX_SC1);
      if (PREWARM & 2) {
#pragma unroll
        for (int k = 0; k < CPR / 4; ++k) t ^= __builtin_amdgcn_raw_buffer_load_b32(r_ws, wg < NRED ? lane * 128 + k * 8192 : OOB, WS_YA + wg * (256 * CPR * 8), AUX_SC1);
      }
      if (PREWARM & 8) t ^= __builtin_amdgcn_raw_buffer_load_b32(r_ws, lane < 28 ? lane * 128 : OOB, WS_YA + wg * (28 * 128), AUX_SC1);
      asm volatile("" :: "v"(t));
    }
    if (wave == 1) {
      lds_wait_ge<2>(lds, C_DONE, 0x10000u * 4u + CP_AT);
      const unsigned t_now = now(), n_cp = lds_ld(lds, C_DONE) & 0xffffu;
      const float tau = (float)(t_now - t_ref) / (float)(n_cp - n_ref);
      const unsigned t_cp = t_now - (unsigned)(tau * (float)(n_cp - CP_AT));
      const int hd = lane & 1;
      float mw[4], lw[4];
#pragma unroll
      for (int w = 0; w < 4; ++w) { const f32x2 t = *reinterpret_cast<const f32x2*>(lds + L_ML + w * 16 + hd * 8); mw[w] = t[0]; lw[w] = t[1]; }
      const float M = fmaxf(fmaxf(mw[0], mw[1]), fmaxf(mw[2], mw[3]));
      float L = 0.f;
#pragma unroll
      for (int w = 0; w < 4; ++w) L = fmaf(__builtin_amdgcn_exp2f((mw[w] - M) * C2), lw[w], L);
      __builtin_amdgcn_raw_buffer_store_b128(u32x4{(lane & 2) ? t_cp : __float_as_uint(M), tag, __float_as_uint((lane & 2) ? tau : L), tag}, r_ws,
                                             lane < 8 ? lane * 16 : OOB, WS_ML + wg * LSTR, AUX_SC1);
      sleep_until<16>(t_now, CP_WAIT);
      u32x4 ml[2];
      const unsigned mo[2] = {(lane >> 2) * LSTR + (lane & 3) * 16, (16 + (lane >> 2)) * LSTR + (lane & 3) * 16};
      if (!gather<2, 32>(ml, r_ws, mo, WS_ML + h * 32 * LSTR, tag)) timed_out = true;
      const float Ma = __uint_as_float(ml[0][0]), Mb = __uint_as_float(ml[1][0]);
      float Mg = fmaxf(Ma, Mb);
      Mg = fmaxf(Mg, dpp<ROR4>(Mg)); Mg = fmaxf(Mg, dpp<XOR8>(Mg)); Mg = x32_max(x16_max(Mg));
      float Lg = __builtin_amdgcn_exp2f((Ma - Mg) * C2) * __uint_as_float(ml[0][2]) + __builtin_amdgcn_exp2f((Mb - Mg) * C2) * __uint_as_float(ml[1][2]);
      if (lane & 2) Lg = (lane & 1) ? __uint_as_float(ml[0][2]) + __uint_as_float(ml[1][2]) : (float)(int)(ml[0][0] - t_cp) + (float)(int)(ml[1][0] - t_cp);
      Lg += dpp<ROR4>(Lg); Lg += dpp<XOR8>(Lg); Lg = x32_sum(x16_sum(Lg));
      if (lane < 2) {
#pragma unroll
        for (int w = 0; w < 4; ++w) *reinterpret_cast<float*>(lds + L_SC + w * 8 + lane * 4) = timed_out ? __builtin_nanf("") : __builtin_amdgcn_exp2f((mw[w] - Mg) * C2) / Lg;
      }
      {
        const float tm = dpp<XOR1>(Lg) * (CP_TAU / 32);
        if (lane == 2 && !timed_out)
          *reinterpret_cast<u32x2*>(lds + C_BASE) = u32x2{t_cp + (unsigned)(int)(Lg * (1.0f / 32) + (float)CP_OFF - tm * (float)CP_AT), (unsigned)(tm * 256.f)};
      }
      STAMP(14);
    }
  } else {
    const int w = wave - 4;
    const rsrc_t r_v = mk_rsrc(a.v, 8u * SK * DH * 2);
    const unsigned vv0 = g * 2048 + piece(0, c >> 2) * 64 + (c & 3) * 16, vv1 = g * 2048 + piece(1, c >> 2) * 64 + (c & 3) * 16;
    const unsigned vk0 = (c >> 2) * 2048 + piece(0, g) * 64 + (c & 3) * 16, vk1 = (c >> 2) * 2048 + piece(1, g) * 64 + (c & 3) * 16;
    const unsigned t0 = wg_off + w * TILE;
    u32x4 ra[8], rb[8];
    __builtin_amdgcn_s_setprio(ST_PRIO);
    u32x4 rx[NI - 2][8];
    if (s & 8) issue<AUX_K>(ra, r_k, vw0, vw1, t0);
    lds_barrier();
    if (!(s & 8)) issue<AUX_K>(ra, r_k, vw0, vw1, t0);
#define CHAIN_STEP(R, SLOT) do { \
      constexpr bool first_ = CH_W < 5;     \
      if (first_ && lds_ld(lds, C_QRDY) == 0u) { asm volatile("s_waitcnt vmcnt(0)" ::: "memory"); lds_arrive(lds, C_LANDED, lane); PROGL(); } \
      chain_wait(lds, 4 * (SLOT) + w); \
      issue<AUX_K>(R, r_k, vw0, vw1, t0 + 4 * (SLOT) * TILE); \
      if (!first_ && lds_ld(lds, C_QRDY) == 0u) { asm volatile("s_waitcnt vmcnt(8)" ::: "memory"); lds_arrive(lds, C_LANDED, lane); PROGL(); } } while (0)
    CHAIN_STEP(rb, 1);
#pragma unroll
    for (int i = 2; i < NI; ++i) CHAIN_STEP(rx[i - 2], i);
#undef CHAIN_STEP
    if (lds_ld(lds, C_QRDY) == 0u) { asm volatile("s_waitcnt vmcnt(0)" ::: "memory"); lds_arrive(lds, C_LANDED, lane); PROGL(); }
    lds_wait_ge(lds, C_QRDY, 1u);
    tag = lds_ld(lds, C_TAG);
    STAMP(2);
    u32x4 qf[2];
    load_qf<true>(qf, lds, c, g);
    unsigned char* const pl = lds + L_P + w * 2048;
    {
      f32x4 d[4];
      unsigned short* sw = reinterpret_cast<unsigned short*>(pl + sc_lane);
      unsigned toff = t0 + 4 * (NI + 3) * TILE;
#define K_STEP(R, ISSUE_NEXT, SLOT) do { qk_mfma(R, qf, d); PROG(); ISSUE_NEXT; sw[(SLOT) * 32] = qk_score(d, b0, b1); } while (0)
      K_STEP(ra, issue<AUX_K>(ra, r_k, vk0, vk1, toff), 0);
      K_STEP(rb, issue<AUX_K>(rb, r_k, vk0, vk1, toff + 4 * TILE), 1);
#pragma unroll
      for (int i = 2; i < NI; ++i) K_STEP(rx[i - 2], (void)0, i);
      lds_add(lds, C_DONE, NI, lane);
      load_qf<false>(qf, lds, c, g);
      sw += (NI + 3) * 32;
      for (int it = 1; it < (29 - NI) / 2; ++it) {
        toff += 8 * TILE;
        K_STEP(ra, issue<AUX_K>(ra, r_k, vk0, vk1, toff), 0);
        K_STEP(rb, issue<AUX_K>(rb, r_k, vk0, vk1, toff + 4 * TILE), 1);
        lds_add(lds, C_DONE, 2u, lane);
        sw += 2 * 32;
      }
      K_STEP(ra, issue<AUX_V>(ra, r_v, vv0, vv1, t0), 0);
      K_STEP(rb, issue<AUX_V>(rb, r_v, vv0, vv1, t0 + 4 * TILE), 1);
#undef K_STEP
    }
    STAMP(12);
    lds_wait_ge(lds, C_KHELP, 3u);
    {
      u32x4* pp = reinterpret_cast<u32x4*>(pl + lane * 32);
      u32x4 t[2] = {pp[0], pp[1]};
      float mx = bf_lo(t[0][0]);
#pragma unroll
      for (int j = 0; j < 2; ++j)
#pragma unroll
        for (int i = 0; i < 4; ++i) mx = fmaxf(mx, fmaxf(bf_lo(t[j][i]), bf_hi(t[j][i])));
      mx = parity_max(mx);
      const float mc = mx * C2;
      float lp = 0.f;
#pragma unroll
      for (int j = 0; j < 2; ++j) {
#pragma unroll
        for (int i = 0; i < 4; ++i) {
          const float p0 = __builtin_amdgcn_exp2f(fmaf(bf_lo(t[j][i]), C2, -mc)), p1 = __builtin_amdgcn_exp2f(fmaf(bf_hi(t[j][i]), C2, -mc));
          lp += p0 + p1; t[j][i] = pk_bf16(p0, p1);
        }
        pp[j] = t[j];
      }
      lp = parity_sum(lp);
      if (lane < 2) *reinterpret_cast<f32x2*>(lds + L_ML + w * 16 + lane * 8) = f32x2{mx, lp};
      asm volatile("s_waitcnt lgkmcnt(0)" ::: "memory");
      lds_add(lds, C_DONE, 0x10002u, lane);
    }
    f32x4 acc0 = {0.f, 0.f, 0.f, 0.f}, acc1 = {0.f, 0.f, 0.f, 0.f};
    {
      const int msel = c >> 1;
      const unsigned char* pr = pl + (c & 1) * 32 + g * 8;
      const unsigned vf0 = g * 2048 + c * 16;
      float* const rec = reinterpret_cast<float*>(lds + L_ACC + w * 2048) + 8 * (c & 3) + g;
      auto put = [&](bool whole) {
        const int e0 = (whole ? c >> 2 : piece(0, c >> 2)) * 32, e1 = (whole ? 4 + (c >> 2) : piece(1, c >> 2)) * 32;
#pragma unroll
        for (int k = 0; k < 4; ++k) { float* r = rec + (k & 1) * 256 + ((k >> 1) ? e1 : e0); r[0] = acc0[k]; r[4] = acc1[k]; }
      };
      auto get = [&](bool whole) {
        const int e0 = (whole ? c >> 2 : piece(0, c >> 2)) * 32, e1 = (whole ? 4 + (c >> 2) : piece(1, c >> 2)) * 32;
#pragma unroll
        for (int k = 0; k < 4; ++k) { const float* r = rec + (k & 1) * 256 + ((k >> 1) ? e1 : e0); acc0[k] = r[0]; acc1[k] = r[4]; }
      };
      unsigned toff = t0 + 8 * TILE;
      bool cur = false;
#define V_STEP(R, ISSUE_NEXT) do { const u32x2 pk_ = *reinterpret_cast<const u32x2*>(pr); pr += 64; pv_mfma(R, pk_, msel, acc0, acc1); PROG(); ISSUE_NEXT; } while (0)
      for (int it = 0; it < 15; ++it) {
        u32x4 cw;
#pragma nounroll
        for (unsigned spin = 0; spin < SPIN_CAP; ++spin) {
          FENCE();
          cw = *reinterpret_cast<const u32x4*>(lds + C_DONE);
          if ((__builtin_amdgcn_readfirstlane(cw[0]) & 0xffffu) + SLACK >= (unsigned)(128 + 8 * it)) break;
          __builtin_amdgcn_s_sleep(2);
        }
        const unsigned done = __builtin_amdgcn_readfirstlane(cw[0]) & 0xffffu, tau = __builtin_amdgcn_readfirstlane(cw[3]);
        const bool nxt = tau != 0u && (int)(now() - __builtin_amdgcn_readfirstlane(cw[2]) - ((done * tau) >> 8)) > 0;
        const unsigned n0 = nxt ? vf0 : vv0, n1 = nxt ? vf0 + 256 : vv1;
        V_STEP(ra, issue<AUX_V>(ra, r_v, n0, n1, toff));
        V_STEP(rb, issue<AUX_V>(rb, r_v, n0, n1, toff + 4 * TILE));
        lds_add(lds, C_DONE, 2u, lane);
        if (nxt != cur) { put(cur); FENCE(); get(nxt); cur = nxt; }
        FENCE();
        toff += 8 * TILE;
      }
      V_STEP(ra, (void)0);
      V_STEP(rb, (void)0);
#undef V_STEP
      STAMP(3);
      put(cur);
    }
#pragma unroll
    for (int j = 0; j < 8; ++j) { wop0[j] = 0u; wop1[j] = 0u; }
  }
  PROG_OUT();
  STAMP(4);
  lds_barrier();
  STAMP(5);
  {
    float O = 0.f;
#pragma unroll
    for (int w = 0; w < 4; ++w)
      O = fmaf(*reinterpret_cast<const float*>(lds + L_SC + w * 8 + (tid >> 8) * 4), *reinterpret_cast<const float*>(lds + L_ACC + w * 2048 + tid * 4), O);
    __builtin_amdgcn_raw_buffer_store_b64(u32x2{__float_as_uint(O), tag}, r_ws, (tid >> 4) * 4096 + (tid & 15) * 8, WS_PO + h * (32 * 4096) + s * 128, AUX_ST);
  }
  STAMP(6);
  if (wave < 4) {
    u32x4 po[4];
    const unsigned oo[4] = {(unsigned)lane * 16, (unsigned)lane * 16 + 1024, (unsigned)lane * 16 + 2048, (unsigned)lane * 16 + 3072};
    if (!gather<4, 0>(po, r_ws, oo, WS_PO + wg * 4096, tag)) timed_out = true;
    STAMP(7);
    float o0 = (__uint_as_float(po[0][0]) + __uint_as_float(po[1][0])) + (__uint_as_float(po[2][0]) + __uint_as_float(po[3][0]));
    float o1 = (__uint_as_float(po[0][2]) + __uint_as_float(po[1][2])) + (__uint_as_float(po[2][2]) + __uint_as_float(po[3][2]));
    o0 += dpp<XOR8>(o0); o1 += dpp<XOR8>(o1);
    o0 = x32_sum(x16_sum(o0)); o1 = x32_sum(x16_sum(o1));
    const unsigned opk = pk_bf16(o0, o1);
#pragma unroll
    for (int j = 0; j < 8; ++j) { const unsigned oj = __builtin_amdgcn_readlane(opk, j); y0 = dot2(wop0[j], oj, y0); y1 = dot2(wop1[j], oj, y1); }
    if (timed_out) y0 = __builtin_nanf("");
    const unsigned yoff = tid < D / 2 ? (unsigned)((2 * tid / CPR) * (256 * CPR) + (2 * tid % CPR)) * 8 : OOB;
    __builtin_amdgcn_raw_buffer_store_b128(u32x4{__float_as_uint(y0), tag, __float_as_uint(y1), tag}, r_ws, yoff, WS_YA + wg * (CPR * 8), AUX_ST);
    STAMP(8);
    if (wave == 0) lds_arrive(lds, C_HOP1, lane);
  }
  if (wg >= NRED) return;
  lds_wait_ge(lds, C_HOP1, 1u);
  {
    constexpr int NLD = (256 * CPR / 2 + NTHR - 1) / NTHR;
    const float xo = tid < CPR ? bf_lo(a.x[wg * CPR + tid]) : 0.f;
    u32x4 t[NLD];
    unsigned yo[NLD];
#pragma unroll
    for (int k = 0; k < NLD; ++k) yo[k] = tid + NTHR * k < 256 * CPR / 2 ? (unsigned)(tid + NTHR * k) * 16 : OOB;
    if (!gather<NLD, 0>(t, r_ws, yo, WS_YA + wg * (256 * CPR * 8), tag)) timed_out = true;
    STAMP(9);
    float s0 = 0.f, s1 = 0.f;
#pragma unroll
    for (int k = 0; k < NLD; ++k) { s0 += __uint_as_float(t[k][0]); s1 += __uint_as_float(t[k][2]); }
    if (CPR / 2 <= 8) { s0 += dpp<XOR8>(s0); s1 += dpp<XOR8>(s1); }
    if (CPR / 2 <= 4) { s0 += dpp<ROR4>(s0); s1 += dpp<ROR4>(s1); }
    if (CPR / 2 <= 2) { s0 += dpp<XOR2>(s0); s1 += dpp<XOR2>(s1); }
    if (CPR / 2 <= 1) { s0 += dpp<XOR1>(s0); s1 += dpp<XOR1>(s1); }
    s0 = x32_sum(x16_sum(s0)); s1 = x32_sum(x16_sum(s1));
    if (timed_out) s0 = __builtin_nanf("");
    if (lane < CPR / 2) *reinterpret_cast<f32x2*>(lds + L_YA + wave * 64 + lane * 8) = f32x2{s0, s1};
    lds_barrier();
    if (tid < CPR) {
      float r = xo;
#pragma unroll
      for (int w = 0; w < 8; ++w) r += *reinterpret_cast<const float*>(lds + L_YA + w * 64 + tid * 4);
#define AUX_OUT 17
      __builtin_amdgcn_raw_buffer_store_b16((unsigned short)(pk_bf16(r, r) & 0xffffu), mk_rsrc(a.out, D * 2), (wg * CPR + tid) * 2, 0, AUX_OUT);
    }
    if (wg == 0 && tid == 64) __builtin_amdgcn_raw_buffer_store_b32(tag, r_ws, 0, WS_EPOCH, AUX_SC1);
    STAMP(10);
  }
}

}

extern "C" void kernel_launch(void* const* d_in, const int* in_sizes, int n_in, void* d_out, int out_size, void* d_ws, size_t ws_size,
                              hipStream_t stream) {
  (void)in_sizes; (void)n_in; (void)out_size; (void)ws_size;
  Args a;
  a.x = (const unsigned short*)d_in[0];
  a.k = (const unsigned char*)d_in[1];
  a.v = (const unsigned char*)d_in[2];
  a.wq = (const unsigned char*)d_in[3];
  a.wo = (const unsigned char*)d_in[4];
  a.wg = (const unsigned char*)d_in[6];
  a.wu = (const unsigned char*)d_in[7];
  a.wd = (const unsigned char*)d_in[8];
  a.out = (unsigned short*)d_out;
  a.ws = (unsigned char*)d_ws;
  (void)hipFuncSetAttribute((const void*)fused_decode, hipFuncAttributeMaxDynamicSharedMemorySize, LDS_BYTES);
  hipLaunchKernelGGL(fused_decode, dim3(NWG), dim3(NTHR), LDS_BYTES, stream, a);
}
```

```cpp
#include <hip/hip_runtime.h>
#include <hip/hip_bf16.h>
#include <cstdint>

namespace {

typedef unsigned u32x4 __attribute__((ext_vector_type(4)));
typedef unsigned u32x2 __attribute__((ext_vector_type(2)));
typedef float f32x4 __attribute__((ext_vector_type(4)));
typedef float f32x2 __attribute__((ext_vector_type(2)));
typedef __bf16 bf16x8 __attribute__((ext_vector_type(8)));
typedef __bf16 bf16x2 __attribute__((ext_vector_type(2)));
typedef __amdgpu_buffer_rsrc_t rsrc_t;

constexpr int D = 448, SK = 65536, DH = 256, FF = 1792, NQ = 4096;
constexpr int NWG = 256, NTHR = 512;
#define CH_W 10
#define NI 5
#define HK_PRIO 2
#define CP_REF 64
#define CP_AT 144
#define CP_WAIT 300
#define CP_OFF 30
#define CP_TAU 1.005f
#define RB_EARLY 1
#define ROWS_EARLY 1
#define PREWARM 2
#define PREWARM_AT 240
#define AUX_ST 16
#define AUX_LD 16
#define SLACK 4
#define Q_FIRST 700
#define Q_SLEEP 0
#define GU_AT 1500
#define CPR 8
constexpr int NRED = D / CPR;
constexpr unsigned TILE = 8192;
constexpr unsigned OOB = 0x40000000u;
constexpr float C2 = 0.0625f * 1.4426950408889634f;
constexpr unsigned SPIN_CAP = 1u << 22;

constexpr unsigned WS_EPOCH = 0;
constexpr unsigned LSTR = 4352;
constexpr unsigned WS_PO = 4096;
constexpr unsigned WS_YA = WS_PO + 8 * 32 * 32 * 16 * 8;
constexpr unsigned WS_GU = WS_YA + 256 * D * 8;
constexpr unsigned WS_QG = WS_GU + 224 * 4 * 128;
constexpr unsigned WS_ML = WS_QG + 128 * LSTR;
constexpr unsigned WS_END = WS_ML + 256 * LSTR;

constexpr int L_RED = 0;
constexpr int L_CTL = 1024;
constexpr int C_PART = L_CTL, C_QRDY = L_CTL + 4, C_TAG = L_CTL + 12, C_LANDED = L_CTL + 16, C_KHELP = L_CTL + 20, C_HRDY = L_CTL + 24, C_HOP1 = L_CTL + 28,
              C_DONE = L_CTL + 32, C_BASE = L_CTL + 40;
constexpr int L_ML = 1088;
constexpr int L_SC = 1152;
constexpr int L_H = 1184;
constexpr int L_YA = 1216;
constexpr int L_Q = 1792;
constexpr int L_XN = 2816;
constexpr int L_ACC = 4864;
constexpr int L_P = L_ACC + 4 * 2048;
#define LDS_KB 44
constexpr int LDS_BYTES = LDS_KB * 1024;

constexpr int AUX_NT = 2, AUX_SC1 = 16;
#define AUX_K 2
#define AUX_V 2
#define SB() __builtin_amdgcn_sched_barrier(0)
#define FENCE() asm volatile("" ::: "memory")

__device__ __forceinline__ float bf_lo(unsigned u) { return __builtin_bit_cast(float, u << 16); }
__device__ __forceinline__ float bf_hi(unsigned u) { return __builtin_bit_cast(float, u & 0xffff0000u); }
__device__ __forceinline__ unsigned pk_bf16(float lo, float hi) {
  bf16x2 v = {(__bf16)lo, (__bf16)hi};
  return __builtin_bit_cast(unsigned, v);
}
__device__ __forceinline__ float rnd_bf16(float x) { return (float)(__bf16)x; }
__device__ __forceinline__ float dot2(unsigned a, unsigned b, float acc) {
  return __builtin_amdgcn_fdot2_f32_bf16(__builtin_bit_cast(bf16x2, a), __builtin_bit_cast(bf16x2, b), acc, false);
}
template <int CTRL> __device__ __forceinline__ float dpp(float x) {
  return __builtin_bit_cast(float, __builtin_amdgcn_mov_dpp(__builtin_bit_cast(int, x), CTRL, 0xf, 0xf, true));
}
constexpr int XOR1 = 0xB1, XOR2 = 0x4E, XOR7 = 0x141, XOR15 = 0x140, XOR8 = 0x128, ROR4 = 0x124;
__device__ __forceinline__ float x16_max(float x) {
  auto s = __builtin_amdgcn_permlane16_swap(__float_as_uint(x), __float_as_uint(x), false, false);
  return fmaxf(__uint_as_float(s[0]), __uint_as_float(s[1]));
}
__device__ __forceinline__ float x32_max(float x) {
  auto s = __builtin_amdgcn_permlane32_swap(__float_as_uint(x), __float_as_uint(x), false, false);
  return fmaxf(__uint_as_float(s[0]), __uint_as_float(s[1]));
}
__device__ __forceinline__ float x16_sum(float x) {
  auto s = __builtin_amdgcn_permlane16_swap(__float_as_uint(x), __float_as_uint(x), false, false);
  return __uint_as_float(s[0]) + __uint_as_float(s[1]);
}
__device__ __forceinline__ float x32_sum(float x) {
  auto s = __builtin_amdgcn_permlane32_swap(__float_as_uint(x), __float_as_uint(x), false, false);
  return __uint_as_float(s[0]) + __uint_as_float(s[1]);
}
__device__ __forceinline__ float wave_sum(float x) {
  x += dpp<XOR1>(x); x += dpp<XOR2>(x); x += dpp<XOR7>(x); x += dpp<XOR15>(x);
  return x32_sum(x16_sum(x));
}
__device__ __forceinline__ float parity_sum(float x) {
  x += dpp<XOR2>(x); x += dpp<ROR4>(x); x += dpp<XOR8>(x);
  return x32_sum(x16_sum(x));
}
__device__ __forceinline__ float parity_max(float x) {
  x = fmaxf(x, dpp<XOR2>(x)); x = fmaxf(x, dpp<ROR4>(x)); x = fmaxf(x, dpp<XOR8>(x));
  return x32_max(x16_max(x));
}
__device__ __forceinline__ void lds_barrier() {
  asm volatile("s_waitcnt lgkmcnt(0)" ::: "memory");
  __builtin_amdgcn_s_barrier();
  FENCE();
}
__device__ __forceinline__ rsrc_t mk_rsrc(const void* p, unsigned bytes) {
  return __builtin_amdgcn_make_buffer_rsrc(const_cast<void*>(p), 0, bytes, 0x00020000);
}
__device__ __forceinline__ unsigned lds_ld(unsigned char* lds, int off) {
  return __builtin_amdgcn_readfirstlane(__hip_atomic_load(reinterpret_cast<unsigned*>(lds + off), __ATOMIC_RELAXED, __HIP_MEMORY_SCOPE_WORKGROUP));
}
template <int SLEEP = 1>
__device__ __forceinline__ void lds_wait_ge(unsigned char* lds, int off, unsigned want) {
#pragma nounroll
  for (unsigned spin = 0; spin < SPIN_CAP * 8u; ++spin) {
    if (lds_ld(lds, off) >= want) break;
    __builtin_amdgcn_s_sleep(SLEEP);
  }
  FENCE();
}
__device__ __forceinline__ void lds_arrive(unsigned char* lds, int off, int lane) {
  asm volatile("s_waitcnt lgkmcnt(0)" ::: "memory");
  if (lane == 0) __hip_atomic_fetch_add(reinterpret_cast<unsigned*>(lds + off), 1u, __ATOMIC_RELAXED, __HIP_MEMORY_SCOPE_WORKGROUP);
}
__device__ __forceinline__ void lds_add(unsigned char* lds, int off, unsigned v, int lane) {
  if (lane == 0) __hip_atomic_fetch_add(reinterpret_cast<unsigned*>(lds + off), v, __ATOMIC_RELAXED, __HIP_MEMORY_SCOPE_WORKGROUP);
}
__device__ __forceinline__ unsigned now() { return (unsigned)__builtin_amdgcn_s_memrealtime(); }
template <int SLEEP>
__device__ __forceinline__ void sleep_until(unsigned t0, unsigned ticks) {
#pragma nounroll
  for (unsigned spin = 0; spin < SPIN_CAP && now() - t0 < ticks; ++spin) __builtin_amdgcn_s_sleep(SLEEP);
}
__device__ __forceinline__ void chain_wait(unsigned char* lds, int idx) {
#pragma nounroll
  for (unsigned spin = 0; spin < SPIN_CAP * 8u; ++spin) {
    if ((int)lds_ld(lds, C_LANDED) >= idx + 1 - CH_W || lds_ld(lds, C_QRDY) != 0u) break;
    __builtin_amdgcn_s_sleep(1);
  }
  FENCE();
}
template <int N, int SLEEP>
__device__ __forceinline__ bool gather(u32x4 (&v)[N], rsrc_t rs, const unsigned (&voff)[N], unsigned soff, unsigned tag) {
  bool have[N];
#pragma unroll
  for (int k = 0; k < N; ++k) { have[k] = voff[k] == OOB; v[k] = u32x4{0u, 0u, 0u, 0u}; }
#pragma nounroll
  for (unsigned spin = 0; spin < SPIN_CAP; ++spin) {
    FENCE();
    u32x4 t[N];
#pragma unroll
    for (int k = 0; k < N; ++k) t[k] = __builtin_amdgcn_raw_buffer_load_b128(rs, have[k] ? OOB : voff[k], soff, AUX_LD);
    bool all = true;
#pragma unroll
    for (int k = 0; k < N; ++k) {
      const bool now = !have[k] && t[k][1] == tag && t[k][3] == tag;
      v[k][0] = now ? t[k][0] : v[k][0]; v[k][2] = now ? t[k][2] : v[k][2];
      have[k] = have[k] || now; all = all && have[k];
    }
    if (__builtin_amdgcn_ballot_w64(!all) == 0) return true;
    if (SLEEP > 0) __builtin_amdgcn_s_sleep(SLEEP);
  }
  return false;
}
#define STAMP(slot) do {} while (0)
#define PROG() do {} while (0)
#define PROG_OUT() do {} while (0)
#define PROGL() do {} while (0)

struct Args {
  const unsigned short* x;
  const unsigned char* k;
  const unsigned char* v;
  const unsigned char* wq;
  const unsigned char* wo;
  const unsigned char* wg;
  const unsigned char* wu;
  const unsigned char* wd;
  unsigned short* out;
  unsigned char* ws;
};

__host__ __device__ constexpr int piece(int e, int n) { return n < 2 ? 2 * e + n : 2 * n + e; }
template <int AUX>
__device__ __forceinline__ void issue(u32x4 (&r)[8], rsrc_t rs, unsigned v0, unsigned v1, unsigned soff) {
#pragma unroll
  for (int i = 0; i < 8; ++i) { r[i] = __builtin_amdgcn_raw_buffer_load_b128(rs, ((i & 1) ? v1 : v0) + (i >> 1) * 512, soff, AUX); SB(); }
}
__device__ __forceinline__ f32x4 mfma(u32x4 x, u32x4 y, f32x4 acc) {
  return __builtin_amdgcn_mfma_f32_16x16x32_bf16(__builtin_bit_cast(bf16x8, x), __builtin_bit_cast(bf16x8, y), acc, 0, 0, 0);
}
__device__ __forceinline__ void qk_mfma(const u32x4 (&r)[8], const u32x4 (&qf)[2], f32x4 (&d)[4]) {
#pragma unroll
  for (int j = 0; j < 4; ++j) d[j] = mfma(qf[1], r[2 * j + 1], mfma(qf[0], r[2 * j], f32x4{0.f, 0.f, 0.f, 0.f}));
  SB();
}
__device__ __forceinline__ unsigned short qk_score(const f32x4 (&d)[4], bool b0, bool b1) {
  float v[4];
#pragma unroll
  for (int j = 0; j < 4; ++j) {
    float e0 = d[j][0], e1 = d[j][1], e2 = d[j][2], e3 = d[j][3];
    asm("" : "+v"(e0), "+v"(e2));
    const float lo = b0 ? e1 : e0, hi = b0 ? e3 : e2;
    v[j] = b1 ? hi : lo;
  }
  const float u0 = (b0 ? v[1] : v[0]) + dpp<XOR1>(b0 ? v[0] : v[1]);
  const float u1 = (b0 ? v[3] : v[2]) + dpp<XOR1>(b0 ? v[2] : v[3]);
  const float w = (b1 ? u1 : u0) + dpp<XOR2>(b1 ? u0 : u1);
  return (unsigned short)(pk_bf16(w, w) & 0xffffu);
}
__device__ __forceinline__ void pv_mfma(const u32x4 (&vr)[8], u32x2 pk, int msel, f32x4& acc0, f32x4& acc1) {
#pragma unroll
  for (int q = 0; q < 4; ++q) {
    const u32x4 pa = {msel == 2 * q ? pk[0] : 0u, msel == 2 * q ? pk[1] : 0u, msel == 2 * q + 1 ? pk[0] : 0u, msel == 2 * q + 1 ? pk[1] : 0u};
    const unsigned sel = (q & 1) ? 0x07060302u : 0x05040100u;
    const int d0 = q >> 1, d1 = 2 + (q >> 1);
    const u32x4 b0 = {__builtin_amdgcn_perm(vr[2][d0], vr[0][d0], sel), __builtin_amdgcn_perm(vr[6][d0], vr[4][d0], sel),
                      __builtin_amdgcn_perm(vr[3][d0], vr[1][d0], sel), __builtin_amdgcn_perm(vr[7][d0], vr[5][d0], sel)};
    const u32x4 b1 = {__builtin_amdgcn_perm(vr[2][d1], vr[0][d1], sel), __builtin_amdgcn_perm(vr[6][d1], vr[4][d1], sel),
                      __builtin_amdgcn_perm(vr[3][d1], vr[1][d1], sel), __builtin_amdgcn_perm(vr[7][d1], vr[5][d1], sel)};
    acc0 = mfma(pa, b0, acc0);
    acc1 = mfma(pa, b1, acc1);
  }
  SB();
}
__device__ __forceinline__ void fma8(float (&s)[8], float xv, u32x4 w) {
#pragma unroll
  for (int i = 0; i < 4; ++i) { s[2 * i] = fmaf(xv, bf_lo(w[i]), s[2 * i]); s[2 * i + 1] = fmaf(xv, bf_hi(w[i]), s[2 * i + 1]); }
}
template <bool WHOLE>
__device__ __forceinline__ void load_qf(u32x4 (&qf)[2], const unsigned char* lds, int c, int g) {
#pragma unroll
  for (int e = 0; e < 2; ++e) qf[e] = *reinterpret_cast<const u32x4*>(lds + L_Q + ((c >> 2) & 1) * 512 + ((WHOLE ? 4 * e + g : piece(e, g)) * 4 + (c & 3)) * 16);
}

__global__ void __launch_bounds__(NTHR, 2) fused_decode(Args a) {
  extern __shared__ __attribute__((aligned(16))) unsigned char lds[];
  const int lane0 = threadIdx.x & 63, wave = __builtin_amdgcn_readfirstlane(threadIdx.x >> 6);
  const int wg = blockIdx.x, h = wg >> 5, s = wg & 31;
  const bool helper = wave < 4;
  const unsigned wg_off = (unsigned)wg * (128 * TILE);
  const unsigned vw0 = (lane0 & 12) * 512 + (lane0 >> 4) * 64 + (lane0 & 3) * 16, vw1 = vw0 + 256;
  const rsrc_t r_k = mk_rsrc(a.k, 8u * SK * DH * 2);
  const rsrc_t r_ws = mk_rsrc(a.ws, WS_END);
#define LANE_VARS int tid = threadIdx.x; asm volatile("" : "+v"(tid)); const int lane = tid & 63, c = lane & 15, g = lane >> 4; (void)c; (void)g
#define SCORE_VARS const bool b0 = (c & 1) != 0, b1 = (c & 2) != 0; const int sc_lane = (g & 1) * 32 + c * 2
#define STAMP0() do {} while (0)
  bool timed_out = false;
  unsigned tag;
  unsigned wop0[8], wop1[8];
  float y0 = 0.f, y1 = 0.f;

  if (helper) {
    __builtin_amdgcn_s_setprio(3);
    LANE_VARS;
    const bool qwg = (s & 8) == 0;
    const int qsl = (s & 7) * 2 + (s >> 4);
    const int fb = h * 16 + qsl;
    const int wrow = (lane >> (qwg ? 2 : 3)) + (qwg ? 16 * wave : (fb & 3) * 112 + (wave >> 1) * 56), wstep = qwg ? 64 : 8;
    const u32x4 xv = __builtin_amdgcn_raw_buffer_load_b128(mk_rsrc(a.x, D * 2), lane * 16, 0, 0);
    const unsigned epoch = __builtin_amdgcn_raw_buffer_load_b32(r_ws, 0, WS_EPOCH, AUX_SC1);
    u32x4 wv[7];
    {
      const unsigned rowb = qwg ? NQ * 2 : FF * 2;
      const rsrc_t r0 = mk_rsrc(qwg ? a.wq : (wave & 1) ? a.wu : a.wg, (qwg || fb < 112) ? D * rowb : 0u);
      const unsigned v0 = wrow * rowb + (lane & (qwg ? 3 : 7)) * 16, s0 = qwg ? (h * 512 + qsl * 32) * 2 : (fb >> 2) * 128;
#pragma unroll
      for (int i = 0; i < 7; ++i) wv[i] = __builtin_amdgcn_raw_buffer_load_b128(r0, v0, s0 + i * wstep * rowb, AUX_NT);
    }
    const unsigned t_start = now();
    unsigned t_ref = 0, n_ref = 0;
    SB();
    STAMP0();
    SCORE_VARS;
    if (tid < 16) *reinterpret_cast<unsigned*>(lds + L_CTL + tid * 4) = 0u;
    lds_barrier();

    {
      float xs[8];
#pragma unroll
      for (int i = 0; i < 4; ++i) { xs[2 * i] = bf_lo(xv[i]); xs[2 * i + 1] = bf_hi(xv[i]); }
      float sm = 0.f;
#pragma unroll
      for (int i = 0; i < 8; ++i) sm += xs[i];
      const float mean = wave_sum(sm) * (1.0f / D);
      float sq = 0.f;
#pragma unroll
      for (int i = 0; i < 8; ++i) { const float d = xs[i] - mean; sq = fmaf(d, d, sq); }
      sq = lane < D / 8 ? sq : 0.f;
      const float var = wave_sum(sq) * (1.0f / D);
      const float mean_b = rnd_bf16(mean);
      const float den_b = rnd_bf16(sqrtf(rnd_bf16(rnd_bf16(var) + 1e-5f)));
#pragma unroll
      for (int i = 0; i < 8; ++i) xs[i] = rnd_bf16(rnd_bf16(xs[i] - mean_b) / den_b);
      f32x4* xp = reinterpret_cast<f32x4*>(lds + L_XN + lane * 32);
      xp[0] = f32x4{xs[0], xs[1], xs[2], xs[3]}; xp[1] = f32x4{xs[4], xs[5], xs[6], xs[7]};
      FENCE();
    }
    tag = __builtin_amdgcn_readfirstlane(epoch) + 1u;
    {
      float sacc[8] = {0.f, 0.f, 0.f, 0.f, 0.f, 0.f, 0.f, 0.f};
#pragma unroll
      for (int i = 0; i < 7; ++i) fma8(sacc, *reinterpret_cast<const float*>(lds + L_XN + (wrow + wstep * i) * 4), wv[i]);
#pragma unroll
      for (int i = 0; i < 8; ++i) { float t = sacc[i]; const float r = dpp<ROR4>(t); t += qwg ? r : 0.f; t += dpp<XOR8>(t); sacc[i] = x32_sum(x16_sum(t)); }
      if (lane < 8) {
        f32x4* p = reinterpret_cast<f32x4*>(lds + L_RED + wave * 256 + lane * 32);
        p[0] = f32x4{sacc[0], sacc[1], sacc[2], sacc[3]}; p[1] = f32x4{sacc[4], sacc[5], sacc[6], sacc[7]};
      }
      lds_arrive(lds, C_PART, lane);
      if (wave == 0) {
        lds_wait_ge(lds, C_PART, 4u);
        if (qwg) {
          float q0 = 0.f, q1 = 0.f;
#pragma unroll
          for (int w = 0; w < 4; ++w) { const f32x2 t = *reinterpret_cast<const f32x2*>(lds + L_RED + w * 256 + c * 8); q0 += t[0]; q1 += t[1]; }
          __builtin_amdgcn_raw_buffer_store_b64(u32x2{pk_bf16(q0, q1), tag}, r_ws, lane < 16 ? lane * 8 : OOB, WS_QG + (h * 16 + qsl) * LSTR, AUX_SC1);
        } else {
          const int o = ((lane >> 2) & 1) * 256 + (lane >> 3) * 32 + (lane & 3) * 8;
          const f32x2 t0 = *reinterpret_cast<const f32x2*>(lds + L_RED + o), t1 = *reinterpret_cast<const f32x2*>(lds + L_RED + 512 + o);
          __builtin_amdgcn_raw_buffer_store_b128(u32x4{__float_as_uint(t0[0] + t1[0]), tag, __float_as_uint(t0[1] + t1[1]), tag}, r_ws,
                                                 fb < 112 ? (lane >> 3) * 512 + (lane & 7) * 16 : OOB, WS_GU + (fb >> 2) * 4096 + (fb & 3) * 128, AUX_SC1);
        }
        STAMP(1);
      }
    }
    const bool ffn = wg < FF / 8;
    unsigned draw[8], wraw[16];
    auto load_rows = [&]() {
      const rsrc_t r_wo = mk_rsrc(a.wo, NQ * D * 2), r_d = mk_rsrc(a.wd, ffn ? FF * D * 2 : 0);
      const unsigned coff = tid < D / 2 ? tid * 4 : OOB;
#pragma unroll
      for (int j = 0; j < 8; ++j) draw[j] = __builtin_amdgcn_raw_buffer_load_b32(r_d, coff, (wg * 8 + j) * (D * 2), AUX_NT);
#pragma unroll
      for (int j = 0; j < 16; ++j) wraw[j] = __builtin_amdgcn_raw_buffer_load_b32(r_wo, coff, (wg * 16 + j) * (D * 2), AUX_NT);
      SB();
    };
    load_rows();
    if (wave == 0) {
      u32x4 qg[2];
      const unsigned qo[2] = {(lane >> 2) * LSTR + (lane & 3) * 32, (lane >> 2) * LSTR + (lane & 3) * 32 + 16};
      sleep_until<1>(t_start, Q_FIRST);
      timed_out = !gather<2, Q_SLEEP>(qg, r_ws, qo, WS_QG + h * 16 * LSTR, tag);
      *reinterpret_cast<u32x4*>(lds + L_Q + lane * 16) = u32x4{qg[0][0], qg[0][2], qg[1][0], qg[1][2]};
      if (lane == 0) *reinterpret_cast<unsigned*>(lds + C_TAG) = tag;
      lds_arrive(lds, C_QRDY, lane);
    }
    if (wave != 0) {
      __builtin_amdgcn_s_setprio(HK_PRIO);
      const int slot = NI + wave - 1;
      u32x4 kt[4][8];
#pragma unroll
      for (int i = 0; i < 4; ++i) { chain_wait(lds, 4 * slot + i); issue<AUX_K>(kt[i], r_k, vw0, vw1, wg_off + (4 * slot + i) * TILE); }
      asm volatile("s_waitcnt vmcnt(24)" ::: "memory"); lds_arrive(lds, C_LANDED, lane); PROGL();
      asm volatile("s_waitcnt vmcnt(16)" ::: "memory"); lds_arrive(lds, C_LANDED, lane); PROGL();
      asm volatile("s_waitcnt vmcnt(8)" ::: "memory"); lds_arrive(lds, C_LANDED, lane); PROGL();
      asm volatile("s_waitcnt vmcnt(0)" ::: "memory"); lds_arrive(lds, C_LANDED, lane); PROGL();
      lds_wait_ge(lds, C_QRDY, 1u);
      u32x4 qf[2];
      load_qf<true>(qf, lds, c, g);
#pragma unroll
      for (int i = 0; i < 4; ++i) {
        f32x4 d[4];
        qk_mfma(kt[i], qf, d);
        *reinterpret_cast<unsigned short*>(lds + L_P + i * 2048 + slot * 64 + sc_lane) = qk_score(d, b0, b1);
        PROG();
      }
      lds_arrive(lds, C_KHELP, lane);
      lds_add(lds, C_DONE, 4u, lane);
      __builtin_amdgcn_s_setprio(3);
    }
    STAMP(3);
    {
#pragma unroll
      for (int j = 0; j < 8; ++j) { wop0[j] = __builtin_amdgcn_perm(wraw[2 * j + 1], wraw[2 * j], 0x05040100u); wop1[j] = __builtin_amdgcn_perm(wraw[2 * j + 1], wraw[2 * j], 0x07060302u); }
      if (wave == 3) {
        sleep_until<32>(t_start, GU_AT);
        u32x4 gu[1];
        const unsigned go[1] = {(ffn && lane < 32) ? (unsigned)lane * 16 : OOB};
        if (!gather<1, 64>(gu, r_ws, go, WS_GU + wg * 512, tag)) timed_out = true;
        float g0 = __uint_as_float(gu[0][0]), g1 = __uint_as_float(gu[0][2]);
        g0 += dpp<XOR8>(g0); g1 += dpp<XOR8>(g1);
        g0 = rnd_bf16(x16_sum(g0)); g1 = rnd_bf16(x16_sum(g1));
        const float u0 = dpp<ROR4>(g0), u1 = dpp<ROR4>(g1);
        const float h0 = rnd_bf16((g0 > 0.f ? g0 : g0 * 0.f) * u0), h1 = rnd_bf16((g1 > 0.f ? g1 : g1 * 0.f) * u1);
        if (lane < 4) *reinterpret_cast<f32x2*>(lds + L_H + lane * 8) = f32x2{h0, h1};
        lds_arrive(lds, C_HRDY, lane);
      }
      if (wave == 1) { lds_wait_ge<2>(lds, C_DONE, CP_REF); t_ref = now(); n_ref = lds_ld(lds, C_DONE) & 0xffffu; }
      lds_wait_ge<32>(lds, C_HRDY, 1u);
#pragma unroll
      for (int j = 0; j < 8; ++j) {
        const float hv = *reinterpret_cast<const float*>(lds + L_H + j * 4);
        y0 = fmaf(hv, bf_lo(draw[j]), y0); y1 = fmaf(hv, bf_hi(draw[j]), y1);
      }
      asm volatile("" : "+v"(y0), "+v"(y1), "+v"(wop0[0]), "+v"(wop0[1]), "+v"(wop0[2]), "+v"(wop0[3]), "+v"(wop0[4]), "+v"(wop0[5]), "+v"(wop0[6]), "+v"(wop0[7]),
                        "+v"(wop1[0]), "+v"(wop1[1]), "+v"(wop1[2]), "+v"(wop1[3]), "+v"(wop1[4]), "+v"(wop1[5]), "+v"(wop1[6]), "+v"(wop1[7]));
    }
    STAMP(11);
    if (wave == 2) {
      lds_wait_ge<32>(lds, C_DONE, 0x10000u * 4u + PREWARM_AT);
      unsigned t = 0;
      if (PREWARM & 1) t ^= __builtin_amdgcn_raw_buffer_load_b32(r_ws, lane < 32 ? lane * 128 : OOB, WS_PO + wg * 4096, AUX_SC1);
      if (PREWARM & 2) {
#pragma unroll
        for (int k = 0; k < CPR / 4; ++k) t ^= __builtin_amdgcn_raw_buffer_load_b32(r_ws, wg < NRED ? lane * 128 + k * 8192 : OOB, WS_YA + wg * (256 * CPR * 8), AUX_SC1);
      }
      if (PREWARM & 16) t ^= __builtin_amdgcn_raw_buffer_load_b32(mk_rsrc(a.out, D * 2), (wg < NRED && lane == 0) ? wg * CPR * 2 : OOB, 0, AUX_SC1);
      if (PREWARM & 8) t ^= __builtin_amdgcn_raw_buffer_load_b32(r_ws, lane < 28 ? lane * 128 : OOB, WS_YA + wg * (28 * 128), AUX_SC1);
      asm volatile("" :: "v"(t));
    }
    if (wave == 1) {
      lds_wait_ge<2>(lds, C_DONE, 0x10000u * 4u + CP_AT);
      const unsigned t_now = now(), n_cp = lds_ld(lds, C_DONE) & 0xffffu;
      const float tau = (float)(t_now - t_ref) / (float)(n_cp - n_ref);
      const unsigned t_cp = t_now - (unsigned)(tau * (float)(n_cp - CP_AT));
      const int hd = lane & 1;
      float mw[4], lw[4];
#pragma unroll
      for (int w = 0; w < 4; ++w) { const f32x2 t = *reinterpret_cast<const f32x2*>(lds + L_ML + w * 16 + hd * 8); mw[w] = t[0]; lw[w] = t[1]; }
      const float M = fmaxf(fmaxf(mw[0], mw[1]), fmaxf(mw[2], mw[3]));
      float L = 0.f;
#pragma unroll
      for (int w = 0; w < 4; ++w) L = fmaf(__builtin_amdgcn_exp2f((mw[w] - M) * C2), lw[w], L);
      __builtin_amdgcn_raw_buffer_store_b128(u32x4{(lane & 2) ? t_cp : __float_as_uint(M), tag, __float_as_uint((lane & 2) ? tau : L), tag}, r_ws,
                                             lane < 8 ? lane * 16 : OOB, WS_ML + wg * LSTR, AUX_SC1);
      sleep_until<16>(t_now, CP_WAIT);
      u32x4 ml[2];
      const unsigned mo[2] = {(lane >> 2) * LSTR + (lane & 3) * 16, (16 + (lane >> 2)) * LSTR + (lane & 3) * 16};
      if (!gather<2, 32>(ml, r_ws, mo, WS_ML + h * 32 * LSTR, tag)) timed_out = true;
      const float Ma = __uint_as_float(ml[0][0]), Mb = __uint_as_float(ml[1][0]);
      float Mg = fmaxf(Ma, Mb);
      Mg = fmaxf(Mg, dpp<ROR4>(Mg)); Mg = fmaxf(Mg, dpp<XOR8>(Mg)); Mg = x32_max(x16_max(Mg));
      float Lg = __builtin_amdgcn_exp2f((Ma - Mg) * C2) * __uint_as_float(ml[0][2]) + __builtin_amdgcn_exp2f((Mb - Mg) * C2) * __uint_as_float(ml[1][2]);
      if (lane & 2) Lg = (lane & 1) ? __uint_as_float(ml[0][2]) + __uint_as_float(ml[1][2]) : (float)(int)(ml[0][0] - t_cp) + (float)(int)(ml[1][0] - t_cp);
      Lg += dpp<ROR4>(Lg); Lg += dpp<XOR8>(Lg); Lg = x32_sum(x16_sum(Lg));
      if (lane < 2) {
#pragma unroll
        for (int w = 0; w < 4; ++w) *reinterpret_cast<float*>(lds + L_SC + w * 8 + lane * 4) = timed_out ? __builtin_nanf("") : __builtin_amdgcn_exp2f((mw[w] - Mg) * C2) / Lg;
      }
      {
        const float tm = dpp<XOR1>(Lg) * (CP_TAU / 32);
        if (lane == 2 && !timed_out)
          *reinterpret_cast<u32x2*>(lds + C_BASE) = u32x2{t_cp + (unsigned)(int)(Lg * (1.0f / 32) + (float)CP_OFF - tm * (float)CP_AT), (unsigned)(tm * 256.f)};
      }
      STAMP(14);
    }
  } else {
    const int w = wave - 4;
    const unsigned t0 = wg_off + w * TILE;
    u32x4 ra[8], rb[8];
    issue<AUX_K>(ra, r_k, vw0, vw1, t0);
    issue<AUX_K>(rb, r_k, vw0, vw1, t0 + 4 * TILE);
    LANE_VARS;
    SCORE_VARS;
    STAMP0();
    const rsrc_t r_v = mk_rsrc(a.v, 8u * SK * DH * 2);
    const unsigned vv0 = g * 2048 + piece(0, c >> 2) * 64 + (c & 3) * 16, vv1 = g * 2048 + piece(1, c >> 2) * 64 + (c & 3) * 16;
    const unsigned vk0 = (c >> 2) * 2048 + piece(0, g) * 64 + (c & 3) * 16, vk1 = (c >> 2) * 2048 + piece(1, g) * 64 + (c & 3) * 16;
    u32x4 rx[NI - 2][8];
    static_assert(CH_W >= 8, "slots 0 and 1 go out at once");
    lds_barrier();
    asm volatile("s_waitcnt vmcnt(8)" ::: "memory"); lds_arrive(lds, C_LANDED, lane); PROGL();
#define CHAIN_STEP(R, SLOT) do { \
      constexpr bool first_ = CH_W < 5;     \
      if (first_ && lds_ld(lds, C_QRDY) == 0u) { asm volatile("s_waitcnt vmcnt(0)" ::: "memory"); lds_arrive(lds, C_LANDED, lane); PROGL(); } \
      chain_wait(lds, 4 * (SLOT) + w); \
      issue<AUX_K>(R, r_k, vw0, vw1, t0 + 4 * (SLOT) * TILE); \
      if (!first_ && lds_ld(lds, C_QRDY) == 0u) { asm volatile("s_waitcnt vmcnt(8)" ::: "memory"); lds_arrive(lds, C_LANDED, lane); PROGL(); } } while (0)
#pragma unroll
    for (int i = 2; i < NI; ++i) CHAIN_STEP(rx[i - 2], i);
#undef CHAIN_STEP
    if (lds_ld(lds, C_QRDY) == 0u) { asm volatile("s_waitcnt vmcnt(0)" ::: "memory"); lds_arrive(lds, C_LANDED, lane); PROGL(); }
    lds_wait_ge(lds, C_QRDY, 1u);
    tag = lds_ld(lds, C_TAG);
    STAMP(2);
    u32x4 qf[2];
    load_qf<true>(qf, lds, c, g);
    unsigned char* const pl = lds + L_P + w * 2048;
    {
      f32x4 d[4];
      unsigned short* sw = reinterpret_cast<unsigned short*>(pl + sc_lane);
      unsigned toff = t0 + 4 * (NI + 3) * TILE;
#define K_STEP(R, ISSUE_NEXT, SLOT) do { qk_mfma(R, qf, d); PROG(); ISSUE_NEXT; sw[(SLOT) * 32] = qk_score(d, b0, b1); } while (0)
      K_STEP(ra, issue<AUX_K>(ra, r_k, vk0, vk1, toff), 0);
      K_STEP(rb, issue<AUX_K>(rb, r_k, vk0, vk1, toff + 4 * TILE), 1);
#pragma unroll
      for (int i = 2; i < NI; ++i) K_STEP(rx[i - 2], (void)0, i);
      lds_add(lds, C_DONE, NI, lane);
      load_qf<false>(qf, lds, c, g);
      sw += (NI + 3) * 32;
      for (int it = 1; it < (29 - NI) / 2; ++it) {
        toff += 8 * TILE;
        K_STEP(ra, issue<AUX_K>(ra, r_k, vk0, vk1, toff), 0);
        K_STEP(rb, issue<AUX_K>(rb, r_k, vk0, vk1, toff + 4 * TILE), 1);
        lds_add(lds, C_DONE, 2u, lane);
        sw += 2 * 32;
      }
      K_STEP(ra, issue<AUX_V>(ra, r_v, vv0, vv1, t0), 0);
      K_STEP(rb, issue<AUX_V>(rb, r_v, vv0, vv1, t0 + 4 * TILE), 1);
#undef K_STEP
    }
    STAMP(12);
    lds_wait_ge(lds, C_KHELP, 3u);
    {
      u32x4* pp = reinterpret_cast<u32x4*>(pl + lane * 32);
      u32x4 t[2] = {pp[0], pp[1]};
      float mx = bf_lo(t[0][0]);
#pragma unroll
      for (int j = 0; j < 2; ++j)
#pragma unroll
        for (int i = 0; i < 4; ++i) mx = fmaxf(mx, fmaxf(bf_lo(t[j][i]), bf_hi(t[j][i])));
      mx = parity_max(mx);
      const float mc = mx * C2;
      float lp = 0.f;
#pragma unroll
      for (int j = 0; j < 2; ++j) {
#pragma unroll
        for (int i = 0; i < 4; ++i) {
          const float p0 = __builtin_amdgcn_exp2f(fmaf(bf_lo(t[j][i]), C2, -mc)), p1 = __builtin_amdgcn_exp2f(fmaf(bf_hi(t[j][i]), C2, -mc));
          lp += p0 + p1; t[j][i] = pk_bf16(p0, p1);
        }
        pp[j] = t[j];
      }
      lp = parity_sum(lp);
      if (lane < 2) *reinterpret_cast<f32x2*>(lds + L_ML + w * 16 + lane * 8) = f32x2{mx, lp};
      asm volatile("s_waitcnt lgkmcnt(0)" ::: "memory");
      lds_add(lds, C_DONE, 0x10002u, lane);
    }
    f32x4 acc0 = {0.f, 0.f, 0.f, 0.f}, acc1 = {0.f, 0.f, 0.f, 0.f};
    {
      const int msel = c >> 1;
      const unsigned char* pr = pl + (c & 1) * 32 + g * 8;
      const unsigned vf0 = g * 2048 + c * 16;
      float* const rec = reinterpret_cast<float*>(lds + L_ACC + w * 2048) + 8 * (c & 3) + g;
      auto put = [&](bool whole) {
        const int e0 = (whole ? c >> 2 : piece(0, c >> 2)) * 32, e1 = (whole ? 4 + (c >> 2) : piece(1, c >> 2)) * 32;
#pragma unroll
        for (int k = 0; k < 4; ++k) { float* r = rec + (k & 1) * 256 + ((k >> 1) ? e1 : e0); r[0] = acc0[k]; r[4] = acc1[k]; }
      };
      auto get = [&](bool whole) {
        const int e0 = (whole ? c >> 2 : piece(0, c >> 2)) * 32, e1 = (whole ? 4 + (c >> 2) : piece(1, c >> 2)) * 32;
#pragma unroll
        for (int k = 0; k < 4; ++k) { const float* r = rec + (k & 1) * 256 + ((k >> 1) ? e1 : e0); acc0[k] = r[0]; acc1[k] = r[4]; }
      };
      unsigned toff = t0 + 8 * TILE;
      bool cur = false;
#define V_STEP(R, ISSUE_NEXT) do { const u32x2 pk_ = *reinterpret_cast<const u32x2*>(pr); pr += 64; pv_mfma(R, pk_, msel, acc0, acc1); PROG(); ISSUE_NEXT; } while (0)
      for (int it = 0; it < 15; ++it) {
        u32x4 cw;
#pragma nounroll
        for (unsigned spin = 0; spin < SPIN_CAP; ++spin) {
          FENCE();
          cw = *reinterpret_cast<const u32x4*>(lds + C_DONE);
          if ((__builtin_amdgcn_readfirstlane(cw[0]) & 0xffffu) + SLACK >= (unsigned)(128 + 8 * it)) break;
          __builtin_amdgcn_s_sleep(2);
        }
        const unsigned done = __builtin_amdgcn_readfirstlane(cw[0]) & 0xffffu, tau = __builtin_amdgcn_readfirstlane(cw[3]);
        const bool nxt = tau != 0u && (int)(now() - __builtin_amdgcn_readfirstlane(cw[2]) - ((done * tau) >> 8)) > 0;
        const unsigned n0 = nxt ? vf0 : vv0, n1 = nxt ? vf0 + 256 : vv1;
        V_STEP(ra, issue<AUX_V>(ra, r_v, n0, n1, toff));
        V_STEP(rb, issue<AUX_V>(rb, r_v, n0, n1, toff + 4 * TILE));
        lds_add(lds, C_DONE, 2u, lane);
        if (nxt != cur) { put(cur); FENCE(); get(nxt); cur = nxt; }
        FENCE();
        toff += 8 * TILE;
      }
      V_STEP(ra, (void)0);
      V_STEP(rb, (void)0);
#undef V_STEP
      STAMP(3);
      put(cur);
    }
#pragma unroll
    for (int j = 0; j < 8; ++j) { wop0[j] = 0u; wop1[j] = 0u; }
  }
  LANE_VARS;
  PROG_OUT();
  STAMP(4);
  lds_barrier();
  STAMP(5);
  {
    float O = 0.f;
#pragma unroll
    for (int w = 0; w < 4; ++w)
      O = fmaf(*reinterpret_cast<const float*>(lds + L_SC + w * 8 + (tid >> 8) * 4), *reinterpret_cast<const float*>(lds + L_ACC + w * 2048 + tid * 4), O);
    __builtin_amdgcn_raw_buffer_store_b64(u32x2{__float_as_uint(O), tag}, r_ws, (tid >> 4) * 4096 + (tid & 15) * 8, WS_PO + h * (32 * 4096) + s * 128, AUX_ST);
  }
  STAMP(6);
  if (helper) {
    u32x4 po[4];
    const unsigned oo[4] = {(unsigned)lane * 16, (unsigned)lane * 16 + 1024, (unsigned)lane * 16 + 2048, (unsigned)lane * 16 + 3072};
    if (!gather<4, 0>(po, r_ws, oo, WS_PO + wg * 4096, tag)) timed_out = true;
    STAMP(7);
    float o0 = (__uint_as_float(po[0][0]) + __uint_as_float(po[1][0])) + (__uint_as_float(po[2][0]) + __uint_as_float(po[3][0]));
    float o1 = (__uint_as_float(po[0][2]) + __uint_as_float(po[1][2])) + (__uint_as_float(po[2][2]) + __uint_as_float(po[3][2]));
    o0 += dpp<XOR8>(o0); o1 += dpp<XOR8>(o1);
    o0 = x32_sum(x16_sum(o0)); o1 = x32_sum(x16_sum(o1));
    const unsigned opk = pk_bf16(o0, o1);
#pragma unroll
    for (int j = 0; j < 8; ++j) { const unsigned oj = __builtin_amdgcn_readlane(opk, j); y0 = dot2(wop0[j], oj, y0); y1 = dot2(wop1[j], oj, y1); }
    if (timed_out) y0 = __builtin_nanf("");
    const unsigned yoff = tid < D / 2 ? (unsigned)((2 * tid / CPR) * (256 * CPR) + (2 * tid % CPR)) * 8 : OOB;
    __builtin_amdgcn_raw_buffer_store_b128(u32x4{__float_as_uint(y0), tag, __float_as_uint(y1), tag}, r_ws, yoff, WS_YA + wg * (CPR * 8), AUX_ST);
    STAMP(8);
    if (wave == 0) lds_arrive(lds, C_HOP1, lane);
  }
  if (wg >= NRED) return;
  lds_wait_ge(lds, C_HOP1, 1u);
  {
    constexpr int NLD = (256 * CPR / 2 + NTHR - 1) / NTHR;
    const float xo = tid < CPR ? bf_lo(a.x[wg * CPR + tid]) : 0.f;
    u32x4 t[NLD];
    unsigned yo[NLD];
#pragma unroll
    for (int k = 0; k < NLD; ++k) yo[k] = tid + NTHR * k < 256 * CPR / 2 ? (unsigned)(tid + NTHR * k) * 16 : OOB;
    if (!gather<NLD, 0>(t, r_ws, yo, WS_YA + wg * (256 * CPR * 8), tag)) timed_out = true;
    STAMP(9);
    float s0 = 0.f, s1 = 0.f;
#pragma unroll
    for (int k = 0; k < NLD; ++k) { s0 += __uint_as_float(t[k][0]); s1 += __uint_as_float(t[k][2]); }
    if (CPR / 2 <= 8) { s0 += dpp<XOR8>(s0); s1 += dpp<XOR8>(s1); }
    if (CPR / 2 <= 4) { s0 += dpp<ROR4>(s0); s1 += dpp<ROR4>(s1); }
    if (CPR / 2 <= 2) { s0 += dpp<XOR2>(s0); s1 += dpp<XOR2>(s1); }
    if (CPR / 2 <= 1) { s0 += dpp<XOR1>(s0); s1 += dpp<XOR1>(s1); }
    s0 = x32_sum(x16_sum(s0)); s1 = x32_sum(x16_sum(s1));
    if (timed_out) s0 = __builtin_nanf("");
    if (lane < CPR / 2) *reinterpret_cast<f32x2*>(lds + L_YA + wave * 64 + lane * 8) = f32x2{s0, s1};
    lds_barrier();
    if (tid < CPR) {
      float r = xo;
#pragma unroll
      for (int w = 0; w < 8; ++w) r += *reinterpret_cast<const float*>(lds + L_YA + w * 64 + tid * 4);
#define AUX_OUT 17
      __builtin_amdgcn_raw_buffer_store_b16((unsigned short)(pk_bf16(r, r) & 0xffffu), mk_rsrc(a.out, D * 2), (wg * CPR + tid) * 2, 0, AUX_OUT);
    }
    if (wg == 0 && tid == 64) __builtin_amdgcn_raw_buffer_store_b32(tag, r_ws, 0, WS_EPOCH, AUX_SC1);
    STAMP(10);
  }
}

}

extern "C" void kernel_launch(void* const* d_in, const int* in_sizes, int n_in, void* d_out, int out_size, void* d_ws, size_t ws_size,
                              hipStream_t stream) {
  (void)in_sizes; (void)n_in; (void)out_size; (void)ws_size;
  Args a;
  a.x = (const unsigned short*)d_in[0];
  a.k = (const unsigned char*)d_in[1];
  a.v = (const unsigned char*)d_in[2];
  a.wq = (const unsigned char*)d_in[3];
  a.wo = (const unsigned char*)d_in[4];
  a.wg = (const unsigned char*)d_in[6];
  a.wu = (const unsigned char*)d_in[7];
  a.wd = (const unsigned char*)d_in[8];
  a.out = (unsigned short*)d_out;
  a.ws = (unsigned char*)d_ws;
  (void)hipFuncSetAttribute((const void*)fused_decode, hipFuncAttributeMaxDynamicSharedMemorySize, LDS_BYTES);
  hipLaunchKernelGGL(fused_decode, dim3(NWG), dim3(NTHR), LDS_BYTES, stream, a);
}
```

```cpp
#include <hip/hip_runtime.h>
#include <hip/hip_bf16.h>
#include <cstdint>

namespace {

typedef unsigned u32x4 __attribute__((ext_vector_type(4)));
typedef unsigned u32x2 __attribute__((ext_vector_type(2)));
typedef float f32x4 __attribute__((ext_vector_type(4)));
typedef float f32x2 __attribute__((ext_vector_type(2)));
typedef __bf16 bf16x8 __attribute__((ext_vector_type(8)));
typedef __bf16 bf16x2 __attribute__((ext_vector_type(2)));
typedef __amdgpu_buffer_rsrc_t rsrc_t;

constexpr int D = 448, SK = 65536, DH = 256, FF = 1792, NQ = 4096;
constexpr int NWG = 256, NTHR = 512;
#define CH_W 10
#define NI 5
#define HK_PRIO 2
#define CP_REF 64
#define CP_AT 144
#define CP_WAIT 300
#define CP_OFF 30
#define CP_TAU 1.005f
#define CODE_PF 0
#define CODE_WGS 32
#define ROWS_EARLY 1
#define PREWARM 2
#define PREWARM_AT 240
#define AUX_ST 16
#define AUX_LD 16
#define SLACK 4
#define Q_FIRST 700
#define Q_SLEEP 0
#define GU_AT 1500
#define CPR 8
constexpr int NRED = D / CPR;
constexpr unsigned TILE = 8192;
constexpr unsigned OOB = 0x40000000u;
constexpr float C2 = 0.0625f * 1.4426950408889634f;
constexpr unsigned SPIN_CAP = 1u << 22;

constexpr unsigned WS_EPOCH = 0;
constexpr unsigned LSTR = 4352;
constexpr unsigned WS_PO = 4096;
constexpr unsigned WS_YA = WS_PO + 8 * 32 * 32 * 16 * 8;
constexpr unsigned WS_GU = WS_YA + 256 * D * 8;
constexpr unsigned WS_QG = WS_GU + 224 * 4 * 128;
constexpr unsigned WS_ML = WS_QG + 128 * LSTR;
constexpr unsigned WS_END = WS_ML + 256 * LSTR;

constexpr int L_RED = 0;
constexpr int L_CTL = 1024;
constexpr int C_PART = L_CTL, C_QRDY = L_CTL + 4, C_TAG = L_CTL + 12, C_LANDED = L_CTL + 16, C_KHELP = L_CTL + 20, C_HRDY = L_CTL + 24, C_HOP1 = L_CTL + 28,
              C_DONE = L_CTL + 32, C_BASE = L_CTL + 40;
constexpr int L_ML = 1088;
constexpr int L_SC = 1152;
constexpr int L_H = 1184;
constexpr int L_YA = 1216;
constexpr int L_Q = 1792;
constexpr int L_XN = 2816;
constexpr int L_ACC = 4864;
constexpr int L_P = L_ACC + 4 * 2048;
#define LDS_KB 44
constexpr int LDS_BYTES = LDS_KB * 1024;

constexpr int AUX_NT = 2, AUX_SC1 = 16;
#define AUX_K 2
#define AUX_V 2
#define SB() __builtin_amdgcn_sched_barrier(0)
#define FENCE() asm volatile("" ::: "memory")

__device__ __forceinline__ float bf_lo(unsigned u) { return __builtin_bit_cast(float, u << 16); }
__device__ __forceinline__ float bf_hi(unsigned u) { return __builtin_bit_cast(float, u & 0xffff0000u); }
__device__ __forceinline__ unsigned pk_bf16(float lo, float hi) {
  bf16x2 v = {(__bf16)lo, (__bf16)hi};
  return __builtin_bit_cast(unsigned, v);
}
__device__ __forceinline__ float rnd_bf16(float x) { return (float)(__bf16)x; }
__device__ __forceinline__ float dot2(unsigned a, unsigned b, float acc) {
  return __builtin_amdgcn_fdot2_f32_bf16(__builtin_bit_cast(bf16x2, a), __builtin_bit_cast(bf16x2, b), acc, false);
}
template <int CTRL> __device__ __forceinline__ float dpp(float x) {
  return __builtin_bit_cast(float, __builtin_amdgcn_mov_dpp(__builtin_bit_cast(int, x), CTRL, 0xf, 0xf, true));
}
constexpr int XOR1 = 0xB1, XOR2 = 0x4E, XOR7 = 0x141, XOR15 = 0x140, XOR8 = 0x128, ROR4 = 0x124;
__device__ __forceinline__ float x16_max(float x) {
  auto s = __builtin_amdgcn_permlane16_swap(__float_as_uint(x), __float_as_uint(x), false, false);
  return fmaxf(__uint_as_float(s[0]), __uint_as_float(s[1]));
}
__device__ __forceinline__ float x32_max(float x) {
  auto s = __builtin_amdgcn_permlane32_swap(__float_as_uint(x), __float_as_uint(x), false, false);
  return fmaxf(__uint_as_float(s[0]), __uint_as_float(s[1]));
}
__device__ __forceinline__ float x16_sum(float x) {
  auto s = __builtin_amdgcn_permlane16_swap(__float_as_uint(x), __float_as_uint(x), false, false);
  return __uint_as_float(s[0]) + __uint_as_float(s[1]);
}
__device__ __forceinline__ float x32_sum(float x) {
  auto s = __builtin_amdgcn_permlane32_swap(__float_as_uint(x), __float_as_uint(x), false, false);
  return __uint_as_float(s[0]) + __uint_as_float(s[1]);
}
__device__ __forceinline__ float wave_sum(float x) {
  x += dpp<XOR1>(x); x += dpp<XOR2>(x); x += dpp<XOR7>(x); x += dpp<XOR15>(x);
  return x32_sum(x16_sum(x));
}
__device__ __forceinline__ float parity_sum(float x) {
  x += dpp<XOR2>(x); x += dpp<ROR4>(x); x += dpp<XOR8>(x);
  return x32_sum(x16_sum(x));
}
__device__ __forceinline__ float parity_max(float x) {
  x = fmaxf(x, dpp<XOR2>(x)); x = fmaxf(x, dpp<ROR4>(x)); x = fmaxf(x, dpp<XOR8>(x));
  return x32_max(x16_max(x));
}
__device__ __forceinline__ void lds_barrier() {
  asm volatile("s_waitcnt lgkmcnt(0)" ::: "memory");
  __builtin_amdgcn_s_barrier();
  FENCE();
}
__device__ __forceinline__ rsrc_t mk_rsrc(const void* p, unsigned bytes) {
  return __builtin_amdgcn_make_buffer_rsrc(const_cast<void*>(p), 0, bytes, 0x00020000);
}
__device__ __forceinline__ unsigned lds_ld(unsigned char* lds, int off) {
  return __builtin_amdgcn_readfirstlane(__hip_atomic_load(reinterpret_cast<unsigned*>(lds + off), __ATOMIC_RELAXED, __HIP_MEMORY_SCOPE_WORKGROUP));
}
template <int SLEEP = 1>
__device__ __forceinline__ void lds_wait_ge(unsigned char* lds, int off, unsigned want) {
#pragma nounroll
  for (unsigned spin = 0; spin < SPIN_CAP * 8u; ++spin) {
    if (lds_ld(lds, off) >= want) break;
    __builtin_amdgcn_s_sleep(SLEEP);
  }
  FENCE();
}
__device__ __forceinline__ void lds_arrive(unsigned char* lds, int off, int lane) {
  asm volatile("s_waitcnt lgkmcnt(0)" ::: "memory");
  if (lane == 0) __hip_atomic_fetch_add(reinterpret_cast<unsigned*>(lds + off), 1u, __ATOMIC_RELAXED, __HIP_MEMORY_SCOPE_WORKGROUP);
}
__device__ __forceinline__ void lds_add(unsigned char* lds, int off, unsigned v, int lane) {
  if (lane == 0) __hip_atomic_fetch_add(reinterpret_cast<unsigned*>(lds + off), v, __ATOMIC_RELAXED, __HIP_MEMORY_SCOPE_WORKGROUP);
}
__device__ __forceinline__ unsigned now() { return (unsigned)__builtin_amdgcn_s_memrealtime(); }
template <int SLEEP>
__device__ __forceinline__ void sleep_until(unsigned t0, unsigned ticks) {
#pragma nounroll
  for (unsigned spin = 0; spin < SPIN_CAP && now() - t0 < ticks; ++spin) __builtin_amdgcn_s_sleep(SLEEP);
}
__device__ __forceinline__ void chain_wait(unsigned char* lds, int idx) {
#pragma nounroll
  for (unsigned spin = 0; spin < SPIN_CAP * 8u; ++spin) {
    if ((int)lds_ld(lds, C_LANDED) >= idx + 1 - CH_W || lds_ld(lds, C_QRDY) != 0u) break;
    __builtin_amdgcn_s_sleep(1);
  }
  FENCE();
}
template <int N, int SLEEP>
__device__ __forceinline__ bool gather(u32x4 (&v)[N], rsrc_t rs, const unsigned (&voff)[N], unsigned soff, unsigned tag) {
  bool have[N];
#pragma unroll
  for (int k = 0; k < N; ++k) { have[k] = voff[k] == OOB; v[k] = u32x4{0u, 0u, 0u, 0u}; }
#pragma nounroll
  for (unsigned spin = 0; spin < SPIN_CAP; ++spin) {
    FENCE();
    u32x4 t[N];
#pragma unroll
    for (int k = 0; k < N; ++k) t[k] = __builtin_amdgcn_raw_buffer_load_b128(rs, have[k] ? OOB : voff[k], soff, AUX_LD);
    bool all = true;
#pragma unroll
    for (int k = 0; k < N; ++k) {
      const bool now = !have[k] && t[k][1] == tag && t[k][3] == tag;
      v[k][0] = now ? t[k][0] : v[k][0]; v[k][2] = now ? t[k][2] : v[k][2];
      have[k] = have[k] || now; all = all && have[k];
    }
    if (__builtin_amdgcn_ballot_w64(!all) == 0) return true;
    if (SLEEP > 0) __builtin_amdgcn_s_sleep(SLEEP);
  }
  return false;
}
#define STAMP(slot) do {} while (0)
#define PROG() do {} while (0)
#define PROG_OUT() do {} while (0)
#define PROGL() do {} while (0)

struct Args {
  const unsigned short* x;
  const unsigned char* k;
  const unsigned char* v;
  const unsigned char* wq;
  const unsigned char* wo;
  const unsigned char* wg;
  const unsigned char* wu;
  const unsigned char* wd;
  unsigned short* out;
  unsigned char* ws;
};

__host__ __device__ constexpr int piece(int e, int n) { return n < 2 ? 2 * e + n : 2 * n + e; }
template <int AUX>
__device__ __forceinline__ void issue(u32x4 (&r)[8], rsrc_t rs, unsigned v0, unsigned v1, unsigned soff) {
#pragma unroll
  for (int i = 0; i < 8; ++i) { r[i] = __builtin_amdgcn_raw_buffer_load_b128(rs, ((i & 1) ? v1 : v0) + (i >> 1) * 512, soff, AUX); SB(); }
}
__device__ __forceinline__ f32x4 mfma(u32x4 x, u32x4 y, f32x4 acc) {
  return __builtin_amdgcn_mfma_f32_16x16x32_bf16(__builtin_bit_cast(bf16x8, x), __builtin_bit_cast(bf16x8, y), acc, 0, 0, 0);
}
__device__ __forceinline__ void qk_mfma(const u32x4 (&r)[8], const u32x4 (&qf)[2], f32x4 (&d)[4]) {
#pragma unroll
  for (int j = 0; j < 4; ++j) d[j] = mfma(qf[1], r[2 * j + 1], mfma(qf[0], r[2 * j], f32x4{0.f, 0.f, 0.f, 0.f}));
  SB();
}
__device__ __forceinline__ unsigned short qk_score(const f32x4 (&d)[4], bool b0, bool b1) {
  float v[4];
#pragma unroll
  for (int j = 0; j < 4; ++j) {
    float e0 = d[j][0], e1 = d[j][1], e2 = d[j][2], e3 = d[j][3];
    asm("" : "+v"(e0), "+v"(e2));
    const float lo = b0 ? e1 : e0, hi = b0 ? e3 : e2;
    v[j] = b1 ? hi : lo;
  }
  const float u0 = (b0 ? v[1] : v[0]) + dpp<XOR1>(b0 ? v[0] : v[1]);
  const float u1 = (b0 ? v[3] : v[2]) + dpp<XOR1>(b0 ? v[2] : v[3]);
  const float w = (b1 ? u1 : u0) + dpp<XOR2>(b1 ? u0 : u1);
  return (unsigned short)(pk_bf16(w, w) & 0xffffu);
}
__device__ __forceinline__ void pv_mfma(const u32x4 (&vr)[8], u32x2 pk, int msel, f32x4& acc0, f32x4& acc1) {
#pragma unroll
  for (int q = 0; q < 4; ++q) {
    const u32x4 pa = {msel == 2 * q ? pk[0] : 0u, msel == 2 * q ? pk[1] : 0u, msel == 2 * q + 1 ? pk[0] : 0u, msel == 2 * q + 1 ? pk[1] : 0u};
    const unsigned sel = (q & 1) ? 0x07060302u : 0x05040100u;
    const int d0 = q >> 1, d1 = 2 + (q >> 1);
    const u32x4 b0 = {__builtin_amdgcn_perm(vr[2][d0], vr[0][d0], sel), __builtin_amdgcn_perm(vr[6][d0], vr[4][d0], sel),
                      __builtin_amdgcn_perm(vr[3][d0], vr[1][d0], sel), __builtin_amdgcn_perm(vr[7][d0], vr[5][d0], sel)};
    const u32x4 b1 = {__builtin_amdgcn_perm(vr[2][d1], vr[0][d1], sel), __builtin_amdgcn_perm(vr[6][d1], vr[4][d1], sel),
                      __builtin_amdgcn_perm(vr[3][d1], vr[1][d1], sel), __builtin_amdgcn_perm(vr[7][d1], vr[5][d1], sel)};
    acc0 = mfma(pa, b0, acc0);
    acc1 = mfma(pa, b1, acc1);
  }
  SB();
}
__device__ __forceinline__ void fma8(float (&s)[8], float xv, u32x4 w) {
#pragma unroll
  for (int i = 0; i < 4; ++i) { s[2 * i] = fmaf(xv, bf_lo(w[i]), s[2 * i]); s[2 * i + 1] = fmaf(xv, bf_hi(w[i]), s[2 * i + 1]); }
}
template <bool WHOLE>
__device__ __forceinline__ void load_qf(u32x4 (&qf)[2], const unsigned char* lds, int c, int g) {
#pragma unroll
  for (int e = 0; e < 2; ++e) qf[e] = *reinterpret_cast<const u32x4*>(lds + L_Q + ((c >> 2) & 1) * 512 + ((WHOLE ? 4 * e + g : piece(e, g)) * 4 + (c & 3)) * 16);
}

__global__ void __launch_bounds__(NTHR, 2) fused_decode(Args a) {
  extern __shared__ __attribute__((aligned(16))) unsigned char lds[];
  const int lane0 = threadIdx.x & 63, wave = __builtin_amdgcn_readfirstlane(threadIdx.x >> 6);
  const int wg = blockIdx.x, h = wg >> 5, s = wg & 31;
  const bool helper = wave < 4;
  const unsigned wg_off = (unsigned)wg * (128 * TILE);
  const unsigned vw0 = (lane0 & 12) * 512 + (lane0 >> 4) * 64 + (lane0 & 3) * 16, vw1 = vw0 + 256;
  const rsrc_t r_k = mk_rsrc(a.k, 8u * SK * DH * 2);
  u32x4 ra[8], rb[8];
#pragma unroll
  for (int i = 0; i < 8; ++i) asm volatile("" : "=v"(ra[i]), "=v"(rb[i]));
  if (!helper) {
    const unsigned t0 = wg_off + (wave - 4) * TILE;
    issue<AUX_K>(ra, r_k, vw0, vw1, t0);
    issue<AUX_K>(rb, r_k, vw0, vw1, t0 + 4 * TILE);
  }
  const rsrc_t r_ws = mk_rsrc(a.ws, WS_END);
#define LANE_VARS int tid = threadIdx.x; asm volatile("" : "+v"(tid)); const int lane = tid & 63, c = lane & 15, g = lane >> 4; (void)c; (void)g
#define SCORE_VARS const bool b0 = (c & 1) != 0, b1 = (c & 2) != 0; const int sc_lane = (g & 1) * 32 + c * 2
#define STAMP0() do {} while (0)
  bool timed_out = false;
  unsigned tag;
  unsigned wop0[8], wop1[8];
  float y0 = 0.f, y1 = 0.f;

  if (__builtin_expect(helper, 1)) {
    __builtin_amdgcn_s_setprio(3);
    LANE_VARS;
    const bool qwg = (s & 8) == 0;
    const int qsl = (s & 7) * 2 + (s >> 4);
    const int fb = h * 16 + qsl;
    const int wrow = (lane >> (qwg ? 2 : 3)) + (qwg ? 16 * wave : (fb & 3) * 112 + (wave >> 1) * 56), wstep = qwg ? 64 : 8;
    const u32x4 xv = __builtin_amdgcn_raw_buffer_load_b128(mk_rsrc(a.x, D * 2), lane * 16, 0, 0);
    const unsigned epoch = __builtin_amdgcn_raw_buffer_load_b32(r_ws, 0, WS_EPOCH, AUX_SC1);
    u32x4 wv[7];
    {
      const unsigned rowb = qwg ? NQ * 2 : FF * 2;
      const rsrc_t r0 = mk_rsrc(qwg ? a.wq : (wave & 1) ? a.wu : a.wg, (qwg || fb < 112) ? D * rowb : 0u);
      const unsigned v0 = wrow * rowb + (lane & (qwg ? 3 : 7)) * 16, s0 = qwg ? (h * 512 + qsl * 32) * 2 : (fb >> 2) * 128;
#pragma unroll
      for (int i = 0; i < 7; ++i) wv[i] = __builtin_amdgcn_raw_buffer_load_b128(r0, v0, s0 + i * wstep * rowb, AUX_NT);
    }
    const unsigned t_start = now();
    unsigned t_ref = 0, n_ref = 0;
    SB();
    STAMP0();
    SCORE_VARS;
    if (tid < 16) *reinterpret_cast<unsigned*>(lds + L_CTL + tid * 4) = 0u;
    lds_barrier();

    {
      float xs[8];
#pragma unroll
      for (int i = 0; i < 4; ++i) { xs[2 * i] = bf_lo(xv[i]); xs[2 * i + 1] = bf_hi(xv[i]); }
      float sm = 0.f;
#pragma unroll
      for (int i = 0; i < 8; ++i) sm += xs[i];
      const float mean = wave_sum(sm) * (1.0f / D);
      float sq = 0.f;
#pragma unroll
      for (int i = 0; i < 8; ++i) { const float d = xs[i] - mean; sq = fmaf(d, d, sq); }
      sq = lane < D / 8 ? sq : 0.f;
      const float var = wave_sum(sq) * (1.0f / D);
      const float mean_b = rnd_bf16(mean);
      const float den_b = rnd_bf16(__builtin_amdgcn_sqrtf(rnd_bf16(rnd_bf16(var) + 1e-5f)));
      float rcp = __builtin_amdgcn_rcpf(den_b);
      rcp = fmaf(fmaf(-den_b, rcp, 1.0f), rcp, rcp);
#pragma unroll
      for (int i = 0; i < 8; ++i) {
        const float n = rnd_bf16(xs[i] - mean_b), q = n * rcp;
        xs[i] = rnd_bf16(fmaf(fmaf(-den_b, q, n), rcp, q));
      }
      f32x4* xp = reinterpret_cast<f32x4*>(lds + L_XN + lane * 32);
      xp[0] = f32x4{xs[0], xs[1], xs[2], xs[3]}; xp[1] = f32x4{xs[4], xs[5], xs[6], xs[7]};
      FENCE();
    }
    tag = __builtin_amdgcn_readfirstlane(epoch) + 1u;
    {
      float sacc[8] = {0.f, 0.f, 0.f, 0.f, 0.f, 0.f, 0.f, 0.f};
#pragma unroll
      for (int i = 0; i < 7; ++i) fma8(sacc, *reinterpret_cast<const float*>(lds + L_XN + (wrow + wstep * i) * 4), wv[i]);
#pragma unroll
      for (int i = 0; i < 8; ++i) { float t = sacc[i]; const float r = dpp<ROR4>(t); t += qwg ? r : 0.f; t += dpp<XOR8>(t); sacc[i] = x32_sum(x16_sum(t)); }
      if (lane < 8) {
        f32x4* p = reinterpret_cast<f32x4*>(lds + L_RED + wave * 256 + lane * 32);
        p[0] = f32x4{sacc[0], sacc[1], sacc[2], sacc[3]}; p[1] = f32x4{sacc[4], sacc[5], sacc[6], sacc[7]};
      }
      lds_arrive(lds, C_PART, lane);
      if (wave == 0) {
        lds_wait_ge(lds, C_PART, 4u);
        if (qwg) {
          float q0 = 0.f, q1 = 0.f;
#pragma unroll
          for (int w = 0; w < 4; ++w) { const f32x2 t = *reinterpret_cast<const f32x2*>(lds + L_RED + w * 256 + c * 8); q0 += t[0]; q1 += t[1]; }
          __builtin_amdgcn_raw_buffer_store_b64(u32x2{pk_bf16(q0, q1), tag}, r_ws, lane < 16 ? lane * 8 : OOB, WS_QG + (h * 16 + qsl) * LSTR, AUX_SC1);
        } else {
          const int o = ((lane >> 2) & 1) * 256 + (lane >> 3) * 32 + (lane & 3) * 8;
          const f32x2 t0 = *reinterpret_cast<const f32x2*>(lds + L_RED + o), t1 = *reinterpret_cast<const f32x2*>(lds + L_RED + 512 + o);
          __builtin_amdgcn_raw_buffer_store_b128(u32x4{__float_as_uint(t0[0] + t1[0]), tag, __float_as_uint(t0[1] + t1[1]), tag}, r_ws,
                                                 fb < 112 ? (lane >> 3) * 512 + (lane & 7) * 16 : OOB, WS_GU + (fb >> 2) * 4096 + (fb & 3) * 128, AUX_SC1);
        }
        STAMP(1);
      }
    }
    const bool ffn = wg < FF / 8;
    unsigned draw[8], wraw[16];
    auto load_rows = [&]() {
      const rsrc_t r_wo = mk_rsrc(a.wo, NQ * D * 2), r_d = mk_rsrc(a.wd, ffn ? FF * D * 2 : 0);
      const unsigned coff = tid < D / 2 ? tid * 4 : OOB;
#pragma unroll
      for (int j = 0; j < 8; ++j) draw[j] = __builtin_amdgcn_raw_buffer_load_b32(r_d, coff, (wg * 8 + j) * (D * 2), AUX_NT);
#pragma unroll
      for (int j = 0; j < 16; ++j) wraw[j] = __builtin_amdgcn_raw_buffer_load_b32(r_wo, coff, (wg * 16 + j) * (D * 2), AUX_NT);
      SB();
    };
    load_rows();
    if (wave == 0) {
      u32x4 qg[2];
      const unsigned qo[2] = {(lane >> 2) * LSTR + (lane & 3) * 32, (lane >> 2) * LSTR + (lane & 3) * 32 + 16};
      sleep_until<1>(t_start, Q_FIRST);
      timed_out = !gather<2, Q_SLEEP>(qg, r_ws, qo, WS_QG + h * 16 * LSTR, tag);
      *reinterpret_cast<u32x4*>(lds + L_Q + lane * 16) = u32x4{qg[0][0], qg[0][2], qg[1][0], qg[1][2]};
      if (lane == 0) *reinterpret_cast<unsigned*>(lds + C_TAG) = tag;
      lds_arrive(lds, C_QRDY, lane);
    }
    if (wave != 0) {
      __builtin_amdgcn_s_setprio(HK_PRIO);
      const int slot = NI + wave - 1;
      u32x4 kt[4][8];
#pragma unroll
      for (int i = 0; i < 4; ++i) { chain_wait(lds, 4 * slot + i); issue<AUX_K>(kt[i], r_k, vw0, vw1, wg_off + (4 * slot + i) * TILE); }
      asm volatile("s_waitcnt vmcnt(24)" ::: "memory"); lds_arrive(lds, C_LANDED, lane); PROGL();
      asm volatile("s_waitcnt vmcnt(16)" ::: "memory"); lds_arrive(lds, C_LANDED, lane); PROGL();
      asm volatile("s_waitcnt vmcnt(8)" ::: "memory"); lds_arrive(lds, C_LANDED, lane); PROGL();
      asm volatile("s_waitcnt vmcnt(0)" ::: "memory"); lds_arrive(lds, C_LANDED, lane); PROGL();
      lds_wait_ge(lds, C_QRDY, 1u);
      u32x4 qf[2];
      load_qf<true>(qf, lds, c, g);
#pragma unroll
      for (int i = 0; i < 4; ++i) {
        f32x4 d[4];
        qk_mfma(kt[i], qf, d);
        *reinterpret_cast<unsigned short*>(lds + L_P + i * 2048 + slot * 64 + sc_lane) = qk_score(d, b0, b1);
        PROG();
      }
      lds_arrive(lds, C_KHELP, lane);
      lds_add(lds, C_DONE, 4u, lane);
      __builtin_amdgcn_s_setprio(3);
    }
    STAMP(3);
    {
#pragma unroll
      for (int j = 0; j < 8; ++j) { wop0[j] = __builtin_amdgcn_perm(wraw[2 * j + 1], wraw[2 * j], 0x05040100u); wop1[j] = __builtin_amdgcn_perm(wraw[2 * j + 1], wraw[2 * j], 0x07060302u); }
      if (wave == 3) {
        sleep_until<32>(t_start, GU_AT);
        u32x4 gu[1];
        const unsigned go[1] = {(ffn && lane < 32) ? (unsigned)lane * 16 : OOB};
        if (!gather<1, 64>(gu, r_ws, go, WS_GU + wg * 512, tag)) timed_out = true;
        float g0 = __uint_as_float(gu[0][0]), g1 = __uint_as_float(gu[0][2]);
        g0 += dpp<XOR8>(g0); g1 += dpp<XOR8>(g1);
        g0 = rnd_bf16(x16_sum(g0)); g1 = rnd_bf16(x16_sum(g1));
        const float u0 = dpp<ROR4>(g0), u1 = dpp<ROR4>(g1);
        const float h0 = rnd_bf16((g0 > 0.f ? g0 : g0 * 0.f) * u0), h1 = rnd_bf16((g1 > 0.f ? g1 : g1 * 0.f) * u1);
        if (lane < 4) *reinterpret_cast<f32x2*>(lds + L_H + lane * 8) = f32x2{h0, h1};
        lds_arrive(lds, C_HRDY, lane);
      }
      if (wave == 1) { lds_wait_ge<2>(lds, C_DONE, CP_REF); t_ref = now(); n_ref = lds_ld(lds, C_DONE) & 0xffffu; }
      lds_wait_ge<32>(lds, C_HRDY, 1u);
#pragma unroll
      for (int j = 0; j < 8; ++j) {
        const float hv = *reinterpret_cast<const float*>(lds + L_H + j * 4);
        y0 = fmaf(hv, bf_lo(draw[j]), y0); y1 = fmaf(hv, bf_hi(draw[j]), y1);
      }
      asm volatile("" : "+v"(y0), "+v"(y1), "+v"(wop0[0]), "+v"(wop0[1]), "+v"(wop0[2]), "+v"(wop0[3]), "+v"(wop0[4]), "+v"(wop0[5]), "+v"(wop0[6]), "+v"(wop0[7]),
                        "+v"(wop1[0]), "+v"(wop1[1]), "+v"(wop1[2]), "+v"(wop1[3]), "+v"(wop1[4]), "+v"(wop1[5]), "+v"(wop1[6]), "+v"(wop1[7]));
    }
    STAMP(11);
    if (wave == 2) {
      lds_wait_ge<32>(lds, C_DONE, 0x10000u * 4u + PREWARM_AT);
      unsigned t = 0;
      if (PREWARM & 1) t ^= __builtin_amdgcn_raw_buffer_load_b32(r_ws, lane < 32 ? lane * 128 : OOB, WS_PO + wg * 4096, AUX_SC1);
      if (PREWARM & 2) {
#pragma unroll
        for (int k = 0; k < CPR / 4; ++k) t ^= __builtin_amdgcn_raw_buffer_load_b32(r_ws, wg < NRED ? lane * 128 + k * 8192 : OOB, WS_YA + wg * (256 * CPR * 8), AUX_SC1);
      }
      if (PREWARM & 16) t ^= __builtin_amdgcn_raw_buffer_load_b32(mk_rsrc(a.out, D * 2), (wg < NRED && lane == 0) ? wg * CPR * 2 : OOB, 0, AUX_SC1);
      if (PREWARM & 8) t ^= __builtin_amdgcn_raw_buffer_load_b32(r_ws, lane < 28 ? lane * 128 : OOB, WS_YA + wg * (28 * 128), AUX_SC1);
      asm volatile("" :: "v"(t));
    }
    if (wave == 1) {
      lds_wait_ge<2>(lds, C_DONE, 0x10000u * 4u + CP_AT);
      const unsigned t_now = now(), n_cp = lds_ld(lds, C_DONE) & 0xffffu;
      const float tau = (float)(t_now - t_ref) / (float)(n_cp - n_ref);
      const unsigned t_cp = t_now - (unsigned)(tau * (float)(n_cp - CP_AT));
      const int hd = lane & 1;
      float mw[4], lw[4];
#pragma unroll
      for (int w = 0; w < 4; ++w) { const f32x2 t = *reinterpret_cast<const f32x2*>(lds + L_ML + w * 16 + hd * 8); mw[w] = t[0]; lw[w] = t[1]; }
      const float M = fmaxf(fmaxf(mw[0], mw[1]), fmaxf(mw[2], mw[3]));
      float L = 0.f;
#pragma unroll
      for (int w = 0; w < 4; ++w) L = fmaf(__builtin_amdgcn_exp2f((mw[w] - M) * C2), lw[w], L);
      __builtin_amdgcn_raw_buffer_store_b128(u32x4{(lane & 2) ? t_cp : __float_as_uint(M), tag, __float_as_uint((lane & 2) ? tau : L), tag}, r_ws,
                                             lane < 8 ? lane * 16 : OOB, WS_ML + wg * LSTR, AUX_SC1);
      sleep_until<16>(t_now, CP_WAIT);
      u32x4 ml[2];
      const unsigned mo[2] = {(lane >> 2) * LSTR + (lane & 3) * 16, (16 + (lane >> 2)) * LSTR + (lane & 3) * 16};
      if (!gather<2, 32>(ml, r_ws, mo, WS_ML + h * 32 * LSTR, tag)) timed_out = true;
      const float Ma = __uint_as_float(ml[0][0]), Mb = __uint_as_float(ml[1][0]);
      float Mg = fmaxf(Ma, Mb);
      Mg = fmaxf(Mg, dpp<ROR4>(Mg)); Mg = fmaxf(Mg, dpp<XOR8>(Mg)); Mg = x32_max(x16_max(Mg));
      float Lg = __builtin_amdgcn_exp2f((Ma - Mg) * C2) * __uint_as_float(ml[0][2]) + __builtin_amdgcn_exp2f((Mb - Mg) * C2) * __uint_as_float(ml[1][2]);
      if (lane & 2) Lg = (lane & 1) ? __uint_as_float(ml[0][2]) + __uint_as_float(ml[1][2]) : (float)(int)(ml[0][0] - t_cp) + (float)(int)(ml[1][0] - t_cp);
      Lg += dpp<ROR4>(Lg); Lg += dpp<XOR8>(Lg); Lg = x32_sum(x16_sum(Lg));
      if (lane < 2) {
#pragma unroll
        for (int w = 0; w < 4; ++w) *reinterpret_cast<float*>(lds + L_SC + w * 8 + lane * 4) = timed_out ? __builtin_nanf("") : __builtin_amdgcn_exp2f((mw[w] - Mg) * C2) / Lg;
      }
      {
        const float tm = dpp<XOR1>(Lg) * (CP_TAU / 32);
        if (lane == 2 && !timed_out)
          *reinterpret_cast<u32x2*>(lds + C_BASE) = u32x2{t_cp + (unsigned)(int)(Lg * (1.0f / 32) + (float)CP_OFF - tm * (float)CP_AT), (unsigned)(tm * 256.f)};
      }
      STAMP(14);
    }
  } else {
    const int w = wave - 4;
    const unsigned t0 = wg_off + w * TILE;
    LANE_VARS;
    SCORE_VARS;
    STAMP0();
    const rsrc_t r_v = mk_rsrc(a.v, 8u * SK * DH * 2);
    const unsigned vv0 = g * 2048 + piece(0, c >> 2) * 64 + (c & 3) * 16, vv1 = g * 2048 + piece(1, c >> 2) * 64 + (c & 3) * 16;
    const unsigned vk0 = (c >> 2) * 2048 + piece(0, g) * 64 + (c & 3) * 16, vk1 = (c >> 2) * 2048 + piece(1, g) * 64 + (c & 3) * 16;
    u32x4 rx[NI - 2][8];
    static_assert(CH_W >= 8, "slots 0 and 1 go out at once");
    lds_barrier();
    asm volatile("s_waitcnt vmcnt(8)" ::: "memory"); lds_arrive(lds, C_LANDED, lane); PROGL();
#define CHAIN_STEP(R, SLOT) do { \
      constexpr bool first_ = CH_W < 5;     \
      if (first_ && lds_ld(lds, C_QRDY) == 0u) { asm volatile("s_waitcnt vmcnt(0)" ::: "memory"); lds_arrive(lds, C_LANDED, lane); PROGL(); } \
      chain_wait(lds, 4 * (SLOT) + w); \
      issue<AUX_K>(R, r_k, vw0, vw1, t0 + 4 * (SLOT) * TILE); \
      if (!first_ && lds_ld(lds, C_QRDY) == 0u) { asm volatile("s_waitcnt vmcnt(8)" ::: "memory"); lds_arrive(lds, C_LANDED, lane); PROGL(); } } while (0)
#pragma unroll
    for (int i = 2; i < NI; ++i) CHAIN_STEP(rx[i - 2], i);
#undef CHAIN_STEP
    if (lds_ld(lds, C_QRDY) == 0u) { asm volatile("s_waitcnt vmcnt(0)" ::: "memory"); lds_arrive(lds, C_LANDED, lane); PROGL(); }
    lds_wait_ge(lds, C_QRDY, 1u);
    tag = lds_ld(lds, C_TAG);
    STAMP(2);
    u32x4 qf[2];
    load_qf<true>(qf, lds, c, g);
    unsigned char* const pl = lds + L_P + w * 2048;
    {
      f32x4 d[4];
      unsigned short* sw = reinterpret_cast<unsigned short*>(pl + sc_lane);
      unsigned toff = t0 + 4 * (NI + 3) * TILE;
#define K_STEP(R, ISSUE_NEXT, SLOT) do { qk_mfma(R, qf, d); PROG(); ISSUE_NEXT; sw[(SLOT) * 32] = qk_score(d, b0, b1); } while (0)
      K_STEP(ra, issue<AUX_K>(ra, r_k, vk0, vk1, toff), 0);
      K_STEP(rb, issue<AUX_K>(rb, r_k, vk0, vk1, toff + 4 * TILE), 1);
#pragma unroll
      for (int i = 2; i < NI; ++i) K_STEP(rx[i - 2], (void)0, i);
      lds_add(lds, C_DONE, NI, lane);
      load_qf<false>(qf, lds, c, g);
      sw += (NI + 3) * 32;
      for (int it = 1; it < (29 - NI) / 2; ++it) {
        toff += 8 * TILE;
        K_STEP(ra, issue<AUX_K>(ra, r_k, vk0, vk1, toff), 0);
        K_STEP(rb, issue<AUX_K>(rb, r_k, vk0, vk1, toff + 4 * TILE), 1);
        lds_add(lds, C_DONE, 2u, lane);
        sw += 2 * 32;
      }
      K_STEP(ra, issue<AUX_V>(ra, r_v, vv0, vv1, t0), 0);
      K_STEP(rb, issue<AUX_V>(rb, r_v, vv0, vv1, t0 + 4 * TILE), 1);
#undef K_STEP
    }
    STAMP(12);
    lds_wait_ge(lds, C_KHELP, 3u);
    {
      u32x4* pp = reinterpret_cast<u32x4*>(pl + lane * 32);
      u32x4 t[2] = {pp[0], pp[1]};
      float mx = bf_lo(t[0][0]);
#pragma unroll
      for (int j = 0; j < 2; ++j)
#pragma unroll
        for (int i = 0; i < 4; ++i) mx = fmaxf(mx, fmaxf(bf_lo(t[j][i]), bf_hi(t[j][i])));
      mx = parity_max(mx);
      const float mc = mx * C2;
      float lp = 0.f;
#pragma unroll
      for (int j = 0; j < 2; ++j) {
#pragma unroll
        for (int i = 0; i < 4; ++i) {
          const float p0 = __builtin_amdgcn_exp2f(fmaf(bf_lo(t[j][i]), C2, -mc)), p1 = __builtin_amdgcn_exp2f(fmaf(bf_hi(t[j][i]), C2, -mc));
          lp += p0 + p1; t[j][i] = pk_bf16(p0, p1);
        }
        pp[j] = t[j];
      }
      lp = parity_sum(lp);
      if (lane < 2) *reinterpret_cast<f32x2*>(lds + L_ML + w * 16 + lane * 8) = f32x2{mx, lp};
      asm volatile("s_waitcnt lgkmcnt(0)" ::: "memory");
      lds_add(lds, C_DONE, 0x10002u, lane);
    }
    f32x4 acc0 = {0.f, 0.f, 0.f, 0.f}, acc1 = {0.f, 0.f, 0.f, 0.f};
    {
      const int msel = c >> 1;
      const unsigned char* pr = pl + (c & 1) * 32 + g * 8;
      const unsigned vf0 = g * 2048 + c * 16;
      float* const rec = reinterpret_cast<float*>(lds + L_ACC + w * 2048) + 8 * (c & 3) + g;
      auto put = [&](bool whole) {
        const int e0 = (whole ? c >> 2 : piece(0, c >> 2)) * 32, e1 = (whole ? 4 + (c >> 2) : piece(1, c >> 2)) * 32;
#pragma unroll
        for (int k = 0; k < 4; ++k) { float* r = rec + (k & 1) * 256 + ((k >> 1) ? e1 : e0); r[0] = acc0[k]; r[4] = acc1[k]; }
      };
      auto get = [&](bool whole) {
        const int e0 = (whole ? c >> 2 : piece(0, c >> 2)) * 32, e1 = (whole ? 4 + (c >> 2) : piece(1, c >> 2)) * 32;
#pragma unroll
        for (int k = 0; k < 4; ++k) { const float* r = rec + (k & 1) * 256 + ((k >> 1) ? e1 : e0); acc0[k] = r[0]; acc1[k] = r[4]; }
      };
      unsigned toff = t0 + 8 * TILE;
      bool cur = false;
#define V_STEP(R, ISSUE_NEXT) do { const u32x2 pk_ = *reinterpret_cast<const u32x2*>(pr); pr += 64; pv_mfma(R, pk_, msel, acc0, acc1); PROG(); ISSUE_NEXT; } while (0)
      for (int it = 0; it < 15; ++it) {
        u32x4 cw;
#pragma nounroll
        for (unsigned spin = 0; spin < SPIN_CAP; ++spin) {
          FENCE();
          cw = *reinterpret_cast<const u32x4*>(lds + C_DONE);
          if ((__builtin_amdgcn_readfirstlane(cw[0]) & 0xffffu) + SLACK >= (unsigned)(128 + 8 * it)) break;
          __builtin_amdgcn_s_sleep(2);
        }
        const unsigned done = __builtin_amdgcn_readfirstlane(cw[0]) & 0xffffu, tau = __builtin_amdgcn_readfirstlane(cw[3]);
        const bool nxt = tau != 0u && (int)(now() - __builtin_amdgcn_readfirstlane(cw[2]) - ((done * tau) >> 8)) > 0;
        const unsigned n0 = nxt ? vf0 : vv0, n1 = nxt ? vf0 + 256 : vv1;
        V_STEP(ra, issue<AUX_V>(ra, r_v, n0, n1, toff));
        V_STEP(rb, issue<AUX_V>(rb, r_v, n0, n1, toff + 4 * TILE));
        lds_add(lds, C_DONE, 2u, lane);
        if (nxt != cur) { put(cur); FENCE(); get(nxt); cur = nxt; }
        FENCE();
        toff += 8 * TILE;
      }
      V_STEP(ra, (void)0);
      V_STEP(rb, (void)0);
#undef V_STEP
      STAMP(3);
      put(cur);
    }
#pragma unroll
    for (int j = 0; j < 8; ++j) { wop0[j] = 0u; wop1[j] = 0u; }
  }
  LANE_VARS;
  PROG_OUT();
  STAMP(4);
  lds_barrier();
  STAMP(5);
  {
    float O = 0.f;
#pragma unroll
    for (int w = 0; w < 4; ++w)
      O = fmaf(*reinterpret_cast<const float*>(lds + L_SC + w * 8 + (tid >> 8) * 4), *reinterpret_cast<const float*>(lds + L_ACC + w * 2048 + tid * 4), O);
    __builtin_amdgcn_raw_buffer_store_b64(u32x2{__float_as_uint(O), tag}, r_ws, (tid >> 4) * 4096 + (tid & 15) * 8, WS_PO + h * (32 * 4096) + s * 128, AUX_ST);
  }
  STAMP(6);
  if (helper) {
    u32x4 po[4];
    const unsigned oo[4] = {(unsigned)lane * 16, (unsigned)lane * 16 + 1024, (unsigned)lane * 16 + 2048, (unsigned)lane * 16 + 3072};
    if (!gather<4, 0>(po, r_ws, oo, WS_PO + wg * 4096, tag)) timed_out = true;
    STAMP(7);
    float o0 = (__uint_as_float(po[0][0]) + __uint_as_float(po[1][0])) + (__uint_as_float(po[2][0]) + __uint_as_float(po[3][0]));
    float o1 = (__uint_as_float(po[0][2]) + __uint_as_float(po[1][2])) + (__uint_as_float(po[2][2]) + __uint_as_float(po[3][2]));
    o0 += dpp<XOR8>(o0); o1 += dpp<XOR8>(o1);
    o0 = x32_sum(x16_sum(o0)); o1 = x32_sum(x16_sum(o1));
    const unsigned opk = pk_bf16(o0, o1);
#pragma unroll
    for (int j = 0; j < 8; ++j) { const unsigned oj = __builtin_amdgcn_readlane(opk, j); y0 = dot2(wop0[j], oj, y0); y1 = dot2(wop1[j], oj, y1); }
    if (timed_out) y0 = __builtin_nanf("");
    const unsigned yoff = tid < D / 2 ? (unsigned)((2 * tid / CPR) * (256 * CPR) + (2 * tid % CPR)) * 8 : OOB;
    __builtin_amdgcn_raw_buffer_store_b128(u32x4{__float_as_uint(y0), tag, __float_as_uint(y1), tag}, r_ws, yoff, WS_YA + wg * (CPR * 8), AUX_ST);
    STAMP(8);
    if (wave == 0) lds_arrive(lds, C_HOP1, lane);
  }
  if (wg >= NRED) return;
  lds_wait_ge(lds, C_HOP1, 1u);
  {
    constexpr int NLD = (256 * CPR / 2 + NTHR - 1) / NTHR;
    const float xo = tid < CPR ? bf_lo(a.x[wg * CPR + tid]) : 0.f;
    u32x4 t[NLD];
    unsigned yo[NLD];
#pragma unroll
    for (int k = 0; k < NLD; ++k) yo[k] = tid + NTHR * k < 256 * CPR / 2 ? (unsigned)(tid + NTHR * k) * 16 : OOB;
    if (!gather<NLD, 0>(t, r_ws, yo, WS_YA + wg * (256 * CPR * 8), tag)) timed_out = true;
    STAMP(9);
    float s0 = 0.f, s1 = 0.f;
#pragma unroll
    for (int k = 0; k < NLD; ++k) { s0 += __uint_as_float(t[k][0]); s1 += __uint_as_float(t[k][2]); }
    if (CPR / 2 <= 8) { s0 += dpp<XOR8>(s0); s1 += dpp<XOR8>(s1); }
    if (CPR / 2 <= 4) { s0 += dpp<ROR4>(s0); s1 += dpp<ROR4>(s1); }
    if (CPR / 2 <= 2) { s0 += dpp<XOR2>(s0); s1 += dpp<XOR2>(s1); }
    if (CPR / 2 <= 1) { s0 += dpp<XOR1>(s0); s1 += dpp<XOR1>(s1); }
    s0 = x32_sum(x16_sum(s0)); s1 = x32_sum(x16_sum(s1));
    if (timed_out) s0 = __builtin_nanf("");
    if (lane < CPR / 2) *reinterpret_cast<f32x2*>(lds + L_YA + wave * 64 + lane * 8) = f32x2{s0, s1};
    lds_barrier();
    if (tid < CPR) {
      float r = xo;
#pragma unroll
      for (int w = 0; w < 8; ++w) r += *reinterpret_cast<const float*>(lds + L_YA + w * 64 + tid * 4);
#define AUX_OUT 17
      __builtin_amdgcn_raw_buffer_store_b16((unsigned short)(pk_bf16(r, r) & 0xffffu), mk_rsrc(a.out, D * 2), (wg * CPR + tid) * 2, 0, AUX_OUT);
    }
    if (wg == 0 && tid == 64) __builtin_amdgcn_raw_buffer_store_b32(tag, r_ws, 0, WS_EPOCH, AUX_SC1);
    STAMP(10);
  }
}

}

extern "C" void kernel_launch(void* const* d_in, const int* in_sizes, int n_in, void* d_out, int out_size, void* d_ws, size_t ws_size,
                              hipStream_t stream) {
  (void)in_sizes; (void)n_in; (void)out_size; (void)ws_size;
  Args a;
  a.x = (const unsigned short*)d_in[0];
  a.k = (const unsigned char*)d_in[1];
  a.v = (const unsigned char*)d_in[2];
  a.wq = (const unsigned char*)d_in[3];
  a.wo = (const unsigned char*)d_in[4];
  a.wg = (const unsigned char*)d_in[6];
  a.wu = (const unsigned char*)d_in[7];
  a.wd = (const unsigned char*)d_in[8];
  a.out = (unsigned short*)d_out;
  a.ws = (unsigned char*)d_ws;
  (void)hipFuncSetAttribute((const void*)fused_decode, hipFuncAttributeMaxDynamicSharedMemorySize, LDS_BYTES);
  hipLaunchKernelGGL(fused_decode, dim3(NWG), dim3(NTHR), LDS_BYTES, stream, a);
}
```

```cpp
#include <hip/hip_runtime.h>
#include <hip/hip_bf16.h>
#include <cstdint>

namespace {

typedef unsigned u32x4 __attribute__((ext_vector_type(4)));
typedef unsigned u32x2 __attribute__((ext_vector_type(2)));
typedef float f32x4 __attribute__((ext_vector_type(4)));
typedef float f32x2 __attribute__((ext_vector_type(2)));
typedef __bf16 bf16x8 __attribute__((ext_vector_type(8)));
typedef __bf16 bf16x2 __attribute__((ext_vector_type(2)));
typedef __amdgpu_buffer_rsrc_t rsrc_t;

constexpr int D = 448, SK = 65536, DH = 256, FF = 1792, NQ = 4096;
constexpr int NWG = 256, NTHR = 512;
#define CH_W 10
#define NI 5
#define HK_PRIO 2
#define CP_REF 64
#define CP_AT 144
#define CP_WAIT 300
#define CP_OFF 20
#define CP_TAU 1.008f
#define CODE_PF 20000
#define CODE_WGS 32
#define PO16 1
#define ROWS_AT 252
#define PREWARM 4
#define PREWARM_AT 240
#define AUX_ST 16
#define AUX_LD 16
#define KFAST 0
#define VFAST 0
#define SLACK 4
#define Q_FIRST 700
#define Q_SLEEP 0
#define GU_AT 1500
#define CPR 8
constexpr int NRED = D / CPR;
constexpr unsigned TILE = 8192;
constexpr unsigned OOB = 0x40000000u;
constexpr float C2 = 0.0625f * 1.4426950408889634f;
constexpr unsigned SPIN_CAP = 1u << 22;

constexpr unsigned WS_EPOCH = 0;
constexpr unsigned LSTR = 4352;
constexpr unsigned WS_PO = 4096;
constexpr unsigned WS_YA = WS_PO + 8 * 32 * 32 * 16 * 8;
constexpr unsigned WS_GU = WS_YA + 256 * D * 8;
constexpr unsigned WS_QG = WS_GU + 224 * 4 * 128;
constexpr unsigned WS_ML = WS_QG + 128 * LSTR;
constexpr unsigned WS_END = WS_ML + 256 * LSTR;

constexpr int L_RED = 0;
constexpr int L_CTL = 1024;
constexpr int C_PART = L_CTL, C_QRDY = L_CTL + 4, C_TAG = L_CTL + 12, C_LANDED = L_CTL + 16, C_KHELP = L_CTL + 20, C_HRDY = L_CTL + 24, C_HOP1 = L_CTL + 28,
              C_DONE = L_CTL + 32, C_BASE = L_CTL + 40;
constexpr int L_ML = 1088;
constexpr int L_SC = 1152;
constexpr int L_H = 1184;
constexpr int L_YA = 1216;
constexpr int L_Q = 1792;
constexpr int L_XN = 2816;
constexpr int L_ACC = 4864;
constexpr int L_P = L_ACC + 4 * 2048;
#define LDS_KB 44
constexpr int LDS_BYTES = LDS_KB * 1024;

constexpr int AUX_NT = 2, AUX_SC1 = 16;
#define AUX_K 2
#define AUX_V 2
#define SB() __builtin_amdgcn_sched_barrier(0)
#define FENCE() asm volatile("" ::: "memory")

__device__ __forceinline__ float bf_lo(unsigned u) { return __builtin_bit_cast(float, u << 16); }
__device__ __forceinline__ float bf_hi(unsigned u) { return __builtin_bit_cast(float, u & 0xffff0000u); }
__device__ __forceinline__ unsigned pk_bf16(float lo, float hi) {
  bf16x2 v = {(__bf16)lo, (__bf16)hi};
  return __builtin_bit_cast(unsigned, v);
}
__device__ __forceinline__ float rnd_bf16(float x) { return (float)(__bf16)x; }
__device__ __forceinline__ float dot2(unsigned a, unsigned b, float acc) {
  return __builtin_amdgcn_fdot2_f32_bf16(__builtin_bit_cast(bf16x2, a), __builtin_bit_cast(bf16x2, b), acc, false);
}
template <int CTRL> __device__ __forceinline__ float dpp(float x) {
  return __builtin_bit_cast(float, __builtin_amdgcn_mov_dpp(__builtin_bit_cast(int, x), CTRL, 0xf, 0xf, true));
}
constexpr int XOR1 = 0xB1, XOR2 = 0x4E, XOR7 = 0x141, XOR15 = 0x140, XOR8 = 0x128, ROR4 = 0x124;
__device__ __forceinline__ float x16_max(float x) {
  auto s = __builtin_amdgcn_permlane16_swap(__float_as_uint(x), __float_as_uint(x), false, false);
  return fmaxf(__uint_as_float(s[0]), __uint_as_float(s[1]));
}
__device__ __forceinline__ float x32_max(float x) {
  auto s = __builtin_amdgcn_permlane32_swap(__float_as_uint(x), __float_as_uint(x), false, false);
  return fmaxf(__uint_as_float(s[0]), __uint_as_float(s[1]));
}
__device__ __forceinline__ float x16_sum(float x) {
  auto s = __builtin_amdgcn_permlane16_swap(__float_as_uint(x), __float_as_uint(x), false, false);
  return __uint_as_float(s[0]) + __uint_as_float(s[1]);
}
__device__ __forceinline__ float x32_sum(float x) {
  auto s = __builtin_amdgcn_permlane32_swap(__float_as_uint(x), __float_as_uint(x), false, false);
  return __uint_as_float(s[0]) + __uint_as_float(s[1]);
}
__device__ __forceinline__ float wave_sum(float x) {
  x += dpp<XOR1>(x); x += dpp<XOR2>(x); x += dpp<XOR7>(x); x += dpp<XOR15>(x);
  return x32_sum(x16_sum(x));
}
__device__ __forceinline__ float parity_sum(float x) {
  x += dpp<XOR2>(x); x += dpp<ROR4>(x); x += dpp<XOR8>(x);
  return x32_sum(x16_sum(x));
}
__device__ __forceinline__ float parity_max(float x) {
  x = fmaxf(x, dpp<XOR2>(x)); x = fmaxf(x, dpp<ROR4>(x)); x = fmaxf(x, dpp<XOR8>(x));
  return x32_max(x16_max(x));
}
__device__ __forceinline__ void lds_barrier() {
  asm volatile("s_waitcnt lgkmcnt(0)" ::: "memory");
  __builtin_amdgcn_s_barrier();
  FENCE();
}
__device__ __forceinline__ rsrc_t mk_rsrc(const void* p, unsigned bytes) {
  return __builtin_amdgcn_make_buffer_rsrc(const_cast<void*>(p), 0, bytes, 0x00020000);
}
__device__ __forceinline__ unsigned lds_ld(unsigned char* lds, int off) {
  return __builtin_amdgcn_readfirstlane(__hip_atomic_load(reinterpret_cast<unsigned*>(lds + off), __ATOMIC_RELAXED, __HIP_MEMORY_SCOPE_WORKGROUP));
}
template <int SLEEP = 1>
__device__ __forceinline__ void lds_wait_ge(unsigned char* lds, int off, unsigned want) {
#pragma nounroll
  for (unsigned spin = 0; spin < SPIN_CAP * 8u; ++spin) {
    if (lds_ld(lds, off) >= want) break;
    __builtin_amdgcn_s_sleep(SLEEP);
  }
  FENCE();
}
__device__ __forceinline__ void lds_arrive(unsigned char* lds, int off, int lane) {
  asm volatile("s_waitcnt lgkmcnt(0)" ::: "memory");
  if (lane == 0) __hip_atomic_fetch_add(reinterpret_cast<unsigned*>(lds + off), 1u, __ATOMIC_RELAXED, __HIP_MEMORY_SCOPE_WORKGROUP);
}
__device__ __forceinline__ void lds_add(unsigned char* lds, int off, unsigned v, int lane) {
  if (lane == 0) __hip_atomic_fetch_add(reinterpret_cast<unsigned*>(lds + off), v, __ATOMIC_RELAXED, __HIP_MEMORY_SCOPE_WORKGROUP);
}
__device__ __forceinline__ unsigned now() { return (unsigned)__builtin_amdgcn_s_memrealtime(); }
template <int SLEEP>
__device__ __forceinline__ void sleep_until(unsigned t0, unsigned ticks) {
#pragma nounroll
  for (unsigned spin = 0; spin < SPIN_CAP && now() - t0 < ticks; ++spin) __builtin_amdgcn_s_sleep(SLEEP);
}
__device__ __forceinline__ void chain_wait(unsigned char* lds, int idx) {
#pragma nounroll
  for (unsigned spin = 0; spin < SPIN_CAP * 8u; ++spin) {
    if ((int)lds_ld(lds, C_LANDED) >= idx + 1 - CH_W || lds_ld(lds, C_QRDY) != 0u) break;
    __builtin_amdgcn_s_sleep(1);
  }
  FENCE();
}
template <int N, int SLEEP>
__device__ __forceinline__ bool gather(u32x4 (&v)[N], rsrc_t rs, const unsigned (&voff)[N], unsigned soff, unsigned tag) {
  bool have[N];
#pragma unroll
  for (int k = 0; k < N; ++k) { have[k] = voff[k] == OOB; v[k] = u32x4{0u, 0u, 0u, 0u}; }
#pragma nounroll
  for (unsigned spin = 0; spin < SPIN_CAP; ++spin) {
    FENCE();
    u32x4 t[N];
#pragma unroll
    for (int k = 0; k < N; ++k) t[k] = __builtin_amdgcn_raw_buffer_load_b128(rs, have[k] ? OOB : voff[k], soff, AUX_LD);
    bool all = true;
#pragma unroll
    for (int k = 0; k < N; ++k) {
      const bool now = !have[k] && t[k][1] == tag && t[k][3] == tag;
      v[k][0] = now ? t[k][0] : v[k][0]; v[k][2] = now ? t[k][2] : v[k][2];
      have[k] = have[k] || now; all = all && have[k];
    }
    if (__builtin_amdgcn_ballot_w64(!all) == 0) return true;
    if (SLEEP > 0) __builtin_amdgcn_s_sleep(SLEEP);
  }
  return false;
}
#define STAMP(slot) do {} while (0)
#define PROG() do {} while (0)
#define PROG_OUT() do {} while (0)
#define PROGL() do {} while (0)

struct Args {
  const unsigned short* x;
  const unsigned char* k;
  const unsigned char* v;
  const unsigned char* wq;
  const unsigned char* wo;
  const unsigned char* wg;
  const unsigned char* wu;
  const unsigned char* wd;
  unsigned short* out;
  unsigned char* ws;
};

__host__ __device__ constexpr int piece(int e, int n) { return n < 2 ? 2 * e + n : 2 * n + e; }
template <int AUX>
__device__ __forceinline__ void issue(u32x4 (&r)[8], rsrc_t rs, unsigned v0, unsigned v1, unsigned soff) {
#pragma unroll
  for (int i = 0; i < 8; ++i) { r[i] = __builtin_amdgcn_raw_buffer_load_b128(rs, ((i & 1) ? v1 : v0) + (i >> 1) * 512, soff, AUX); SB(); }
}
__device__ __forceinline__ f32x4 mfma(u32x4 x, u32x4 y, f32x4 acc) {
  return __builtin_amdgcn_mfma_f32_16x16x32_bf16(__builtin_bit_cast(bf16x8, x), __builtin_bit_cast(bf16x8, y), acc, 0, 0, 0);
}
__device__ __forceinline__ void qk_mfma(const u32x4 (&r)[8], const u32x4 (&qf)[2], f32x4 (&d)[4]) {
#pragma unroll
  for (int j = 0; j < 4; ++j) d[j] = mfma(qf[1], r[2 * j + 1], mfma(qf[0], r[2 * j], f32x4{0.f, 0.f, 0.f, 0.f}));
  SB();
}
__device__ __forceinline__ unsigned short qk_score(const f32x4 (&d)[4], bool b0, bool b1) {
  float v[4];
#pragma unroll
  for (int j = 0; j < 4; ++j) {
    float e0 = d[j][0], e1 = d[j][1], e2 = d[j][2], e3 = d[j][3];
    asm("" : "+v"(e0), "+v"(e2));
    const float lo = b0 ? e1 : e0, hi = b0 ? e3 : e2;
    v[j] = b1 ? hi : lo;
  }
  const float u0 = (b0 ? v[1] : v[0]) + dpp<XOR1>(b0 ? v[0] : v[1]);
  const float u1 = (b0 ? v[3] : v[2]) + dpp<XOR1>(b0 ? v[2] : v[3]);
  const float w = (b1 ? u1 : u0) + dpp<XOR2>(b1 ? u0 : u1);
  return (unsigned short)(pk_bf16(w, w) & 0xffffu);
}
__device__ __forceinline__ void pv_mfma(const u32x4 (&vr)[8], u32x2 pk, int msel, f32x4& acc0, f32x4& acc1) {
#pragma unroll
  for (int q = 0; q < 4; ++q) {
    const u32x4 pa = {msel == 2 * q ? pk[0] : 0u, msel == 2 * q ? pk[1] : 0u, msel == 2 * q + 1 ? pk[0] : 0u, msel == 2 * q + 1 ? pk[1] : 0u};
    const unsigned sel = (q & 1) ? 0x07060302u : 0x05040100u;
    const int d0 = q >> 1, d1 = 2 + (q >> 1);
    const u32x4 b0 = {__builtin_amdgcn_perm(vr[2][d0], vr[0][d0], sel), __builtin_amdgcn_perm(vr[6][d0], vr[4][d0], sel),
                      __builtin_amdgcn_perm(vr[3][d0], vr[1][d0], sel), __builtin_amdgcn_perm(vr[7][d0], vr[5][d0], sel)};
    const u32x4 b1 = {__builtin_amdgcn_perm(vr[2][d1], vr[0][d1], sel), __builtin_amdgcn_perm(vr[6][d1], vr[4][d1], sel),
                      __builtin_amdgcn_perm(vr[3][d1], vr[1][d1], sel), __builtin_amdgcn_perm(vr[7][d1], vr[5][d1], sel)};
    acc0 = mfma(pa, b0, acc0);
    acc1 = mfma(pa, b1, acc1);
  }
  SB();
}
__device__ __forceinline__ void fma8(float (&s)[8], float xv, u32x4 w) {
#pragma unroll
  for (int i = 0; i < 4; ++i) { s[2 * i] = fmaf(xv, bf_lo(w[i]), s[2 * i]); s[2 * i + 1] = fmaf(xv, bf_hi(w[i]), s[2 * i + 1]); }
}
template <bool WHOLE>
__device__ __forceinline__ void load_qf(u32x4 (&qf)[2], const unsigned char* lds, int c, int g) {
#pragma unroll
  for (int e = 0; e < 2; ++e) qf[e] = *reinterpret_cast<const u32x4*>(lds + L_Q + ((c >> 2) & 1) * 512 + ((WHOLE ? 4 * e + g : piece(e, g)) * 4 + (c & 3)) * 16);
}

__global__ void __launch_bounds__(NTHR, 2) fused_decode(Args a) {
  extern __shared__ __attribute__((aligned(16))) unsigned char lds[];
  const int lane0 = threadIdx.x & 63, wave = __builtin_amdgcn_readfirstlane(threadIdx.x >> 6);
  const int wg = blockIdx.x, h = wg >> 5, s = wg & 31;
  unsigned long long pc;
  asm volatile("s_getpc_b64 %0" : "=s"(pc));
  const unsigned cpf = __builtin_amdgcn_raw_buffer_load_b32(mk_rsrc(reinterpret_cast<const void*>(pc), wg < CODE_WGS ? CODE_PF : 0), threadIdx.x * 128, 0, 0);
  SB();
  const bool helper = wave < 4;
  const unsigned wg_off = (unsigned)wg * (128 * TILE);
  const unsigned vw0 = (lane0 & 12) * 512 + (lane0 >> 4) * 64 + (lane0 & 3) * 16, vw1 = vw0 + 256;
  const rsrc_t r_k = mk_rsrc(a.k, 8u * SK * DH * 2);
  u32x4 ra[8], rb[8];
#pragma unroll
  for (int i = 0; i < 8; ++i) asm volatile("" : "=v"(ra[i]), "=v"(rb[i]));
  if (!helper) {
    const unsigned t0 = wg_off + (wave - 4) * TILE;
    issue<AUX_K>(ra, r_k, vw0, vw1, t0);
    issue<AUX_K>(rb, r_k, vw0, vw1, t0 + 4 * TILE);
  }
  const rsrc_t r_ws = mk_rsrc(a.ws, WS_END);
#define LANE_VARS int tid = threadIdx.x; asm volatile("" : "+v"(tid)); const int lane = tid & 63, c = lane & 15, g = lane >> 4; (void)c; (void)g
#define SCORE_VARS const bool b0 = (c & 1) != 0, b1 = (c & 2) != 0; const int sc_lane = (g & 1) * 32 + c * 2
#define STAMP0() do {} while (0)
  bool timed_out = false;
  unsigned tag;
  unsigned draw[8], wraw[16];
#pragma unroll
  for (int j = 0; j < 8; ++j) asm volatile("" : "=v"(draw[j]), "=v"(wraw[j]), "=v"(wraw[8 + j]));

  if (__builtin_expect(helper, 1)) {
    __builtin_amdgcn_s_setprio(3);
    LANE_VARS;
    const bool qwg = (s & 8) == 0;
    const int qsl = (s & 7) * 2 + (s >> 4);
    const int fb = h * 16 + qsl;
    const int wrow = (lane >> (qwg ? 2 : 3)) + (qwg ? 16 * wave : (fb & 3) * 112 + (wave >> 1) * 56), wstep = qwg ? 64 : 8;
    const u32x4 xv = __builtin_amdgcn_raw_buffer_load_b128(mk_rsrc(a.x, D * 2), lane * 16, 0, 0);
    const unsigned epoch = __builtin_amdgcn_raw_buffer_load_b32(r_ws, 0, WS_EPOCH, AUX_SC1);
    u32x4 wv[7];
    {
      const unsigned rowb = qwg ? NQ * 2 : FF * 2;
      const rsrc_t r0 = mk_rsrc(qwg ? a.wq : (wave & 1) ? a.wu : a.wg, (qwg || fb < 112) ? D * rowb : 0u);
      const unsigned v0 = wrow * rowb + (lane & (qwg ? 3 : 7)) * 16, s0 = qwg ? (h * 512 + qsl * 32) * 2 : (fb >> 2) * 128;
#pragma unroll
      for (int i = 0; i < 7; ++i) wv[i] = __builtin_amdgcn_raw_buffer_load_b128(r0, v0, s0 + i * wstep * rowb, AUX_NT);
    }
    const unsigned t_start = now();
    unsigned t_ref = 0, n_ref = 0;
    SB();
    STAMP0();
    SCORE_VARS;
    if (tid < 16) *reinterpret_cast<unsigned*>(lds + L_CTL + tid * 4) = 0u;
    lds_barrier();

    {
      float xs[8];
#pragma unroll
      for (int i = 0; i < 4; ++i) { xs[2 * i] = bf_lo(xv[i]); xs[2 * i + 1] = bf_hi(xv[i]); }
      float sm = 0.f;
#pragma unroll
      for (int i = 0; i < 8; ++i) sm += xs[i];
      const float mean = wave_sum(sm) * (1.0f / D);
      float sq = 0.f;
#pragma unroll
      for (int i = 0; i < 8; ++i) { const float d = xs[i] - mean; sq = fmaf(d, d, sq); }
      sq = lane < D / 8 ? sq : 0.f;
      const float var = wave_sum(sq) * (1.0f / D);
      const float mean_b = rnd_bf16(mean);
      const float den_b = rnd_bf16(__builtin_amdgcn_sqrtf(rnd_bf16(rnd_bf16(var) + 1e-5f)));
      float rcp = __builtin_amdgcn_rcpf(den_b);
      rcp = fmaf(fmaf(-den_b, rcp, 1.0f), rcp, rcp);
#pragma unroll
      for (int i = 0; i < 8; ++i) {
        const float n = rnd_bf16(xs[i] - mean_b), q = n * rcp;
        xs[i] = rnd_bf16(fmaf(fmaf(-den_b, q, n), rcp, q));
      }
      f32x4* xp = reinterpret_cast<f32x4*>(lds + L_XN + lane * 32);
      xp[0] = f32x4{xs[0], xs[1], xs[2], xs[3]}; xp[1] = f32x4{xs[4], xs[5], xs[6], xs[7]};
      FENCE();
    }
    tag = __builtin_amdgcn_readfirstlane(epoch) + 1u;
    asm volatile("" :: "v"(cpf));
    {
      float sacc[8] = {0.f, 0.f, 0.f, 0.f, 0.f, 0.f, 0.f, 0.f};
#pragma unroll
      for (int i = 0; i < 7; ++i) fma8(sacc, *reinterpret_cast<const float*>(lds + L_XN + (wrow + wstep * i) * 4), wv[i]);
#pragma unroll
      for (int i = 0; i < 8; ++i) { float t = sacc[i]; const float r = dpp<ROR4>(t); t += qwg ? r : 0.f; t += dpp<XOR8>(t); sacc[i] = x32_sum(x16_sum(t)); }
      if (lane < 8) {
        f32x4* p = reinterpret_cast<f32x4*>(lds + L_RED + wave * 256 + lane * 32);
        p[0] = f32x4{sacc[0], sacc[1], sacc[2], sacc[3]}; p[1] = f32x4{sacc[4], sacc[5], sacc[6], sacc[7]};
      }
      lds_arrive(lds, C_PART, lane);
      if (wave == 0) {
        lds_wait_ge(lds, C_PART, 4u);
        if (qwg) {
          float q0 = 0.f, q1 = 0.f;
#pragma unroll
          for (int w = 0; w < 4; ++w) { const f32x2 t = *reinterpret_cast<const f32x2*>(lds + L_RED + w * 256 + c * 8); q0 += t[0]; q1 += t[1]; }
          __builtin_amdgcn_raw_buffer_store_b64(u32x2{pk_bf16(q0, q1), tag}, r_ws, lane < 16 ? lane * 8 : OOB, WS_QG + (h * 16 + qsl) * LSTR, AUX_SC1);
        } else {
          const int o = ((lane >> 2) & 1) * 256 + (lane >> 3) * 32 + (lane & 3) * 8;
          const f32x2 t0 = *reinterpret_cast<const f32x2*>(lds + L_RED + o), t1 = *reinterpret_cast<const f32x2*>(lds + L_RED + 512 + o);
          __builtin_amdgcn_raw_buffer_store_b128(u32x4{__float_as_uint(t0[0] + t1[0]), tag, __float_as_uint(t0[1] + t1[1]), tag}, r_ws,
                                                 fb < 112 ? (lane >> 3) * 512 + (lane & 7) * 16 : OOB, WS_GU + (fb >> 2) * 4096 + (fb & 3) * 128, AUX_SC1);
        }
        STAMP(1);
      }
    }
    const bool ffn = wg < FF / 8;
    if (wave == 0) {
      u32x4 qg[2];
      const unsigned qo[2] = {(lane >> 2) * LSTR + (lane & 3) * 32, (lane >> 2) * LSTR + (lane & 3) * 32 + 16};
      sleep_until<1>(t_start, Q_FIRST);
      timed_out = !gather<2, Q_SLEEP>(qg, r_ws, qo, WS_QG + h * 16 * LSTR, tag);
      *reinterpret_cast<u32x4*>(lds + L_Q + lane * 16) = u32x4{qg[0][0], qg[0][2], qg[1][0], qg[1][2]};
      if (lane == 0) *reinterpret_cast<unsigned*>(lds + C_TAG) = tag;
      lds_arrive(lds, C_QRDY, lane);
    }
    if (wave != 0) {
      __builtin_amdgcn_s_setprio(HK_PRIO);
      const int slot = NI + wave - 1;
      u32x4 kt[4][8];
#pragma unroll
      for (int i = 0; i < 4; ++i) { chain_wait(lds, 4 * slot + i); issue<AUX_K>(kt[i], r_k, vw0, vw1, wg_off + (4 * slot + i) * TILE); }
      asm volatile("s_waitcnt vmcnt(24)" ::: "memory"); lds_arrive(lds, C_LANDED, lane); PROGL();
      asm volatile("s_waitcnt vmcnt(16)" ::: "memory"); lds_arrive(lds, C_LANDED, lane); PROGL();
      asm volatile("s_waitcnt vmcnt(8)" ::: "memory"); lds_arrive(lds, C_LANDED, lane); PROGL();
      asm volatile("s_waitcnt vmcnt(0)" ::: "memory"); lds_arrive(lds, C_LANDED, lane); PROGL();
      lds_wait_ge(lds, C_QRDY, 1u);
      u32x4 qf[2];
      load_qf<true>(qf, lds, c, g);
#pragma unroll
      for (int i = 0; i < 4; ++i) {
        f32x4 d[4];
        qk_mfma(kt[i], qf, d);
        *reinterpret_cast<unsigned short*>(lds + L_P + i * 2048 + slot * 64 + sc_lane) = qk_score(d, b0, b1);
        PROG();
      }
      lds_arrive(lds, C_KHELP, lane);
      lds_add(lds, C_DONE, 4u, lane);
      __builtin_amdgcn_s_setprio(3);
    }
    STAMP(3);
    {
      if (wave == 3) {
        sleep_until<32>(t_start, GU_AT);
        u32x4 gu[1];
        const unsigned go[1] = {(ffn && lane < 32) ? (unsigned)lane * 16 : OOB};
        if (!gather<1, 64>(gu, r_ws, go, WS_GU + wg * 512, tag)) timed_out = true;
        float g0 = __uint_as_float(gu[0][0]), g1 = __uint_as_float(gu[0][2]);
        g0 += dpp<XOR8>(g0); g1 += dpp<XOR8>(g1);
        g0 = rnd_bf16(x16_sum(g0)); g1 = rnd_bf16(x16_sum(g1));
        const float u0 = dpp<ROR4>(g0), u1 = dpp<ROR4>(g1);
        const float h0 = rnd_bf16((g0 > 0.f ? g0 : g0 * 0.f) * u0), h1 = rnd_bf16((g1 > 0.f ? g1 : g1 * 0.f) * u1);
        if (lane < 4) *reinterpret_cast<f32x2*>(lds + L_H + lane * 8) = f32x2{h0, h1};
      }
      if (wave == 1) { lds_wait_ge<2>(lds, C_DONE, CP_REF); t_ref = now(); n_ref = lds_ld(lds, C_DONE) & 0xffffu; }
    }
    STAMP(11);
    if (wave == 1) {
      lds_wait_ge<2>(lds, C_DONE, 0x10000u * 4u + CP_AT);
      const unsigned t_now = now(), n_cp = lds_ld(lds, C_DONE) & 0xffffu;
      const float tau = (float)(t_now - t_ref) / (float)(n_cp - n_ref);
      const unsigned t_cp = t_now - (unsigned)(tau * (float)(n_cp - CP_AT));
      const int hd = lane & 1;
      float mw[4], lw[4];
#pragma unroll
      for (int w = 0; w < 4; ++w) { const f32x2 t = *reinterpret_cast<const f32x2*>(lds + L_ML + w * 16 + hd * 8); mw[w] = t[0]; lw[w] = t[1]; }
      const float M = fmaxf(fmaxf(mw[0], mw[1]), fmaxf(mw[2], mw[3]));
      float L = 0.f;
#pragma unroll
      for (int w = 0; w < 4; ++w) L = fmaf(__builtin_amdgcn_exp2f((mw[w] - M) * C2), lw[w], L);
      __builtin_amdgcn_raw_buffer_store_b128(u32x4{(lane & 2) ? t_cp : __float_as_uint(M), tag, __float_as_uint((lane & 2) ? tau : L), tag}, r_ws,
                                             lane < 8 ? lane * 16 : OOB, WS_ML + wg * LSTR, AUX_SC1);
      sleep_until<16>(t_now, CP_WAIT);
      u32x4 ml[2];
      const unsigned mo[2] = {(lane >> 2) * LSTR + (lane & 3) * 16, (16 + (lane >> 2)) * LSTR + (lane & 3) * 16};
      if (!gather<2, 32>(ml, r_ws, mo, WS_ML + h * 32 * LSTR, tag)) timed_out = true;
      const float Ma = __uint_as_float(ml[0][0]), Mb = __uint_as_float(ml[1][0]);
      float Mg = fmaxf(Ma, Mb);
      Mg = fmaxf(Mg, dpp<ROR4>(Mg)); Mg = fmaxf(Mg, dpp<XOR8>(Mg)); Mg = x32_max(x16_max(Mg));
      float Lg = __builtin_amdgcn_exp2f((Ma - Mg) * C2) * __uint_as_float(ml[0][2]) + __builtin_amdgcn_exp2f((Mb - Mg) * C2) * __uint_as_float(ml[1][2]);
      if (lane & 2) Lg = (lane & 1) ? __uint_as_float(ml[0][2]) + __uint_as_float(ml[1][2]) : (float)(int)(ml[0][0] - t_cp) + (float)(int)(ml[1][0] - t_cp);
      Lg += dpp<ROR4>(Lg); Lg += dpp<XOR8>(Lg); Lg = x32_sum(x16_sum(Lg));
      if (lane < 2) {
#pragma unroll
        for (int w = 0; w < 4; ++w) *reinterpret_cast<float*>(lds + L_SC + w * 8 + lane * 4) = timed_out ? __builtin_nanf("") : __builtin_amdgcn_exp2f((mw[w] - Mg) * C2) / Lg;
      }
      {
        const float tm = dpp<XOR1>(Lg) * (CP_TAU / 32);
        if (lane == 2 && !timed_out)
          *reinterpret_cast<u32x2*>(lds + C_BASE) = u32x2{t_cp + (unsigned)(int)(Lg * (1.0f / 32) + (float)CP_OFF - tm * (float)CP_AT), (unsigned)(tm * 256.f)};
      }
      STAMP(14);
    }
    {
      lds_wait_ge<4>(lds, C_DONE, 0x10000u * 4u + ROWS_AT);
      const rsrc_t r_wo = mk_rsrc(a.wo, NQ * D * 2), r_d = mk_rsrc(a.wd, ffn ? FF * D * 2 : 0);
      const unsigned coff = tid < D / 2 ? tid * 4 : OOB;
#pragma unroll
      for (int j = 0; j < 8; ++j) draw[j] = __builtin_amdgcn_raw_buffer_load_b32(r_d, coff, (wg * 8 + j) * (D * 2), AUX_NT);
#pragma unroll
      for (int j = 0; j < 16; ++j) wraw[j] = __builtin_amdgcn_raw_buffer_load_b32(r_wo, coff, (wg * 16 + j) * (D * 2), AUX_NT);
      SB();
    }
  } else {
    const int w = wave - 4;
    const unsigned t0 = wg_off + w * TILE;
    LANE_VARS;
    SCORE_VARS;
    STAMP0();
    const rsrc_t r_v = mk_rsrc(a.v, 8u * SK * DH * 2);
    const unsigned vv0 = g * 2048 + piece(0, c >> 2) * 64 + (c & 3) * 16, vv1 = g * 2048 + piece(1, c >> 2) * 64 + (c & 3) * 16;
    const bool kwhole = w < KFAST, vwhole = w < VFAST;
    const unsigned vk0 = kwhole ? vw0 : (c >> 2) * 2048 + piece(0, g) * 64 + (c & 3) * 16, vk1 = kwhole ? vw1 : (c >> 2) * 2048 + piece(1, g) * 64 + (c & 3) * 16;
    u32x4 rx[NI - 2][8];
    static_assert(CH_W >= 5, "");
    lds_barrier();
    asm volatile("s_waitcnt vmcnt(8)" ::: "memory"); lds_arrive(lds, C_LANDED, lane); PROGL();
#define CHAIN_STEP(R, SLOT) do { \
      constexpr bool first_ = CH_W < 5;     \
      if (first_ && lds_ld(lds, C_QRDY) == 0u) { asm volatile("s_waitcnt vmcnt(0)" ::: "memory"); lds_arrive(lds, C_LANDED, lane); PROGL(); } \
      chain_wait(lds, 4 * (SLOT) + w); \
      issue<AUX_K>(R, r_k, vw0, vw1, t0 + 4 * (SLOT) * TILE); \
      if (!first_ && lds_ld(lds, C_QRDY) == 0u) { asm volatile("s_waitcnt vmcnt(8)" ::: "memory"); lds_arrive(lds, C_LANDED, lane); PROGL(); } } while (0)
#pragma unroll
    for (int i = 2; i < NI; ++i) CHAIN_STEP(rx[i - 2], i);
#undef CHAIN_STEP
    if (lds_ld(lds, C_QRDY) == 0u) { asm volatile("s_waitcnt vmcnt(0)" ::: "memory"); lds_arrive(lds, C_LANDED, lane); PROGL(); }
    lds_wait_ge(lds, C_QRDY, 1u);
    tag = lds_ld(lds, C_TAG);
    STAMP(2);
    u32x4 qf[2];
    load_qf<true>(qf, lds, c, g);
    unsigned char* const pl = lds + L_P + w * 2048;
    {
      f32x4 d[4];
      unsigned short* sw = reinterpret_cast<unsigned short*>(pl + sc_lane);
      unsigned toff = t0 + 4 * (NI + 3) * TILE;
#define K_STEP(R, ISSUE_NEXT, SLOT) do { qk_mfma(R, qf, d); PROG(); ISSUE_NEXT; sw[(SLOT) * 32] = qk_score(d, b0, b1); } while (0)
      K_STEP(ra, issue<AUX_K>(ra, r_k, vk0, vk1, toff), 0);
      K_STEP(rb, issue<AUX_K>(rb, r_k, vk0, vk1, toff + 4 * TILE), 1);
#pragma unroll
      for (int i = 2; i < NI; ++i) K_STEP(rx[i - 2], (void)0, i);
      lds_add(lds, C_DONE, NI, lane);
      if (!kwhole) load_qf<false>(qf, lds, c, g);
      sw += (NI + 3) * 32;
      for (int it = 1; it < (29 - NI) / 2; ++it) {
        toff += 8 * TILE;
        K_STEP(ra, issue<AUX_K>(ra, r_k, vk0, vk1, toff), 0);
        K_STEP(rb, issue<AUX_K>(rb, r_k, vk0, vk1, toff + 4 * TILE), 1);
        lds_add(lds, C_DONE, 2u, lane);
        sw += 2 * 32;
      }
      K_STEP(ra, issue<AUX_V>(ra, r_v, vv0, vv1, t0), 0);
      K_STEP(rb, issue<AUX_V>(rb, r_v, vv0, vv1, t0 + 4 * TILE), 1);
#undef K_STEP
    }
    STAMP(12);
    lds_wait_ge(lds, C_KHELP, 3u);
    {
      u32x4* pp = reinterpret_cast<u32x4*>(pl + lane * 32);
      u32x4 t[2] = {pp[0], pp[1]};
      float mx = bf_lo(t[0][0]);
#pragma unroll
      for (int j = 0; j < 2; ++j)
#pragma unroll
        for (int i = 0; i < 4; ++i) mx = fmaxf(mx, fmaxf(bf_lo(t[j][i]), bf_hi(t[j][i])));
      mx = parity_max(mx);
      const float mc = mx * C2;
      float lp = 0.f;
#pragma unroll
      for (int j = 0; j < 2; ++j) {
#pragma unroll
        for (int i = 0; i < 4; ++i) {
          const float p0 = __builtin_amdgcn_exp2f(fmaf(bf_lo(t[j][i]), C2, -mc)), p1 = __builtin_amdgcn_exp2f(fmaf(bf_hi(t[j][i]), C2, -mc));
          lp += p0 + p1; t[j][i] = pk_bf16(p0, p1);
        }
        pp[j] = t[j];
      }
      lp = parity_sum(lp);
      if (lane < 2) *reinterpret_cast<f32x2*>(lds + L_ML + w * 16 + lane * 8) = f32x2{mx, lp};
      asm volatile("s_waitcnt lgkmcnt(0)" ::: "memory");
      lds_add(lds, C_DONE, 0x10002u, lane);
    }
    f32x4 acc0 = {0.f, 0.f, 0.f, 0.f}, acc1 = {0.f, 0.f, 0.f, 0.f};
    {
      const int msel = c >> 1;
      const unsigned char* pr = pl + (c & 1) * 32 + g * 8;
      const unsigned vf0 = g * 2048 + c * 16;
      float* const rec = reinterpret_cast<float*>(lds + L_ACC + w * 2048) + 8 * (c & 3) + g;
      auto put = [&](bool whole) {
        const int e0 = (whole ? c >> 2 : piece(0, c >> 2)) * 32, e1 = (whole ? 4 + (c >> 2) : piece(1, c >> 2)) * 32;
#pragma unroll
        for (int k = 0; k < 4; ++k) { float* r = rec + (k & 1) * 256 + ((k >> 1) ? e1 : e0); r[0] = acc0[k]; r[4] = acc1[k]; }
      };
      auto get = [&](bool whole) {
        const int e0 = (whole ? c >> 2 : piece(0, c >> 2)) * 32, e1 = (whole ? 4 + (c >> 2) : piece(1, c >> 2)) * 32;
#pragma unroll
        for (int k = 0; k < 4; ++k) { const float* r = rec + (k & 1) * 256 + ((k >> 1) ? e1 : e0); acc0[k] = r[0]; acc1[k] = r[4]; }
      };
      unsigned toff = t0 + 8 * TILE;
      bool cur = false;
#define V_STEP(R, ISSUE_NEXT) do { const u32x2 pk_ = *reinterpret_cast<const u32x2*>(pr); pr += 64; pv_mfma(R, pk_, msel, acc0, acc1); PROG(); ISSUE_NEXT; } while (0)
      for (int it = 0; it < 15; ++it) {
        u32x4 cw;
#pragma nounroll
        for (unsigned spin = 0; spin < SPIN_CAP; ++spin) {
          FENCE();
          cw = *reinterpret_cast<const u32x4*>(lds + C_DONE);
          if ((__builtin_amdgcn_readfirstlane(cw[0]) & 0xffffu) + SLACK >= (unsigned)(128 + 8 * it)) break;
          __builtin_amdgcn_s_sleep(2);
        }
        const unsigned done = __builtin_amdgcn_readfirstlane(cw[0]) & 0xffffu, tau = __builtin_amdgcn_readfirstlane(cw[3]);
        const bool nxt = vwhole || tau != 0u && (int)(now() - __builtin_amdgcn_readfirstlane(cw[2]) - ((done * tau) >> 8)) > 0;
        const unsigned n0 = nxt ? vf0 : vv0, n1 = nxt ? vf0 + 256 : vv1;
        V_STEP(ra, issue<AUX_V>(ra, r_v, n0, n1, toff));
        V_STEP(rb, issue<AUX_V>(rb, r_v, n0, n1, toff + 4 * TILE));
        lds_add(lds, C_DONE, 2u, lane);
        if (nxt != cur) { put(cur); FENCE(); get(nxt); cur = nxt; }
        FENCE();
        toff += 8 * TILE;
      }
      V_STEP(ra, lds_add(lds, C_DONE, 1u, lane));
      V_STEP(rb, (void)0);
#undef V_STEP
      STAMP(3);
      put(cur);
      asm volatile("" :: "v"(cpf));
    }
  }
  LANE_VARS;
  PROG_OUT();
  STAMP(4);
  lds_barrier();
  STAMP(5);
  if (!helper) {
    const int u = tid - 256;
    float o0 = 0.f, o1 = 0.f;
#pragma unroll
    for (int w = 0; w < 4; ++w) {
      const float sc = *reinterpret_cast<const float*>(lds + L_SC + w * 8 + (u >> 7) * 4);
      const f32x2 t = *reinterpret_cast<const f32x2*>(lds + L_ACC + w * 2048 + u * 8);
      o0 = fmaf(sc, t[0], o0); o1 = fmaf(sc, t[1], o1);
    }
    __builtin_amdgcn_raw_buffer_store_b128(u32x4{__float_as_uint(o0), tag, __float_as_uint(o1), tag}, r_ws, (u >> 3) * 4096 + (u & 7) * 16,
                                           WS_PO + h * (32 * 4096) + s * 128, AUX_ST);
  }
  STAMP(6);
  if (helper) {
    unsigned wop0[8], wop1[8];
    float y0 = 0.f, y1 = 0.f;
#pragma unroll
    for (int j = 0; j < 8; ++j) {
      const float hv = *reinterpret_cast<const float*>(lds + L_H + j * 4);
      y0 = fmaf(hv, bf_lo(draw[j]), y0); y1 = fmaf(hv, bf_hi(draw[j]), y1);
      wop0[j] = __builtin_amdgcn_perm(wraw[2 * j + 1], wraw[2 * j], 0x05040100u); wop1[j] = __builtin_amdgcn_perm(wraw[2 * j + 1], wraw[2 * j], 0x07060302u);
    }
    asm volatile("" : "+v"(y0), "+v"(y1), "+v"(wop0[0]), "+v"(wop0[1]), "+v"(wop0[2]), "+v"(wop0[3]), "+v"(wop0[4]), "+v"(wop0[5]), "+v"(wop0[6]), "+v"(wop0[7]),
                      "+v"(wop1[0]), "+v"(wop1[1]), "+v"(wop1[2]), "+v"(wop1[3]), "+v"(wop1[4]), "+v"(wop1[5]), "+v"(wop1[6]), "+v"(wop1[7]));
    u32x4 po[4];
    const unsigned oo[4] = {(unsigned)lane * 16, (unsigned)lane * 16 + 1024, (unsigned)lane * 16 + 2048, (unsigned)lane * 16 + 3072};
    if (!gather<4, 0>(po, r_ws, oo, WS_PO + wg * 4096, tag)) timed_out = true;
    STAMP(7);
    float o0 = (__uint_as_float(po[0][0]) + __uint_as_float(po[1][0])) + (__uint_as_float(po[2][0]) + __uint_as_float(po[3][0]));
    float o1 = (__uint_as_float(po[0][2]) + __uint_as_float(po[1][2])) + (__uint_as_float(po[2][2]) + __uint_as_float(po[3][2]));
    o0 += dpp<XOR8>(o0); o1 += dpp<XOR8>(o1);
    o0 = x32_sum(x16_sum(o0)); o1 = x32_sum(x16_sum(o1));
    const unsigned opk = pk_bf16(o0, o1);
#pragma unroll
    for (int j = 0; j < 8; ++j) { const unsigned oj = __builtin_amdgcn_readlane(opk, j); y0 = dot2(wop0[j], oj, y0); y1 = dot2(wop1[j], oj, y1); }
    if (timed_out) y0 = __builtin_nanf("");
    const unsigned yoff = tid < D / 2 ? (unsigned)((2 * tid / CPR) * (256 * CPR) + (2 * tid % CPR)) * 8 : OOB;
    __builtin_amdgcn_raw_buffer_store_b128(u32x4{__float_as_uint(y0), tag, __float_as_uint(y1), tag}, r_ws, yoff, WS_YA + wg * (CPR * 8), AUX_ST);
    STAMP(8);
    if (wave == 0) lds_arrive(lds, C_HOP1, lane);
  }
  if (wg >= NRED) return;
  if (wave >= 4) {
    const unsigned t = __builtin_amdgcn_raw_buffer_load_b32(r_ws, lane < CPR * 4 ? ((wave - 4) * CPR * 4 + lane) * 128 : OOB, WS_YA + wg * (256 * CPR * 8), AUX_SC1);
    asm volatile("" :: "v"(t));
  }
  lds_wait_ge(lds, C_HOP1, 1u);
  {
    constexpr int NLD = (256 * CPR / 2 + NTHR - 1) / NTHR;
    const float xo = tid < CPR ? bf_lo(a.x[wg * CPR + tid]) : 0.f;
    u32x4 t[NLD];
    unsigned yo[NLD];
#pragma unroll
    for (int k = 0; k < NLD; ++k) yo[k] = tid + NTHR * k < 256 * CPR / 2 ? (unsigned)(tid + NTHR * k) * 16 : OOB;
    if (!gather<NLD, 0>(t, r_ws, yo, WS_YA + wg * (256 * CPR * 8), tag)) timed_out = true;
    STAMP(9);
    float s0 = 0.f, s1 = 0.f;
#pragma unroll
    for (int k = 0; k < NLD; ++k) { s0 += __uint_as_float(t[k][0]); s1 += __uint_as_float(t[k][2]); }
    if (CPR / 2 <= 8) { s0 += dpp<XOR8>(s0); s1 += dpp<XOR8>(s1); }
    if (CPR / 2 <= 4) { s0 += dpp<ROR4>(s0); s1 += dpp<ROR4>(s1); }
    if (CPR / 2 <= 2) { s0 += dpp<XOR2>(s0); s1 += dpp<XOR2>(s1); }
    if (CPR / 2 <= 1) { s0 += dpp<XOR1>(s0); s1 += dpp<XOR1>(s1); }
    s0 = x32_sum(x16_sum(s0)); s1 = x32_sum(x16_sum(s1));
    if (timed_out) s0 = __builtin_nanf("");
    if (lane < CPR / 2) *reinterpret_cast<f32x2*>(lds + L_YA + wave * 64 + lane * 8) = f32x2{s0, s1};
    lds_barrier();
    if (tid < CPR) {
      float r = xo;
#pragma unroll
      for (int w = 0; w < 8; ++w) r += *reinterpret_cast<const float*>(lds + L_YA + w * 64 + tid * 4);
#define AUX_OUT 17
      __builtin_amdgcn_raw_buffer_store_b16((unsigned short)(pk_bf16(r, r) & 0xffffu), mk_rsrc(a.out, D * 2), (wg * CPR + tid) * 2, 0, AUX_OUT);
    }
    if (wg == 0 && tid == 64) __builtin_amdgcn_raw_buffer_store_b32(tag, r_ws, 0, WS_EPOCH, AUX_SC1);
    STAMP(10);
  }
}

}

extern "C" void kernel_launch(void* const* d_in, const int* in_sizes, int n_in, void* d_out, int out_size, void* d_ws, size_t ws_size,
                              hipStream_t stream) {
  (void)in_sizes; (void)n_in; (void)out_size; (void)ws_size;
  Args a;
  a.x = (const unsigned short*)d_in[0];
  a.k = (const unsigned char*)d_in[1];
  a.v = (const unsigned char*)d_in[2];
  a.wq = (const unsigned char*)d_in[3];
  a.wo = (const unsigned char*)d_in[4];
  a.wg = (const unsigned char*)d_in[6];
  a.wu = (const unsigned char*)d_in[7];
  a.wd = (const unsigned char*)d_in[8];
  a.out = (unsigned short*)d_out;
  a.ws = (unsigned char*)d_ws;
  (void)hipFuncSetAttribute((const void*)fused_decode, hipFuncAttributeMaxDynamicSharedMemorySize, LDS_BYTES);
  hipLaunchKernelGGL(fused_decode, dim3(NWG), dim3(NTHR), LDS_BYTES, stream, a);
}
```

```cpp
#include <hip/hip_runtime.h>
#include <hip/hip_bf16.h>
#include <cstdint>

namespace {

typedef unsigned u32x4 __attribute__((ext_vector_type(4)));
typedef unsigned u32x2 __attribute__((ext_vector_type(2)));
typedef float f32x4 __attribute__((ext_vector_type(4)));
typedef float f32x2 __attribute__((ext_vector_type(2)));
typedef __bf16 bf16x8 __attribute__((ext_vector_type(8)));
typedef __bf16 bf16x2 __attribute__((ext_vector_type(2)));
typedef __amdgpu_buffer_rsrc_t rsrc_t;

constexpr int D = 448, SK = 65536, DH = 256, FF = 1792, NQ = 4096;
constexpr int NWG = 256, NTHR = 512;
#define CH_W 10
#define NI 5
#define HK_PRIO 2
#define CP_REF 64
#define CP_AT 144
#define CP_WAIT 300
#define CP_OFF 20
#define CP_TAU 1.008f
#define CODE_PF 20000
#define CODE_WGS 32
#define PO16 1
#define ROWS_AT 252
#define PREWARM 4
#define PREWARM_AT 240
#define AUX_ST 16
#define AUX_LD 16
#define KFAST 0
#define VFAST 0
#define SLACK 4
#define Q_FIRST 700
#define Q_SLEEP 0
#define GU_AT 1500
#define CPR 8
constexpr int NRED = D / CPR;
constexpr unsigned TILE = 8192;
#define TILE_MAP {0, 3, 5, 6, 7, 10, 11, 1, 2, 4, 8, 9}
constexpr int TPOS[12] = TILE_MAP;
template <int FIRST, int N> __host__ __device__ constexpr unsigned scatter(unsigned v) {
  unsigned o = 0;
  for (int i = 0; i < N; ++i) o |= ((v >> i) & 1u) << TPOS[FIRST + i];
  return o * TILE;
}
constexpr unsigned SLOT1 = scatter<2, 5>(1), PAIR_MASK = scatter<2, 5>(30);
__device__ __forceinline__ unsigned next_pair(unsigned t) { return ((t | ~PAIR_MASK) + 1u) & PAIR_MASK; }
constexpr unsigned OOB = 0x40000000u;
constexpr float C2 = 0.0625f * 1.4426950408889634f;
constexpr unsigned SPIN_CAP = 1u << 22;

constexpr unsigned WS_EPOCH = 0;
constexpr unsigned LSTR = 4352;
constexpr unsigned WS_PO = 4096;
constexpr unsigned WS_YA = WS_PO + 8 * 32 * 32 * 16 * 8;
constexpr unsigned WS_GU = WS_YA + 256 * D * 8;
constexpr unsigned WS_QG = WS_GU + 224 * 4 * 128;
constexpr unsigned WS_ML = WS_QG + 128 * LSTR;
constexpr unsigned WS_END = WS_ML + 256 * LSTR;

constexpr int L_RED = 0;
constexpr int L_CTL = 1024;
constexpr int C_PART = L_CTL, C_QRDY = L_CTL + 4, C_TAG = L_CTL + 12, C_LANDED = L_CTL + 16, C_KHELP = L_CTL + 20, C_HRDY = L_CTL + 24, C_HOP1 = L_CTL + 28,
              C_DONE = L_CTL + 32, C_BASE = L_CTL + 40;
constexpr int L_ML = 1088;
constexpr int L_SC = 1152;
constexpr int L_H = 1184;
constexpr int L_YA = 1216;
constexpr int L_Q = 1792;
constexpr int L_XN = 2816;
constexpr int L_ACC = 4864;
constexpr int L_P = L_ACC + 4 * 2048;
#define LDS_KB 44
constexpr int LDS_BYTES = LDS_KB * 1024;

constexpr int AUX_NT = 2, AUX_SC1 = 16;
#define AUX_K 2
#define AUX_V 2
#define SB() __builtin_amdgcn_sched_barrier(0)
#define FENCE() asm volatile("" ::: "memory")

__device__ __forceinline__ float bf_lo(unsigned u) { return __builtin_bit_cast(float, u << 16); }
__device__ __forceinline__ float bf_hi(unsigned u) { return __builtin_bit_cast(float, u & 0xffff0000u); }
__device__ __forceinline__ unsigned pk_bf16(float lo, float hi) {
  bf16x2 v = {(__bf16)lo, (__bf16)hi};
  return __builtin_bit_cast(unsigned, v);
}
__device__ __forceinline__ float rnd_bf16(float x) { return (float)(__bf16)x; }
__device__ __forceinline__ float dot2(unsigned a, unsigned b, float acc) {
  return __builtin_amdgcn_fdot2_f32_bf16(__builtin_bit_cast(bf16x2, a), __builtin_bit_cast(bf16x2, b), acc, false);
}
template <int CTRL> __device__ __forceinline__ float dpp(float x) {
  return __builtin_bit_cast(float, __builtin_amdgcn_mov_dpp(__builtin_bit_cast(int, x), CTRL, 0xf, 0xf, true));
}
constexpr int XOR1 = 0xB1, XOR2 = 0x4E, XOR7 = 0x141, XOR15 = 0x140, XOR8 = 0x128, ROR4 = 0x124;
__device__ __forceinline__ float x16_max(float x) {
  auto s = __builtin_amdgcn_permlane16_swap(__float_as_uint(x), __float_as_uint(x), false, false);
  return fmaxf(__uint_as_float(s[0]), __uint_as_float(s[1]));
}
__device__ __forceinline__ float x32_max(float x) {
  auto s = __builtin_amdgcn_permlane32_swap(__float_as_uint(x), __float_as_uint(x), false, false);
  return fmaxf(__uint_as_float(s[0]), __uint_as_float(s[1]));
}
__device__ __forceinline__ float x16_sum(float x) {
  auto s = __builtin_amdgcn_permlane16_swap(__float_as_uint(x), __float_as_uint(x), false, false);
  return __uint_as_float(s[0]) + __uint_as_float(s[1]);
}
__device__ __forceinline__ float x32_sum(float x) {
  auto s = __builtin_amdgcn_permlane32_swap(__float_as_uint(x), __float_as_uint(x), false, false);
  return __uint_as_float(s[0]) + __uint_as_float(s[1]);
}
__device__ __forceinline__ float wave_sum(float x) {
  x += dpp<XOR1>(x); x += dpp<XOR2>(x); x += dpp<XOR7>(x); x += dpp<XOR15>(x);
  return x32_sum(x16_sum(x));
}
__device__ __forceinline__ float parity_sum(float x) {
  x += dpp<XOR2>(x); x += dpp<ROR4>(x); x += dpp<XOR8>(x);
  return x32_sum(x16_sum(x));
}
__device__ __forceinline__ float parity_max(float x) {
  x = fmaxf(x, dpp<XOR2>(x)); x = fmaxf(x, dpp<ROR4>(x)); x = fmaxf(x, dpp<XOR8>(x));
  return x32_max(x16_max(x));
}
__device__ __forceinline__ void lds_barrier() {
  asm volatile("s_waitcnt lgkmcnt(0)" ::: "memory");
  __builtin_amdgcn_s_barrier();
  FENCE();
}
__device__ __forceinline__ rsrc_t mk_rsrc(const void* p, unsigned bytes) {
  return __builtin_amdgcn_make_buffer_rsrc(const_cast<void*>(p), 0, bytes, 0x00020000);
}
__device__ __forceinline__ unsigned lds_ld(unsigned char* lds, int off) {
  return __builtin_amdgcn_readfirstlane(__hip_atomic_load(reinterpret_cast<unsigned*>(lds + off), __ATOMIC_RELAXED, __HIP_MEMORY_SCOPE_WORKGROUP));
}
template <int SLEEP = 1>
__device__ __forceinline__ void lds_wait_ge(unsigned char* lds, int off, unsigned want) {
#pragma nounroll
  for (unsigned spin = 0; spin < SPIN_CAP * 8u; ++spin) {
    if (lds_ld(lds, off) >= want) break;
    __builtin_amdgcn_s_sleep(SLEEP);
  }
  FENCE();
}
__device__ __forceinline__ void lds_arrive(unsigned char* lds, int off, int lane) {
  asm volatile("s_waitcnt lgkmcnt(0)" ::: "memory");
  if (lane == 0) __hip_atomic_fetch_add(reinterpret_cast<unsigned*>(lds + off), 1u, __ATOMIC_RELAXED, __HIP_MEMORY_SCOPE_WORKGROUP);
}
__device__ __forceinline__ void lds_add(unsigned char* lds, int off, unsigned v, int lane) {
  if (lane == 0) __hip_atomic_fetch_add(reinterpret_cast<unsigned*>(lds + off), v, __ATOMIC_RELAXED, __HIP_MEMORY_SCOPE_WORKGROUP);
}
__device__ __forceinline__ unsigned now() { return (unsigned)__builtin_amdgcn_s_memrealtime(); }
template <int SLEEP>
__device__ __forceinline__ void sleep_until(unsigned t0, unsigned ticks) {
#pragma nounroll
  for (unsigned spin = 0; spin < SPIN_CAP && now() - t0 < ticks; ++spin) __builtin_amdgcn_s_sleep(SLEEP);
}
__device__ __forceinline__ void chain_wait(unsigned char* lds, int idx) {
#pragma nounroll
  for (unsigned spin = 0; spin < SPIN_CAP * 8u; ++spin) {
    if ((int)lds_ld(lds, C_LANDED) >= idx + 1 - CH_W || lds_ld(lds, C_QRDY) != 0u) break;
    __builtin_amdgcn_s_sleep(1);
  }
  FENCE();
}
template <int N, int SLEEP>
__device__ __forceinline__ bool gather(u32x4 (&v)[N], rsrc_t rs, const unsigned (&voff)[N], unsigned soff, unsigned tag) {
  bool have[N];
#pragma unroll
  for (int k = 0; k < N; ++k) { have[k] = voff[k] == OOB; v[k] = u32x4{0u, 0u, 0u, 0u}; }
#pragma nounroll
  for (unsigned spin = 0; spin < SPIN_CAP; ++spin) {
    FENCE();
    u32x4 t[N];
#pragma unroll
    for (int k = 0; k < N; ++k) t[k] = __builtin_amdgcn_raw_buffer_load_b128(rs, have[k] ? OOB : voff[k], soff, AUX_LD);
    bool all = true;
#pragma unroll
    for (int k = 0; k < N; ++k) {
      const bool now = !have[k] && t[k][1] == tag && t[k][3] == tag;
      v[k][0] = now ? t[k][0] : v[k][0]; v[k][2] = now ? t[k][2] : v[k][2];
      have[k] = have[k] || now; all = all && have[k];
    }
    if (__builtin_amdgcn_ballot_w64(!all) == 0) return true;
    if (SLEEP > 0) __builtin_amdgcn_s_sleep(SLEEP);
  }
  return false;
}
#define STAMP(slot) do {} while (0)
#define PROG() do {} while (0)
#define PROG_OUT() do {} while (0)
#define PROGL() do {} while (0)

struct Args {
  const unsigned short* x;
  const unsigned char* k;
  const unsigned char* v;
  const unsigned char* wq;
  const unsigned char* wo;
  const unsigned char* wg;
  const unsigned char* wu;
  const unsigned char* wd;
  unsigned short* out;
  unsigned char* ws;
};

__host__ __device__ constexpr int piece(int e, int n) { return n < 2 ? 2 * e + n : 2 * n + e; }
template <int AUX>
__device__ __forceinline__ void issue(u32x4 (&r)[8], rsrc_t rs, unsigned v0, unsigned v1, unsigned soff) {
#pragma unroll
  for (int i = 0; i < 8; ++i) { r[i] = __builtin_amdgcn_raw_buffer_load_b128(rs, ((i & 1) ? v1 : v0) + (i >> 1) * 512, soff, AUX); SB(); }
}
__device__ __forceinline__ f32x4 mfma(u32x4 x, u32x4 y, f32x4 acc) {
  return __builtin_amdgcn_mfma_f32_16x16x32_bf16(__builtin_bit_cast(bf16x8, x), __builtin_bit_cast(bf16x8, y), acc, 0, 0, 0);
}
__device__ __forceinline__ void qk_mfma(const u32x4 (&r)[8], const u32x4 (&qf)[2], f32x4 (&d)[4]) {
#pragma unroll
  for (int j = 0; j < 4; ++j) d[j] = mfma(qf[1], r[2 * j + 1], mfma(qf[0], r[2 * j], f32x4{0.f, 0.f, 0.f, 0.f}));
  SB();
}
__device__ __forceinline__ unsigned short qk_score(const f32x4 (&d)[4], bool b0, bool b1) {
  float v[4];
#pragma unroll
  for (int j = 0; j < 4; ++j) {
    float e0 = d[j][0], e1 = d[j][1], e2 = d[j][2], e3 = d[j][3];
    asm("" : "+v"(e0), "+v"(e2));
    const float lo = b0 ? e1 : e0, hi = b0 ? e3 : e2;
    v[j] = b1 ? hi : lo;
  }
  const float u0 = (b0 ? v[1] : v[0]) + dpp<XOR1>(b0 ? v[0] : v[1]);
  const float u1 = (b0 ? v[3] : v[2]) + dpp<XOR1>(b0 ? v[2] : v[3]);
  const float w = (b1 ? u1 : u0) + dpp<XOR2>(b1 ? u0 : u1);
  return (unsigned short)(pk_bf16(w, w) & 0xffffu);
}
__device__ __forceinline__ void pv_mfma(const u32x4 (&vr)[8], u32x2 pk, int msel, f32x4& acc0, f32x4& acc1) {
#pragma unroll
  for (int q = 0; q < 4; ++q) {
    const u32x4 pa = {msel == 2 * q ? pk[0] : 0u, msel == 2 * q ? pk[1] : 0u, msel == 2 * q + 1 ? pk[0] : 0u, msel == 2 * q + 1 ? pk[1] : 0u};
    const unsigned sel = (q & 1) ? 0x07060302u : 0x05040100u;
    const int d0 = q >> 1, d1 = 2 + (q >> 1);
    const u32x4 b0 = {__builtin_amdgcn_perm(vr[2][d0], vr[0][d0], sel), __builtin_amdgcn_perm(vr[6][d0], vr[4][d0], sel),
                      __builtin_amdgcn_perm(vr[3][d0], vr[1][d0], sel), __builtin_amdgcn_perm(vr[7][d0], vr[5][d0], sel)};
    const u32x4 b1 = {__builtin_amdgcn_perm(vr[2][d1], vr[0][d1], sel), __builtin_amdgcn_perm(vr[6][d1], vr[4][d1], sel),
                      __builtin_amdgcn_perm(vr[3][d1], vr[1][d1], sel), __builtin_amdgcn_perm(vr[7][d1], vr[5][d1], sel)};
    acc0 = mfma(pa, b0, acc0);
    acc1 = mfma(pa, b1, acc1);
  }
  SB();
}
__device__ __forceinline__ void fma8(float (&s)[8], float xv, u32x4 w) {
#pragma unroll
  for (int i = 0; i < 4; ++i) { s[2 * i] = fmaf(xv, bf_lo(w[i]), s[2 * i]); s[2 * i + 1] = fmaf(xv, bf_hi(w[i]), s[2 * i + 1]); }
}
template <bool WHOLE>
__device__ __forceinline__ void load_qf(u32x4 (&qf)[2], const unsigned char* lds, int c, int g) {
#pragma unroll
  for (int e = 0; e < 2; ++e) qf[e] = *reinterpret_cast<const u32x4*>(lds + L_Q + ((c >> 2) & 1) * 512 + ((WHOLE ? 4 * e + g : piece(e, g)) * 4 + (c & 3)) * 16);
}

__global__ void __launch_bounds__(NTHR, 2) fused_decode(Args a) {
  extern __shared__ __attribute__((aligned(16))) unsigned char lds[];
  const int lane0 = threadIdx.x & 63, wave = __builtin_amdgcn_readfirstlane(threadIdx.x >> 6);
  const int wg = blockIdx.x, h = wg >> 5, s = wg & 31;
  unsigned long long pc;
  asm volatile("s_getpc_b64 %0" : "=s"(pc));
  const unsigned cpf = __builtin_amdgcn_raw_buffer_load_b32(mk_rsrc(reinterpret_cast<const void*>(pc), wg < CODE_WGS ? CODE_PF : 0), threadIdx.x * 128, 0, 0);
  SB();
  const bool helper = wave < 4;
  const unsigned wg_off = (unsigned)h * (4096 * TILE) + scatter<7, 5>(wg);
  const unsigned vw0 = (lane0 & 12) * 512 + (lane0 >> 4) * 64 + (lane0 & 3) * 16, vw1 = vw0 + 256;
  const rsrc_t r_k = mk_rsrc(a.k, 8u * SK * DH * 2);
  u32x4 ra[8], rb[8];
#pragma unroll
  for (int i = 0; i < 8; ++i) asm volatile("" : "=v"(ra[i]), "=v"(rb[i]));
  if (!helper) {
    const unsigned t0 = wg_off + scatter<0, 2>(wave);
    issue<AUX_K>(ra, r_k, vw0, vw1, t0);
    issue<AUX_K>(rb, r_k, vw0, vw1, t0 + SLOT1);
  }
  const rsrc_t r_ws = mk_rsrc(a.ws, WS_END);
#define LANE_VARS int tid = threadIdx.x; asm volatile("" : "+v"(tid)); const int lane = tid & 63, c = lane & 15, g = lane >> 4; (void)c; (void)g
#define SCORE_VARS const bool b0 = (c & 1) != 0, b1 = (c & 2) != 0; const int sc_lane = (g & 1) * 32 + c * 2
#define STAMP0() do {} while (0)
  bool timed_out = false;
  unsigned tag;
  unsigned draw[8], wraw[16];
#pragma unroll
  for (int j = 0; j < 8; ++j) asm volatile("" : "=v"(draw[j]), "=v"(wraw[j]), "=v"(wraw[8 + j]));

  if (__builtin_expect(helper, 1)) {
    __builtin_amdgcn_s_setprio(3);
    LANE_VARS;
    const bool qwg = (s & 8) == 0;
    const int qsl = (s & 7) * 2 + (s >> 4);
    const int fb = h * 16 + qsl;
    const int wrow = (lane >> (qwg ? 2 : 3)) + (qwg ? 16 * wave : (fb & 3) * 112 + (wave >> 1) * 56), wstep = qwg ? 64 : 8;
    const u32x4 xv = __builtin_amdgcn_raw_buffer_load_b128(mk_rsrc(a.x, D * 2), lane * 16, 0, 0);
    const unsigned epoch = __builtin_amdgcn_raw_buffer_load_b32(r_ws, 0, WS_EPOCH, AUX_SC1);
    u32x4 wv[7];
    {
      const unsigned rowb = qwg ? NQ * 2 : FF * 2;
      const rsrc_t r0 = mk_rsrc(qwg ? a.wq : (wave & 1) ? a.wu : a.wg, (qwg || fb < 112) ? D * rowb : 0u);
      const unsigned v0 = wrow * rowb + (lane & (qwg ? 3 : 7)) * 16, s0 = qwg ? (h * 512 + qsl * 32) * 2 : (fb >> 2) * 128;
#pragma unroll
      for (int i = 0; i < 7; ++i) wv[i] = __builtin_amdgcn_raw_buffer_load_b128(r0, v0, s0 + i * wstep * rowb, AUX_NT);
    }
    const unsigned t_start = now();
    unsigned t_ref = 0, n_ref = 0;
    SB();
    STAMP0();
    SCORE_VARS;
    if (tid < 16) *reinterpret_cast<unsigned*>(lds + L_CTL + tid * 4) = 0u;
    lds_barrier();

    {
      float xs[8];
#pragma unroll
      for (int i = 0; i < 4; ++i) { xs[2 * i] = bf_lo(xv[i]); xs[2 * i + 1] = bf_hi(xv[i]); }
      float sm = 0.f;
#pragma unroll
      for (int i = 0; i < 8; ++i) sm += xs[i];
      const float mean = wave_sum(sm) * (1.0f / D);
      float sq = 0.f;
#pragma unroll
      for (int i = 0; i < 8; ++i) { const float d = xs[i] - mean; sq = fmaf(d, d, sq); }
      sq = lane < D / 8 ? sq : 0.f;
      const float var = wave_sum(sq) * (1.0f / D);
      const float mean_b = rnd_bf16(mean);
      const float den_b = rnd_bf16(__builtin_amdgcn_sqrtf(rnd_bf16(rnd_bf16(var) + 1e-5f)));
      float rcp = __builtin_amdgcn_rcpf(den_b);
      rcp = fmaf(fmaf(-den_b, rcp, 1.0f), rcp, rcp);
#pragma unroll
      for (int i = 0; i < 8; ++i) {
        const float n = rnd_bf16(xs[i] - mean_b), q = n * rcp;
        xs[i] = rnd_bf16(fmaf(fmaf(-den_b, q, n), rcp, q));
      }
      f32x4* xp = reinterpret_cast<f32x4*>(lds + L_XN + lane * 32);
      xp[0] = f32x4{xs[0], xs[1], xs[2], xs[3]}; xp[1] = f32x4{xs[4], xs[5], xs[6], xs[7]};
      FENCE();
    }
    tag = __builtin_amdgcn_readfirstlane(epoch) + 1u;
    asm volatile("" :: "v"(cpf));
    {
      float sacc[8] = {0.f, 0.f, 0.f, 0.f, 0.f, 0.f, 0.f, 0.f};
#pragma unroll
      for (int i = 0; i < 7; ++i) fma8(sacc, *reinterpret_cast<const float*>(lds + L_XN + (wrow + wstep * i) * 4), wv[i]);
#pragma unroll
      for (int i = 0; i < 8; ++i) { float t = sacc[i]; const float r = dpp<ROR4>(t); t += qwg ? r : 0.f; t += dpp<XOR8>(t); sacc[i] = x32_sum(x16_sum(t)); }
      if (lane < 8) {
        f32x4* p = reinterpret_cast<f32x4*>(lds + L_RED + wave * 256 + lane * 32);
        p[0] = f32x4{sacc[0], sacc[1], sacc[2], sacc[3]}; p[1] = f32x4{sacc[4], sacc[5], sacc[6], sacc[7]};
      }
      lds_arrive(lds, C_PART, lane);
      if (wave == 0) {
        lds_wait_ge(lds, C_PART, 4u);
        if (qwg) {
          float q0 = 0.f, q1 = 0.f;
#pragma unroll
          for (int w = 0; w < 4; ++w) { const f32x2 t = *reinterpret_cast<const f32x2*>(lds + L_RED + w * 256 + c * 8); q0 += t[0]; q1 += t[1]; }
          __builtin_amdgcn_raw_buffer_store_b64(u32x2{pk_bf16(q0, q1), tag}, r_ws, lane < 16 ? lane * 8 : OOB, WS_QG + (h * 16 + qsl) * LSTR, AUX_SC1);
        } else {
          const int o = ((lane >> 2) & 1) * 256 + (lane >> 3) * 32 + (lane & 3) * 8;
          const f32x2 t0 = *reinterpret_cast<const f32x2*>(lds + L_RED + o), t1 = *reinterpret_cast<const f32x2*>(lds + L_RED + 512 + o);
          __builtin_amdgcn_raw_buffer_store_b128(u32x4{__float_as_uint(t0[0] + t1[0]), tag, __float_as_uint(t0[1] + t1[1]), tag}, r_ws,
                                                 fb < 112 ? (lane >> 3) * 512 + (lane & 7) * 16 : OOB, WS_GU + (fb >> 2) * 4096 + (fb & 3) * 128, AUX_SC1);
        }
        STAMP(1);
      }
    }
    const bool ffn = wg < FF / 8;
    if (wave == 0) {
      u32x4 qg[2];
      const unsigned qo[2] = {(lane >> 2) * LSTR + (lane & 3) * 32, (lane >> 2) * LSTR + (lane & 3) * 32 + 16};
      sleep_until<1>(t_start, Q_FIRST);
      timed_out = !gather<2, Q_SLEEP>(qg, r_ws, qo, WS_QG + h * 16 * LSTR, tag);
      *reinterpret_cast<u32x4*>(lds + L_Q + lane * 16) = u32x4{qg[0][0], qg[0][2], qg[1][0], qg[1][2]};
      if (lane == 0) *reinterpret_cast<unsigned*>(lds + C_TAG) = tag;
      lds_arrive(lds, C_QRDY, lane);
    }
    if (wave != 0) {
      __builtin_amdgcn_s_setprio(HK_PRIO);
      const int slot = NI + wave - 1;
      u32x4 kt[4][8];
#pragma unroll
      for (int i = 0; i < 4; ++i) { chain_wait(lds, 4 * slot + i); issue<AUX_K>(kt[i], r_k, vw0, vw1, wg_off + scatter<2, 5>(slot) + scatter<0, 2>(i)); }
      asm volatile("s_waitcnt vmcnt(24)" ::: "memory"); lds_arrive(lds, C_LANDED, lane); PROGL();
      asm volatile("s_waitcnt vmcnt(16)" ::: "memory"); lds_arrive(lds, C_LANDED, lane); PROGL();
      asm volatile("s_waitcnt vmcnt(8)" ::: "memory"); lds_arrive(lds, C_LANDED, lane); PROGL();
      asm volatile("s_waitcnt vmcnt(0)" ::: "memory"); lds_arrive(lds, C_LANDED, lane); PROGL();
      lds_wait_ge(lds, C_QRDY, 1u);
      u32x4 qf[2];
      load_qf<true>(qf, lds, c, g);
#pragma unroll
      for (int i = 0; i < 4; ++i) {
        f32x4 d[4];
        qk_mfma(kt[i], qf, d);
        *reinterpret_cast<unsigned short*>(lds + L_P + i * 2048 + slot * 64 + sc_lane) = qk_score(d, b0, b1);
        PROG();
      }
      lds_arrive(lds, C_KHELP, lane);
      lds_add(lds, C_DONE, 4u, lane);
      __builtin_amdgcn_s_setprio(3);
    }
    STAMP(3);
    {
      if (wave == 3) {
        sleep_until<32>(t_start, GU_AT);
        u32x4 gu[1];
        const unsigned go[1] = {(ffn && lane < 32) ? (unsigned)lane * 16 : OOB};
        if (!gather<1, 64>(gu, r_ws, go, WS_GU + wg * 512, tag)) timed_out = true;
        float g0 = __uint_as_float(gu[0][0]), g1 = __uint_as_float(gu[0][2]);
        g0 += dpp<XOR8>(g0); g1 += dpp<XOR8>(g1);
        g0 = rnd_bf16(x16_sum(g0)); g1 = rnd_bf16(x16_sum(g1));
        const float u0 = dpp<ROR4>(g0), u1 = dpp<ROR4>(g1);
        const float h0 = rnd_bf16((g0 > 0.f ? g0 : g0 * 0.f) * u0), h1 = rnd_bf16((g1 > 0.f ? g1 : g1 * 0.f) * u1);
        if (lane < 4) *reinterpret_cast<f32x2*>(lds + L_H + lane * 8) = f32x2{h0, h1};
      }
      if (wave == 1) { lds_wait_ge<2>(lds, C_DONE, CP_REF); t_ref = now(); n_ref = lds_ld(lds, C_DONE) & 0xffffu; }
    }
    STAMP(11);
    if (wave == 1) {
      lds_wait_ge<2>(lds, C_DONE, 0x10000u * 4u + CP_AT);
      const unsigned t_now = now(), n_cp = lds_ld(lds, C_DONE) & 0xffffu;
      const float tau = (float)(t_now - t_ref) / (float)(n_cp - n_ref);
      const unsigned t_cp = t_now - (unsigned)(tau * (float)(n_cp - CP_AT));
      const int hd = lane & 1;
      float mw[4], lw[4];
#pragma unroll
      for (int w = 0; w < 4; ++w) { const f32x2 t = *reinterpret_cast<const f32x2*>(lds + L_ML + w * 16 + hd * 8); mw[w] = t[0]; lw[w] = t[1]; }
      const float M = fmaxf(fmaxf(mw[0], mw[1]), fmaxf(mw[2], mw[3]));
      float L = 0.f;
#pragma unroll
      for (int w = 0; w < 4; ++w) L = fmaf(__builtin_amdgcn_exp2f((mw[w] - M) * C2), lw[w], L);
      __builtin_amdgcn_raw_buffer_store_b128(u32x4{(lane & 2) ? t_cp : __float_as_uint(M), tag, __float_as_uint((lane & 2) ? tau : L), tag}, r_ws,
                                             lane < 8 ? lane * 16 : OOB, WS_ML + wg * LSTR, AUX_SC1);
      sleep_until<16>(t_now, CP_WAIT);
      u32x4 ml[2];
      const unsigned mo[2] = {(lane >> 2) * LSTR + (lane & 3) * 16, (16 + (lane >> 2)) * LSTR + (lane & 3) * 16};
      if (!gather<2, 32>(ml, r_ws, mo, WS_ML + h * 32 * LSTR, tag)) timed_out = true;
      const float Ma = __uint_as_float(ml[0][0]), Mb = __uint_as_float(ml[1][0]);
      float Mg = fmaxf(Ma, Mb);
      Mg = fmaxf(Mg, dpp<ROR4>(Mg)); Mg = fmaxf(Mg, dpp<XOR8>(Mg)); Mg = x32_max(x16_max(Mg));
      float Lg = __builtin_amdgcn_exp2f((Ma - Mg) * C2) * __uint_as_float(ml[0][2]) + __builtin_amdgcn_exp2f((Mb - Mg) * C2) * __uint_as_float(ml[1][2]);
      if (lane & 2) Lg = (lane & 1) ? __uint_as_float(ml[0][2]) + __uint_as_float(ml[1][2]) : (float)(int)(ml[0][0] - t_cp) + (float)(int)(ml[1][0] - t_cp);
      Lg += dpp<ROR4>(Lg); Lg += dpp<XOR8>(Lg); Lg = x32_sum(x16_sum(Lg));
      if (lane < 2) {
#pragma unroll
        for (int w = 0; w < 4; ++w) *reinterpret_cast<float*>(lds + L_SC + w * 8 + lane * 4) = timed_out ? __builtin_nanf("") : __builtin_amdgcn_exp2f((mw[w] - Mg) * C2) / Lg;
      }
      {
        const float tm = dpp<XOR1>(Lg) * (CP_TAU / 32);
        if (lane == 2 && !timed_out)
          *reinterpret_cast<u32x2*>(lds + C_BASE) = u32x2{t_cp + (unsigned)(int)(Lg * (1.0f / 32) + (float)CP_OFF - tm * (float)CP_AT), (unsigned)(tm * 256.f)};
      }
      STAMP(14);
    }
    {
      lds_wait_ge<4>(lds, C_DONE, 0x10000u * 4u + ROWS_AT);
      const rsrc_t r_wo = mk_rsrc(a.wo, NQ * D * 2), r_d = mk_rsrc(a.wd, ffn ? FF * D * 2 : 0);
      const unsigned coff = tid < D / 2 ? tid * 4 : OOB;
#pragma unroll
      for (int j = 0; j < 8; ++j) draw[j] = __builtin_amdgcn_raw_buffer_load_b32(r_d, coff, (wg * 8 + j) * (D * 2), AUX_NT);
#pragma unroll
      for (int j = 0; j < 16; ++j) wraw[j] = __builtin_amdgcn_raw_buffer_load_b32(r_wo, coff, (wg * 16 + j) * (D * 2), AUX_NT);
      SB();
    }
  } else {
    const int w = wave - 4;
    const unsigned t0 = wg_off + scatter<0, 2>(wave);
    LANE_VARS;
    SCORE_VARS;
    STAMP0();
    const rsrc_t r_v = mk_rsrc(a.v, 8u * SK * DH * 2);
    const unsigned vv0 = g * 2048 + piece(0, c >> 2) * 64 + (c & 3) * 16, vv1 = g * 2048 + piece(1, c >> 2) * 64 + (c & 3) * 16;
    const bool kwhole = w < KFAST, vwhole = w < VFAST;
    const unsigned vk0 = kwhole ? vw0 : (c >> 2) * 2048 + piece(0, g) * 64 + (c & 3) * 16, vk1 = kwhole ? vw1 : (c >> 2) * 2048 + piece(1, g) * 64 + (c & 3) * 16;
    u32x4 rx[NI - 2][8];
    static_assert(CH_W >= 5, "");
    lds_barrier();
    asm volatile("s_waitcnt vmcnt(8)" ::: "memory"); lds_arrive(lds, C_LANDED, lane); PROGL();
#define CHAIN_STEP(R, SLOT) do { \
      constexpr bool first_ = CH_W < 5;     \
      if (first_ && lds_ld(lds, C_QRDY) == 0u) { asm volatile("s_waitcnt vmcnt(0)" ::: "memory"); lds_arrive(lds, C_LANDED, lane); PROGL(); } \
      chain_wait(lds, 4 * (SLOT) + w); \
      issue<AUX_K>(R, r_k, vw0, vw1, t0 + scatter<2, 5>(SLOT)); \
      if (!first_ && lds_ld(lds, C_QRDY) == 0u) { asm volatile("s_waitcnt vmcnt(8)" ::: "memory"); lds_arrive(lds, C_LANDED, lane); PROGL(); } } while (0)
#pragma unroll
    for (int i = 2; i < NI; ++i) CHAIN_STEP(rx[i - 2], i);
#undef CHAIN_STEP
    if (lds_ld(lds, C_QRDY) == 0u) { asm volatile("s_waitcnt vmcnt(0)" ::: "memory"); lds_arrive(lds, C_LANDED, lane); PROGL(); }
    lds_wait_ge(lds, C_QRDY, 1u);
    tag = lds_ld(lds, C_TAG);
    STAMP(2);
    u32x4 qf[2];
    load_qf<true>(qf, lds, c, g);
    unsigned char* const pl = lds + L_P + w * 2048;
    {
      f32x4 d[4];
      unsigned short* sw = reinterpret_cast<unsigned short*>(pl + sc_lane);
      unsigned tp = scatter<2, 5>(NI + 3);
#define K_STEP(R, ISSUE_NEXT, SLOT) do { qk_mfma(R, qf, d); PROG(); ISSUE_NEXT; sw[(SLOT) * 32] = qk_score(d, b0, b1); } while (0)
      K_STEP(ra, issue<AUX_K>(ra, r_k, vk0, vk1, t0 + tp), 0);
      K_STEP(rb, issue<AUX_K>(rb, r_k, vk0, vk1, t0 + tp + SLOT1), 1);
#pragma unroll
      for (int i = 2; i < NI; ++i) K_STEP(rx[i - 2], (void)0, i);
      lds_add(lds, C_DONE, NI, lane);
      if (!kwhole) load_qf<false>(qf, lds, c, g);
      sw += (NI + 3) * 32;
      for (int it = 1; it < (29 - NI) / 2; ++it) {
        tp = next_pair(tp);
        K_STEP(ra, issue<AUX_K>(ra, r_k, vk0, vk1, t0 + tp), 0);
        K_STEP(rb, issue<AUX_K>(rb, r_k, vk0, vk1, t0 + tp + SLOT1), 1);
        lds_add(lds, C_DONE, 2u, lane);
        sw += 2 * 32;
      }
      K_STEP(ra, issue<AUX_V>(ra, r_v, vv0, vv1, t0), 0);
      K_STEP(rb, issue<AUX_V>(rb, r_v, vv0, vv1, t0 + SLOT1), 1);
#undef K_STEP
    }
    STAMP(12);
    lds_wait_ge(lds, C_KHELP, 3u);
    {
      u32x4* pp = reinterpret_cast<u32x4*>(pl + lane * 32);
      u32x4 t[2] = {pp[0], pp[1]};
      float mx = bf_lo(t[0][0]);
#pragma unroll
      for (int j = 0; j < 2; ++j)
#pragma unroll
        for (int i = 0; i < 4; ++i) mx = fmaxf(mx, fmaxf(bf_lo(t[j][i]), bf_hi(t[j][i])));
      mx = parity_max(mx);
      const float mc = mx * C2;
      float lp = 0.f;
#pragma unroll
      for (int j = 0; j < 2; ++j) {
#pragma unroll
        for (int i = 0; i < 4; ++i) {
          const float p0 = __builtin_amdgcn_exp2f(fmaf(bf_lo(t[j][i]), C2, -mc)), p1 = __builtin_amdgcn_exp2f(fmaf(bf_hi(t[j][i]), C2, -mc));
          lp += p0 + p1; t[j][i] = pk_bf16(p0, p1);
        }
        pp[j] = t[j];
      }
      lp = parity_sum(lp);
      if (lane < 2) *reinterpret_cast<f32x2*>(lds + L_ML + w * 16 + lane * 8) = f32x2{mx, lp};
      asm volatile("s_waitcnt lgkmcnt(0)" ::: "memory");
      lds_add(lds, C_DONE, 0x10002u, lane);
    }
    f32x4 acc0 = {0.f, 0.f, 0.f, 0.f}, acc1 = {0.f, 0.f, 0.f, 0.f};
    {
      const int msel = c >> 1;
      const unsigned char* pr = pl + (c & 1) * 32 + g * 8;
      const unsigned vf0 = g * 2048 + c * 16;
      float* const rec = reinterpret_cast<float*>(lds + L_ACC + w * 2048) + 8 * (c & 3) + g;
      auto put = [&](bool whole) {
        const int e0 = (whole ? c >> 2 : piece(0, c >> 2)) * 32, e1 = (whole ? 4 + (c >> 2) : piece(1, c >> 2)) * 32;
#pragma unroll
        for (int k = 0; k < 4; ++k) { float* r = rec + (k & 1) * 256 + ((k >> 1) ? e1 : e0); r[0] = acc0[k]; r[4] = acc1[k]; }
      };
      auto get = [&](bool whole) {
        const int e0 = (whole ? c >> 2 : piece(0, c >> 2)) * 32, e1 = (whole ? 4 + (c >> 2) : piece(1, c >> 2)) * 32;
#pragma unroll
        for (int k = 0; k < 4; ++k) { const float* r = rec + (k & 1) * 256 + ((k >> 1) ? e1 : e0); acc0[k] = r[0]; acc1[k] = r[4]; }
      };
      unsigned tp = scatter<2, 5>(2);
      bool cur = false;
#define V_STEP(R, ISSUE_NEXT) do { const u32x2 pk_ = *reinterpret_cast<const u32x2*>(pr); pr += 64; pv_mfma(R, pk_, msel, acc0, acc1); PROG(); ISSUE_NEXT; } while (0)
      for (int it = 0; it < 15; ++it) {
        u32x4 cw;
#pragma nounroll
        for (unsigned spin = 0; spin < SPIN_CAP; ++spin) {
          FENCE();
          cw = *reinterpret_cast<const u32x4*>(lds + C_DONE);
          if ((__builtin_amdgcn_readfirstlane(cw[0]) & 0xffffu) + SLACK >= (unsigned)(128 + 8 * it)) break;
          __builtin_amdgcn_s_sleep(2);
        }
        const unsigned done = __builtin_amdgcn_readfirstlane(cw[0]) & 0xffffu, tau = __builtin_amdgcn_readfirstlane(cw[3]);
        const bool nxt = vwhole || tau != 0u && (int)(now() - __builtin_amdgcn_readfirstlane(cw[2]) - ((done * tau) >> 8)) > 0;
        const unsigned n0 = nxt ? vf0 : vv0, n1 = nxt ? vf0 + 256 : vv1;
        V_STEP(ra, issue<AUX_V>(ra, r_v, n0, n1, t0 + tp));
        V_STEP(rb, issue<AUX_V>(rb, r_v, n0, n1, t0 + tp + SLOT1));
        lds_add(lds, C_DONE, 2u, lane);
        if (nxt != cur) { put(cur); FENCE(); get(nxt); cur = nxt; }
        FENCE();
        tp = next_pair(tp);
      }
      V_STEP(ra, lds_add(lds, C_DONE, 1u, lane));
      V_STEP(rb, (void)0);
#undef V_STEP
      STAMP(3);
      put(cur);
      asm volatile("" :: "v"(cpf));
    }
  }
  LANE_VARS;
  PROG_OUT();
  STAMP(4);
  lds_barrier();
  STAMP(5);
  if (!helper) {
    const int u = tid - 256;
    float o0 = 0.f, o1 = 0.f;
#pragma unroll
    for (int w = 0; w < 4; ++w) {
      const float sc = *reinterpret_cast<const float*>(lds + L_SC + w * 8 + (u >> 7) * 4);
      const f32x2 t = *reinterpret_cast<const f32x2*>(lds + L_ACC + w * 2048 + u * 8);
      o0 = fmaf(sc, t[0], o0); o1 = fmaf(sc, t[1], o1);
    }
    __builtin_amdgcn_raw_buffer_store_b128(u32x4{__float_as_uint(o0), tag, __float_as_uint(o1), tag}, r_ws, (u >> 3) * 4096 + (u & 7) * 16,
                                           WS_PO + h * (32 * 4096) + s * 128, AUX_ST);
  }
  STAMP(6);
  if (helper) {
    unsigned wop0[8], wop1[8];
    float y0 = 0.f, y1 = 0.f;
#pragma unroll
    for (int j = 0; j < 8; ++j) {
      const float hv = *reinterpret_cast<const float*>(lds + L_H + j * 4);
      y0 = fmaf(hv, bf_lo(draw[j]), y0); y1 = fmaf(hv, bf_hi(draw[j]), y1);
      wop0[j] = __builtin_amdgcn_perm(wraw[2 * j + 1], wraw[2 * j], 0x05040100u); wop1[j] = __builtin_amdgcn_perm(wraw[2 * j + 1], wraw[2 * j], 0x07060302u);
    }
    asm volatile("" : "+v"(y0), "+v"(y1), "+v"(wop0[0]), "+v"(wop0[1]), "+v"(wop0[2]), "+v"(wop0[3]), "+v"(wop0[4]), "+v"(wop0[5]), "+v"(wop0[6]), "+v"(wop0[7]),
                      "+v"(wop1[0]), "+v"(wop1[1]), "+v"(wop1[2]), "+v"(wop1[3]), "+v"(wop1[4]), "+v"(wop1[5]), "+v"(wop1[6]), "+v"(wop1[7]));
    u32x4 po[4];
    const unsigned oo[4] = {(unsigned)lane * 16, (unsigned)lane * 16 + 1024, (unsigned)lane * 16 + 2048, (unsigned)lane * 16 + 3072};
    if (!gather<4, 0>(po, r_ws, oo, WS_PO + wg * 4096, tag)) timed_out = true;
    STAMP(7);
    float o0 = (__uint_as_float(po[0][0]) + __uint_as_float(po[1][0])) + (__uint_as_float(po[2][0]) + __uint_as_float(po[3][0]));
    float o1 = (__uint_as_float(po[0][2]) + __uint_as_float(po[1][2])) + (__uint_as_float(po[2][2]) + __uint_as_float(po[3][2]));
    o0 += dpp<XOR8>(o0); o1 += dpp<XOR8>(o1);
    o0 = x32_sum(x16_sum(o0)); o1 = x32_sum(x16_sum(o1));
    const unsigned opk = pk_bf16(o0, o1);
#pragma unroll
    for (int j = 0; j < 8; ++j) { const unsigned oj = __builtin_amdgcn_readlane(opk, j); y0 = dot2(wop0[j], oj, y0); y1 = dot2(wop1[j], oj, y1); }
    if (timed_out) y0 = __builtin_nanf("");
    const unsigned yoff = tid < D / 2 ? (unsigned)((2 * tid / CPR) * (256 * CPR) + (2 * tid % CPR)) * 8 : OOB;
    __builtin_amdgcn_raw_buffer_store_b128(u32x4{__float_as_uint(y0), tag, __float_as_uint(y1), tag}, r_ws, yoff, WS_YA + wg * (CPR * 8), AUX_ST);
    STAMP(8);
    if (wave == 0) lds_arrive(lds, C_HOP1, lane);
  }
  if (wg >= NRED) return;
  if (wave >= 4) {
    const unsigned t = __builtin_amdgcn_raw_buffer_load_b32(r_ws, lane < CPR * 4 ? ((wave - 4) * CPR * 4 + lane) * 128 : OOB, WS_YA + wg * (256 * CPR * 8), AUX_SC1);
    asm volatile("" :: "v"(t));
  }
  lds_wait_ge(lds, C_HOP1, 1u);
  {
    constexpr int NLD = (256 * CPR / 2 + NTHR - 1) / NTHR;
    const float xo = tid < CPR ? bf_lo(a.x[wg * CPR + tid]) : 0.f;
    u32x4 t[NLD];
    unsigned yo[NLD];
#pragma unroll
    for (int k = 0; k < NLD; ++k) yo[k] = tid + NTHR * k < 256 * CPR / 2 ? (unsigned)(tid + NTHR * k) * 16 : OOB;
    if (!gather<NLD, 0>(t, r_ws, yo, WS_YA + wg * (256 * CPR * 8), tag)) timed_out = true;
    STAMP(9);
    float s0 = 0.f, s1 = 0.f;
#pragma unroll
    for (int k = 0; k < NLD; ++k) { s0 += __uint_as_float(t[k][0]); s1 += __uint_as_float(t[k][2]); }
    if (CPR / 2 <= 8) { s0 += dpp<XOR8>(s0); s1 += dpp<XOR8>(s1); }
    if (CPR / 2 <= 4) { s0 += dpp<ROR4>(s0); s1 += dpp<ROR4>(s1); }
    if (CPR / 2 <= 2) { s0 += dpp<XOR2>(s0); s1 += dpp<XOR2>(s1); }
    if (CPR / 2 <= 1) { s0 += dpp<XOR1>(s0); s1 += dpp<XOR1>(s1); }
    s0 = x32_sum(x16_sum(s0)); s1 = x32_sum(x16_sum(s1));
    if (timed_out) s0 = __builtin_nanf("");
    if (lane < CPR / 2) *reinterpret_cast<f32x2*>(lds + L_YA + wave * 64 + lane * 8) = f32x2{s0, s1};
    lds_barrier();
    if (tid < CPR) {
      float r = xo;
#pragma unroll
      for (int w = 0; w < 8; ++w) r += *reinterpret_cast<const float*>(lds + L_YA + w * 64 + tid * 4);
#define AUX_OUT 17
      __builtin_amdgcn_raw_buffer_store_b16((unsigned short)(pk_bf16(r, r) & 0xffffu), mk_rsrc(a.out, D * 2), (wg * CPR + tid) * 2, 0, AUX_OUT);
    }
    if (wg == 0 && tid == 64) __builtin_amdgcn_raw_buffer_store_b32(tag, r_ws, 0, WS_EPOCH, AUX_SC1);
    STAMP(10);
  }
}

}

extern "C" void kernel_launch(void* const* d_in, const int* in_sizes, int n_in, void* d_out, int out_size, void* d_ws, size_t ws_size,
                              hipStream_t stream) {
  (void)in_sizes; (void)n_in; (void)out_size; (void)ws_size;
  Args a;
  a.x = (const unsigned short*)d_in[0];
  a.k = (const unsigned char*)d_in[1];
  a.v = (const unsigned char*)d_in[2];
  a.wq = (const unsigned char*)d_in[3];
  a.wo = (const unsigned char*)d_in[4];
  a.wg = (const unsigned char*)d_in[6];
  a.wu = (const unsigned char*)d_in[7];
  a.wd = (const unsigned char*)d_in[8];
  a.out = (unsigned short*)d_out;
  a.ws = (unsigned char*)d_ws;
  (void)hipFuncSetAttribute((const void*)fused_decode, hipFuncAttributeMaxDynamicSharedMemorySize, LDS_BYTES);
  hipLaunchKernelGGL(fused_decode, dim3(NWG), dim3(NTHR), LDS_BYTES, stream, a);
}
```

```cpp
#include <hip/hip_runtime.h>
#include <hip/hip_bf16.h>
#include <cstdint>

namespace {

typedef unsigned u32x4 __attribute__((ext_vector_type(4)));
typedef unsigned u32x2 __attribute__((ext_vector_type(2)));
typedef float f32x4 __attribute__((ext_vector_type(4)));
typedef float f32x2 __attribute__((ext_vector_type(2)));
typedef __bf16 bf16x8 __attribute__((ext_vector_type(8)));
typedef __bf16 bf16x2 __attribute__((ext_vector_type(2)));
typedef __amdgpu_buffer_rsrc_t rsrc_t;

constexpr int D = 448, SK = 65536, DH = 256, FF = 1792, NQ = 4096;
constexpr int NWG = 256, NTHR = 512;
#define CH_W 10
#define NI 5
#define HK_PRIO 2
#define CP_REF 64
#define CP_AT 116
#define CP_WAIT 250
#define CP_OFF -20
#define CP_TAU 1.0f
#define CODE_PF 20000
#define CODE_WGS 32
#define PO16 1
#define ROWS_AT 252
#define PREWARM 4
#define PREWARM_AT 240
#define AUX_ST 16
#define AUX_LD 16
#define CP_STEP 60
#define CP_STEPS 30
#define CP_AHEAD 25
#define SLACK 4
#define Q_FIRST 700
#define Q_SLEEP 0
#define GU_AT 1500
#define CPR 8
constexpr int NRED = D / CPR;
#define KEY_MAP {3, 4, 8, 7, 6, 0, 10, 14, 11, 15, 1, 9, 2, 13, 5, 12}
constexpr int KPOS[16] = KEY_MAP;
template <int FIRST, int N> __host__ __device__ constexpr unsigned scatter(unsigned v) {
  unsigned o = 0;
  for (int i = 0; i < N; ++i) o |= ((v >> i) & 1u) << (KPOS[FIRST + i] + 9);
  return o;
}
#define OFF_J(j) scatter<0, 2>(j)
#define OFF_G(G) scatter<2, 2>(G)
#define OFF_W(w) scatter<4, 2>(w)
#define OFF_SLOT(t) scatter<6, 5>(t)
#define OFF_S(s) scatter<11, 5>(s)
constexpr unsigned SLOT1 = OFF_SLOT(1);
constexpr unsigned OOB = 0x40000000u;
constexpr float C2 = 0.0625f * 1.4426950408889634f;
constexpr unsigned SPIN_CAP = 1u << 22;

constexpr unsigned WS_EPOCH = 0;
constexpr unsigned LSTR = 4352;
constexpr unsigned WS_PO = 4096;
#define PO_MAP 0
#define YA_ROT 0
__device__ __forceinline__ unsigned po_line(unsigned c, unsigned p) {
  return PO_MAP == 0 ? c * 4096 + p * 128 : PO_MAP == 1 ? p * 4096 + c * 128 : (c ^ p) * 4096 + p * 128;
}
constexpr unsigned WS_YA = WS_PO + 8 * 32 * 32 * 16 * 8;
constexpr unsigned WS_GU = WS_YA + 256 * D * 8;
constexpr unsigned WS_QG = WS_GU + 224 * 4 * 128;
constexpr unsigned WS_ML = WS_QG + 128 * LSTR;
constexpr unsigned WS_END = WS_ML + 256 * LSTR;

constexpr int L_RED = 0;
constexpr int L_CTL = 1024;
constexpr int C_PART = L_CTL, C_QRDY = L_CTL + 4, C_TAG = L_CTL + 12, C_LANDED = L_CTL + 16, C_KHELP = L_CTL + 20, C_HRDY = L_CTL + 24, C_HOP1 = L_CTL + 28,
              C_DONE = L_CTL + 32, C_BASE = L_CTL + 40;
constexpr int L_ML = 1088;
constexpr int L_SC = 1152;
constexpr int L_H = 1184;
constexpr int L_YA = 1216;
constexpr int L_Q = 1792;
constexpr int L_XN = 2816;
constexpr int L_ACC = 4864;
constexpr int L_P = L_ACC + 4 * 2048;
#define LDS_KB 44
constexpr int LDS_BYTES = LDS_KB * 1024;

constexpr int AUX_NT = 2, AUX_SC1 = 16;
#define AUX_K 2
#define AUX_V 2
#define SB() __builtin_amdgcn_sched_barrier(0)
#define FENCE() asm volatile("" ::: "memory")

__device__ __forceinline__ float bf_lo(unsigned u) { return __builtin_bit_cast(float, u << 16); }
__device__ __forceinline__ float bf_hi(unsigned u) { return __builtin_bit_cast(float, u & 0xffff0000u); }
__device__ __forceinline__ unsigned pk_bf16(float lo, float hi) {
  bf16x2 v = {(__bf16)lo, (__bf16)hi};
  return __builtin_bit_cast(unsigned, v);
}
__device__ __forceinline__ float rnd_bf16(float x) { return (float)(__bf16)x; }
__device__ __forceinline__ float dot2(unsigned a, unsigned b, float acc) {
  return __builtin_amdgcn_fdot2_f32_bf16(__builtin_bit_cast(bf16x2, a), __builtin_bit_cast(bf16x2, b), acc, false);
}
template <int CTRL> __device__ __forceinline__ float dpp(float x) {
  return __builtin_bit_cast(float, __builtin_amdgcn_mov_dpp(__builtin_bit_cast(int, x), CTRL, 0xf, 0xf, true));
}
constexpr int XOR1 = 0xB1, XOR2 = 0x4E, XOR7 = 0x141, XOR15 = 0x140, XOR8 = 0x128, ROR4 = 0x124;
__device__ __forceinline__ float x16_max(float x) {
  auto s = __builtin_amdgcn_permlane16_swap(__float_as_uint(x), __float_as_uint(x), false, false);
  return fmaxf(__uint_as_float(s[0]), __uint_as_float(s[1]));
}
__device__ __forceinline__ float x32_max(float x) {
  auto s = __builtin_amdgcn_permlane32_swap(__float_as_uint(x), __float_as_uint(x), false, false);
  return fmaxf(__uint_as_float(s[0]), __uint_as_float(s[1]));
}
__device__ __forceinline__ float x16_sum(float x) {
  auto s = __builtin_amdgcn_permlane16_swap(__float_as_uint(x), __float_as_uint(x), false, false);
  return __uint_as_float(s[0]) + __uint_as_float(s[1]);
}
__device__ __forceinline__ float x32_sum(float x) {
  auto s = __builtin_amdgcn_permlane32_swap(__float_as_uint(x), __float_as_uint(x), false, false);
  return __uint_as_float(s[0]) + __uint_as_float(s[1]);
}
__device__ __forceinline__ float wave_sum(float x) {
  x += dpp<XOR1>(x); x += dpp<XOR2>(x); x += dpp<XOR7>(x); x += dpp<XOR15>(x);
  return x32_sum(x16_sum(x));
}
__device__ __forceinline__ float parity_sum(float x) {
  x += dpp<XOR2>(x); x += dpp<ROR4>(x); x += dpp<XOR8>(x);
  return x32_sum(x16_sum(x));
}
__device__ __forceinline__ float parity_max(float x) {
  x = fmaxf(x, dpp<XOR2>(x)); x = fmaxf(x, dpp<ROR4>(x)); x = fmaxf(x, dpp<XOR8>(x));
  return x32_max(x16_max(x));
}
__device__ __forceinline__ void lds_barrier() {
  asm volatile("s_waitcnt lgkmcnt(0)" ::: "memory");
  __builtin_amdgcn_s_barrier();
  FENCE();
}
__device__ __forceinline__ rsrc_t mk_rsrc(const void* p, unsigned bytes) {
  return __builtin_amdgcn_make_buffer_rsrc(const_cast<void*>(p), 0, bytes, 0x00020000);
}
__device__ __forceinline__ unsigned lds_ld(unsigned char* lds, int off) {
  return __builtin_amdgcn_readfirstlane(__hip_atomic_load(reinterpret_cast<unsigned*>(lds + off), __ATOMIC_RELAXED, __HIP_MEMORY_SCOPE_WORKGROUP));
}
template <int SLEEP = 1>
__device__ __forceinline__ void lds_wait_ge(unsigned char* lds, int off, unsigned want) {
#pragma nounroll
  for (unsigned spin = 0; spin < SPIN_CAP * 8u; ++spin) {
    if (lds_ld(lds, off) >= want) break;
    __builtin_amdgcn_s_sleep(SLEEP);
  }
  FENCE();
}
__device__ __forceinline__ void lds_arrive(unsigned char* lds, int off, int lane) {
  asm volatile("s_waitcnt lgkmcnt(0)" ::: "memory");
  if (lane == 0) __hip_atomic_fetch_add(reinterpret_cast<unsigned*>(lds + off), 1u, __ATOMIC_RELAXED, __HIP_MEMORY_SCOPE_WORKGROUP);
}
__device__ __forceinline__ void lds_add(unsigned char* lds, int off, unsigned v, int lane) {
  if (lane == 0) __hip_atomic_fetch_add(reinterpret_cast<unsigned*>(lds + off), v, __ATOMIC_RELAXED, __HIP_MEMORY_SCOPE_WORKGROUP);
}
__device__ __forceinline__ unsigned now() { return (unsigned)__builtin_amdgcn_s_memrealtime(); }
template <int SLEEP>
__device__ __forceinline__ void sleep_until(unsigned t0, unsigned ticks) {
#pragma nounroll
  for (unsigned spin = 0; spin < SPIN_CAP && now() - t0 < ticks; ++spin) __builtin_amdgcn_s_sleep(SLEEP);
}
__device__ __forceinline__ void chain_wait(unsigned char* lds, int idx) {
#pragma nounroll
  for (unsigned spin = 0; spin < SPIN_CAP * 8u; ++spin) {
    if ((int)lds_ld(lds, C_LANDED) >= idx + 1 - CH_W || lds_ld(lds, C_QRDY) != 0u) break;
    __builtin_amdgcn_s_sleep(1);
  }
  FENCE();
}
template <int N, int SLEEP>
__device__ __forceinline__ bool gather(u32x4 (&v)[N], rsrc_t rs, const unsigned (&voff)[N], unsigned soff, unsigned tag) {
  bool have[N];
#pragma unroll
  for (int k = 0; k < N; ++k) { have[k] = voff[k] == OOB; v[k] = u32x4{0u, 0u, 0u, 0u}; }
#pragma nounroll
  for (unsigned spin = 0; spin < SPIN_CAP; ++spin) {
    FENCE();
    u32x4 t[N];
#pragma unroll
    for (int k = 0; k < N; ++k) t[k] = __builtin_amdgcn_raw_buffer_load_b128(rs, have[k] ? OOB : voff[k], soff, AUX_LD);
    bool all = true;
#pragma unroll
    for (int k = 0; k < N; ++k) {
      const bool now = !have[k] && t[k][1] == tag && t[k][3] == tag;
      v[k][0] = now ? t[k][0] : v[k][0]; v[k][2] = now ? t[k][2] : v[k][2];
      have[k] = have[k] || now; all = all && have[k];
    }
    if (__builtin_amdgcn_ballot_w64(!all) == 0) return true;
    if (SLEEP > 0) __builtin_amdgcn_s_sleep(SLEEP);
  }
  return false;
}
#define STAMP(slot) do {} while (0)
#define PROG() do {} while (0)
#define PROG_OUT() do {} while (0)
#define PROGL() do {} while (0)

struct Args {
  const unsigned short* x;
  const unsigned char* k;
  const unsigned char* v;
  const unsigned char* wq;
  const unsigned char* wo;
  const unsigned char* wg;
  const unsigned char* wu;
  const unsigned char* wd;
  unsigned short* out;
  unsigned char* ws;
};

__host__ __device__ constexpr int piece(int e, int n) { return n < 2 ? 2 * e + n : 2 * n + e; }
__device__ __forceinline__ int piece_m(int m, int e, int n) { return m == 1 ? 4 * e + n : (m == 0 && n < 2) ? 2 * e + n : 2 * n + e; }
template <int AUX>
__device__ __forceinline__ void issue(u32x4 (&r)[8], rsrc_t rs, unsigned v0, unsigned v1, unsigned soff) {
#pragma unroll
  for (int i = 0; i < 8; ++i) { r[i] = __builtin_amdgcn_raw_buffer_load_b128(rs, (i & 1) ? v1 : v0, soff + OFF_J(i >> 1), AUX); SB(); }
}
__device__ __forceinline__ f32x4 mfma(u32x4 x, u32x4 y, f32x4 acc) {
  return __builtin_amdgcn_mfma_f32_16x16x32_bf16(__builtin_bit_cast(bf16x8, x), __builtin_bit_cast(bf16x8, y), acc, 0, 0, 0);
}
__device__ __forceinline__ void qk_mfma(const u32x4 (&r)[8], const u32x4 (&qf)[2], f32x4 (&d)[4]) {
#pragma unroll
  for (int j = 0; j < 4; ++j) d[j] = mfma(qf[1], r[2 * j + 1], mfma(qf[0], r[2 * j], f32x4{0.f, 0.f, 0.f, 0.f}));
  SB();
}
__device__ __forceinline__ unsigned short qk_score(const f32x4 (&d)[4], bool b0, bool b1) {
  float v[4];
#pragma unroll
  for (int j = 0; j < 4; ++j) {
    float e0 = d[j][0], e1 = d[j][1], e2 = d[j][2], e3 = d[j][3];
    asm("" : "+v"(e0), "+v"(e2));
    const float lo = b0 ? e1 : e0, hi = b0 ? e3 : e2;
    v[j] = b1 ? hi : lo;
  }
  const float u0 = (b0 ? v[1] : v[0]) + dpp<XOR1>(b0 ? v[0] : v[1]);
  const float u1 = (b0 ? v[3] : v[2]) + dpp<XOR1>(b0 ? v[2] : v[3]);
  const float w = (b1 ? u1 : u0) + dpp<XOR2>(b1 ? u0 : u1);
  return (unsigned short)(pk_bf16(w, w) & 0xffffu);
}
__device__ __forceinline__ void pv_mfma(const u32x4 (&vr)[8], u32x2 pk, int msel, f32x4& acc0, f32x4& acc1) {
#pragma unroll
  for (int q = 0; q < 4; ++q) {
    const u32x4 pa = {msel == 2 * q ? pk[0] : 0u, msel == 2 * q ? pk[1] : 0u, msel == 2 * q + 1 ? pk[0] : 0u, msel == 2 * q + 1 ? pk[1] : 0u};
    const unsigned sel = (q & 1) ? 0x07060302u : 0x05040100u;
    const int d0 = q >> 1, d1 = 2 + (q >> 1);
    const u32x4 b0 = {__builtin_amdgcn_perm(vr[2][d0], vr[0][d0], sel), __builtin_amdgcn_perm(vr[6][d0], vr[4][d0], sel),
                      __builtin_amdgcn_perm(vr[3][d0], vr[1][d0], sel), __builtin_amdgcn_perm(vr[7][d0], vr[5][d0], sel)};
    const u32x4 b1 = {__builtin_amdgcn_perm(vr[2][d1], vr[0][d1], sel), __builtin_amdgcn_perm(vr[6][d1], vr[4][d1], sel),
                      __builtin_amdgcn_perm(vr[3][d1], vr[1][d1], sel), __builtin_amdgcn_perm(vr[7][d1], vr[5][d1], sel)};
    acc0 = mfma(pa, b0, acc0);
    acc1 = mfma(pa, b1, acc1);
  }
  SB();
}
__device__ __forceinline__ void fma8(float (&s)[8], float xv, u32x4 w) {
#pragma unroll
  for (int i = 0; i < 4; ++i) { s[2 * i] = fmaf(xv, bf_lo(w[i]), s[2 * i]); s[2 * i + 1] = fmaf(xv, bf_hi(w[i]), s[2 * i + 1]); }
}
__device__ __forceinline__ void load_qf(u32x4 (&qf)[2], const unsigned char* lds, int m, int c, int g) {
#pragma unroll
  for (int e = 0; e < 2; ++e) qf[e] = *reinterpret_cast<const u32x4*>(lds + L_Q + ((c >> 2) & 1) * 512 + (piece_m(m, e, g) * 4 + (c & 3)) * 16);
}

__global__ void __launch_bounds__(NTHR, 2) fused_decode(Args a) {
  extern __shared__ __attribute__((aligned(16))) unsigned char lds[];
  const int lane0 = threadIdx.x & 63, wave = __builtin_amdgcn_readfirstlane(threadIdx.x >> 6);
  const int wg = blockIdx.x, h = wg >> 5, s = wg & 31;
  unsigned long long pc;
  asm volatile("s_getpc_b64 %0" : "=s"(pc));
  const unsigned cpf = __builtin_amdgcn_raw_buffer_load_b32(mk_rsrc(reinterpret_cast<const void*>(pc), wg < CODE_WGS ? CODE_PF : 0), threadIdx.x * 128, 0, 0);
  SB();
  const bool helper = wave < 4;
  const unsigned wg_off = (unsigned)h * (SK * DH * 2) + OFF_S(wg);
  const unsigned vw0 = OFF_G(lane0 >> 2) + (lane0 >> 4) * 64 + (lane0 & 3) * 16, vw1 = vw0 + 256;
  const rsrc_t r_k = mk_rsrc(a.k, 8u * SK * DH * 2);
  u32x4 ra[8], rb[8];
#pragma unroll
  for (int i = 0; i < 8; ++i) asm volatile("" : "=v"(ra[i]), "=v"(rb[i]));
  if (!helper) {
    const unsigned t0 = wg_off + OFF_W(wave);
    issue<AUX_K>(ra, r_k, vw0, vw1, t0);
    issue<AUX_K>(rb, r_k, vw0, vw1, t0 + SLOT1);
  }
  const rsrc_t r_ws = mk_rsrc(a.ws, WS_END);
#define LANE_VARS int tid = threadIdx.x; asm volatile("" : "+v"(tid)); const int lane = tid & 63, c = lane & 15, g = lane >> 4; (void)c; (void)g
#define SCORE_VARS const bool b0 = (c & 1) != 0, b1 = (c & 2) != 0; const int sc_lane = (g & 1) * 32 + c * 2
#define STAMP0() do {} while (0)
  bool timed_out = false;
  unsigned tag;
  unsigned draw[8], wraw[16];
#pragma unroll
  for (int j = 0; j < 8; ++j) asm volatile("" : "=v"(draw[j]), "=v"(wraw[j]), "=v"(wraw[8 + j]));

  if (__builtin_expect(helper, 1)) {
    __builtin_amdgcn_s_setprio(3);
    LANE_VARS;
    const bool qwg = (s & 8) == 0;
    const int qsl = (s & 7) * 2 + (s >> 4);
    const int fb = h * 16 + qsl;
    const int wrow = (lane >> (qwg ? 2 : 3)) + (qwg ? 16 * wave : (fb & 3) * 112 + (wave >> 1) * 56), wstep = qwg ? 64 : 8;
    const u32x4 xv = __builtin_amdgcn_raw_buffer_load_b128(mk_rsrc(a.x, D * 2), lane * 16, 0, 0);
    const unsigned epoch = __builtin_amdgcn_raw_buffer_load_b32(r_ws, 0, WS_EPOCH, AUX_SC1);
    u32x4 wv[7];
    {
      const unsigned rowb = qwg ? NQ * 2 : FF * 2;
      const rsrc_t r0 = mk_rsrc(qwg ? a.wq : (wave & 1) ? a.wu : a.wg, (qwg || fb < 112) ? D * rowb : 0u);
      const unsigned v0 = wrow * rowb + (lane & (qwg ? 3 : 7)) * 16, s0 = qwg ? (h * 512 + qsl * 32) * 2 : (fb >> 2) * 128;
#pragma unroll
      for (int i = 0; i < 7; ++i) wv[i] = __builtin_amdgcn_raw_buffer_load_b128(r0, v0, s0 + i * wstep * rowb, AUX_NT);
    }
    const unsigned t_start = now();
    unsigned t_ref = 0, n_ref = 0;
    SB();
    STAMP0();
    SCORE_VARS;
    if (tid < 16) *reinterpret_cast<unsigned*>(lds + L_CTL + tid * 4) = 0u;
    lds_barrier();

    {
      float xs[8];
#pragma unroll
      for (int i = 0; i < 4; ++i) { xs[2 * i] = bf_lo(xv[i]); xs[2 * i + 1] = bf_hi(xv[i]); }
      float sm = 0.f;
#pragma unroll
      for (int i = 0; i < 8; ++i) sm += xs[i];
      const float mean = wave_sum(sm) * (1.0f / D);
      float sq = 0.f;
#pragma unroll
      for (int i = 0; i < 8; ++i) { const float d = xs[i] - mean; sq = fmaf(d, d, sq); }
      sq = lane < D / 8 ? sq : 0.f;
      const float var = wave_sum(sq) * (1.0f / D);
      const float mean_b = rnd_bf16(mean);
      const float den_b = rnd_bf16(__builtin_amdgcn_sqrtf(rnd_bf16(rnd_bf16(var) + 1e-5f)));
      float rcp = __builtin_amdgcn_rcpf(den_b);
      rcp = fmaf(fmaf(-den_b, rcp, 1.0f), rcp, rcp);
#pragma unroll
      for (int i = 0; i < 8; ++i) {
        const float n = rnd_bf16(xs[i] - mean_b), q = n * rcp;
        xs[i] = rnd_bf16(fmaf(fmaf(-den_b, q, n), rcp, q));
      }
      f32x4* xp = reinterpret_cast<f32x4*>(lds + L_XN + lane * 32);
      xp[0] = f32x4{xs[0], xs[1], xs[2], xs[3]}; xp[1] = f32x4{xs[4], xs[5], xs[6], xs[7]};
      FENCE();
    }
    tag = __builtin_amdgcn_readfirstlane(epoch) + 1u;
    asm volatile("" :: "v"(cpf));
    {
      float sacc[8] = {0.f, 0.f, 0.f, 0.f, 0.f, 0.f, 0.f, 0.f};
#pragma unroll
      for (int i = 0; i < 7; ++i) fma8(sacc, *reinterpret_cast<const float*>(lds + L_XN + (wrow + wstep * i) * 4), wv[i]);
#pragma unroll
      for (int i = 0; i < 8; ++i) { float t = sacc[i]; const float r = dpp<ROR4>(t); t += qwg ? r : 0.f; t += dpp<XOR8>(t); sacc[i] = x32_sum(x16_sum(t)); }
      if (lane < 8) {
        f32x4* p = reinterpret_cast<f32x4*>(lds + L_RED + wave * 256 + lane * 32);
        p[0] = f32x4{sacc[0], sacc[1], sacc[2], sacc[3]}; p[1] = f32x4{sacc[4], sacc[5], sacc[6], sacc[7]};
      }
      lds_arrive(lds, C_PART, lane);
      if (wave == 0) {
        lds_wait_ge(lds, C_PART, 4u);
        if (qwg) {
          float q0 = 0.f, q1 = 0.f;
#pragma unroll
          for (int w = 0; w < 4; ++w) { const f32x2 t = *reinterpret_cast<const f32x2*>(lds + L_RED + w * 256 + c * 8); q0 += t[0]; q1 += t[1]; }
          __builtin_amdgcn_raw_buffer_store_b64(u32x2{pk_bf16(q0, q1), tag}, r_ws, lane < 16 ? lane * 8 : OOB, WS_QG + (h * 16 + qsl) * LSTR, AUX_SC1);
        } else {
          const int o = ((lane >> 2) & 1) * 256 + (lane >> 3) * 32 + (lane & 3) * 8;
          const f32x2 t0 = *reinterpret_cast<const f32x2*>(lds + L_RED + o), t1 = *reinterpret_cast<const f32x2*>(lds + L_RED + 512 + o);
          __builtin_amdgcn_raw_buffer_store_b128(u32x4{__float_as_uint(t0[0] + t1[0]), tag, __float_as_uint(t0[1] + t1[1]), tag}, r_ws,
                                                 fb < 112 ? (lane >> 3) * 512 + (lane & 7) * 16 : OOB, WS_GU + (fb >> 2) * 4096 + (fb & 3) * 128, AUX_SC1);
        }
        STAMP(1);
      }
    }
    const bool ffn = wg < FF / 8;
    if (wave == 0) {
      u32x4 qg[2];
      const unsigned qo[2] = {(lane >> 2) * LSTR + (lane & 3) * 32, (lane >> 2) * LSTR + (lane & 3) * 32 + 16};
      sleep_until<1>(t_start, Q_FIRST);
      timed_out = !gather<2, Q_SLEEP>(qg, r_ws, qo, WS_QG + h * 16 * LSTR, tag);
      *reinterpret_cast<u32x4*>(lds + L_Q + lane * 16) = u32x4{qg[0][0], qg[0][2], qg[1][0], qg[1][2]};
      if (lane == 0) *reinterpret_cast<unsigned*>(lds + C_TAG) = tag;
      lds_arrive(lds, C_QRDY, lane);
    }
    if (wave != 0) {
      __builtin_amdgcn_s_setprio(HK_PRIO);
      const int slot = NI + wave - 1;
      u32x4 kt[4][8];
#pragma unroll
      for (int i = 0; i < 4; ++i) { chain_wait(lds, 4 * slot + i); issue<AUX_K>(kt[i], r_k, vw0, vw1, wg_off + OFF_SLOT(slot) + OFF_W(i)); }
      asm volatile("s_waitcnt vmcnt(24)" ::: "memory"); lds_arrive(lds, C_LANDED, lane); PROGL();
      asm volatile("s_waitcnt vmcnt(16)" ::: "memory"); lds_arrive(lds, C_LANDED, lane); PROGL();
      asm volatile("s_waitcnt vmcnt(8)" ::: "memory"); lds_arrive(lds, C_LANDED, lane); PROGL();
      asm volatile("s_waitcnt vmcnt(0)" ::: "memory"); lds_arrive(lds, C_LANDED, lane); PROGL();
      lds_wait_ge(lds, C_QRDY, 1u);
      u32x4 qf[2];
      load_qf(qf, lds, 1, c, g);
#pragma unroll
      for (int i = 0; i < 4; ++i) {
        f32x4 d[4];
        qk_mfma(kt[i], qf, d);
        *reinterpret_cast<unsigned short*>(lds + L_P + i * 2048 + slot * 64 + sc_lane) = qk_score(d, b0, b1);
        PROG();
      }
      lds_arrive(lds, C_KHELP, lane);
      lds_add(lds, C_DONE, 4u, lane);
      __builtin_amdgcn_s_setprio(3);
    }
    STAMP(3);
    {
      if (wave == 3) {
        sleep_until<32>(t_start, GU_AT);
        u32x4 gu[1];
        const unsigned go[1] = {(ffn && lane < 32) ? (unsigned)lane * 16 : OOB};
        if (!gather<1, 64>(gu, r_ws, go, WS_GU + wg * 512, tag)) timed_out = true;
        float g0 = __uint_as_float(gu[0][0]), g1 = __uint_as_float(gu[0][2]);
        g0 += dpp<XOR8>(g0); g1 += dpp<XOR8>(g1);
        g0 = rnd_bf16(x16_sum(g0)); g1 = rnd_bf16(x16_sum(g1));
        const float u0 = dpp<ROR4>(g0), u1 = dpp<ROR4>(g1);
        const float h0 = rnd_bf16((g0 > 0.f ? g0 : g0 * 0.f) * u0), h1 = rnd_bf16((g1 > 0.f ? g1 : g1 * 0.f) * u1);
        if (lane < 4) *reinterpret_cast<f32x2*>(lds + L_H + lane * 8) = f32x2{h0, h1};
      }
      if (wave == 1) { lds_wait_ge<2>(lds, C_DONE, CP_REF); t_ref = now(); n_ref = lds_ld(lds, C_DONE) & 0xffffu; }
    }
    STAMP(11);
    if (wave == 1) {
#pragma nounroll
      for (unsigned spin = 0; spin < SPIN_CAP * 8u && (lds_ld(lds, C_DONE) & 0xffffu) < CP_AT; ++spin) __builtin_amdgcn_s_sleep(2);
      const unsigned t_at = now(), n_cp = lds_ld(lds, C_DONE) & 0xffffu;
      const float tau = (float)(t_at - t_ref) / (float)(n_cp - n_ref);
      const unsigned t_cp = t_at - (unsigned)(tau * (float)(n_cp - CP_AT));
      lds_wait_ge<4>(lds, C_DONE, 0x10000u * 4u);
      const unsigned t_now = now();
      const int hd = lane & 1;
      float mw[4], lw[4];
#pragma unroll
      for (int w = 0; w < 4; ++w) { const f32x2 t = *reinterpret_cast<const f32x2*>(lds + L_ML + w * 16 + hd * 8); mw[w] = t[0]; lw[w] = t[1]; }
      const float M = fmaxf(fmaxf(mw[0], mw[1]), fmaxf(mw[2], mw[3]));
      float L = 0.f;
#pragma unroll
      for (int w = 0; w < 4; ++w) L = fmaf(__builtin_amdgcn_exp2f((mw[w] - M) * C2), lw[w], L);
      __builtin_amdgcn_raw_buffer_store_b128(u32x4{(lane & 2) ? t_cp : __float_as_uint(M), tag, __float_as_uint((lane & 2) ? tau : L), tag}, r_ws,
                                             lane < 8 ? lane * 16 : OOB, WS_ML + wg * LSTR, AUX_SC1);
      sleep_until<16>(t_now, CP_WAIT);
      u32x4 ml[2];
      const unsigned mo[2] = {(lane >> 2) * LSTR + (lane & 3) * 16, (16 + (lane >> 2)) * LSTR + (lane & 3) * 16};
      if (!gather<2, 32>(ml, r_ws, mo, WS_ML + h * 32 * LSTR, tag)) timed_out = true;
      const float Ma = __uint_as_float(ml[0][0]), Mb = __uint_as_float(ml[1][0]);
      float Mg = fmaxf(Ma, Mb);
      Mg = fmaxf(Mg, dpp<ROR4>(Mg)); Mg = fmaxf(Mg, dpp<XOR8>(Mg)); Mg = x32_max(x16_max(Mg));
      float Lg = __builtin_amdgcn_exp2f((Ma - Mg) * C2) * __uint_as_float(ml[0][2]) + __builtin_amdgcn_exp2f((Mb - Mg) * C2) * __uint_as_float(ml[1][2]);
      if (lane & 2) Lg = (lane & 1) ? __uint_as_float(ml[0][2]) + __uint_as_float(ml[1][2]) : (float)(int)(ml[0][0] - t_cp) + (float)(int)(ml[1][0] - t_cp);
      Lg += dpp<ROR4>(Lg); Lg += dpp<XOR8>(Lg); Lg = x32_sum(x16_sum(Lg));
      if (lane < 2) {
#pragma unroll
        for (int w = 0; w < 4; ++w) *reinterpret_cast<float*>(lds + L_SC + w * 8 + lane * 4) = timed_out ? __builtin_nanf("") : __builtin_amdgcn_exp2f((mw[w] - Mg) * C2) / Lg;
      }
      {
        const float tm = dpp<XOR1>(Lg) * (CP_TAU / 32);
        if (lane == 2 && !timed_out)
          *reinterpret_cast<u32x2*>(lds + C_BASE) = u32x2{t_cp + (unsigned)(int)(Lg * (1.0f / 32) + (float)CP_OFF - tm * (float)CP_AT), (unsigned)(tm * 256.f)};
      }
      STAMP(14);
    }
    {
      lds_wait_ge<4>(lds, C_DONE, 0x10000u * 4u + ROWS_AT);
      const rsrc_t r_wo = mk_rsrc(a.wo, NQ * D * 2), r_d = mk_rsrc(a.wd, ffn ? FF * D * 2 : 0);
      const unsigned coff = tid < D / 2 ? tid * 4 : OOB;
#pragma unroll
      for (int j = 0; j < 8; ++j) draw[j] = __builtin_amdgcn_raw_buffer_load_b32(r_d, coff, (wg * 8 + j) * (D * 2), AUX_NT);
#pragma unroll
      for (int j = 0; j < 16; ++j) wraw[j] = __builtin_amdgcn_raw_buffer_load_b32(r_wo, coff, (wg * 16 + j) * (D * 2), AUX_NT);
      SB();
    }
  } else {
    const int w = wave - 4;
    const unsigned t0 = wg_off + OFF_W(wave);
    LANE_VARS;
    SCORE_VARS;
    STAMP0();
    const rsrc_t r_v = mk_rsrc(a.v, 8u * SK * DH * 2);
    const unsigned vv0 = OFF_G(g) + piece(0, c >> 2) * 64 + (c & 3) * 16, vv1 = OFF_G(g) + piece(1, c >> 2) * 64 + (c & 3) * 16;
    constexpr int km = 0;
    const unsigned vk0 = OFF_G(c >> 2) + piece_m(km, 0, g) * 64 + (c & 3) * 16, vk1 = OFF_G(c >> 2) + piece_m(km, 1, g) * 64 + (c & 3) * 16;
    u32x4 rx[NI - 2][8];
    static_assert(CH_W >= 5, "");
    lds_barrier();
    asm volatile("s_waitcnt vmcnt(8)" ::: "memory"); lds_arrive(lds, C_LANDED, lane); PROGL();
#define CHAIN_STEP(R, SLOT) do { \
      constexpr bool first_ = CH_W < 5;     \
      if (first_ && lds_ld(lds, C_QRDY) == 0u) { asm volatile("s_waitcnt vmcnt(0)" ::: "memory"); lds_arrive(lds, C_LANDED, lane); PROGL(); } \
      chain_wait(lds, 4 * (SLOT) + w); \
      issue<AUX_K>(R, r_k, vw0, vw1, t0 + OFF_SLOT(SLOT)); \
      if (!first_ && lds_ld(lds, C_QRDY) == 0u) { asm volatile("s_waitcnt vmcnt(8)" ::: "memory"); lds_arrive(lds, C_LANDED, lane); PROGL(); } } while (0)
#pragma unroll
    for (int i = 2; i < NI; ++i) CHAIN_STEP(rx[i - 2], i);
#undef CHAIN_STEP
    if (lds_ld(lds, C_QRDY) == 0u) { asm volatile("s_waitcnt vmcnt(0)" ::: "memory"); lds_arrive(lds, C_LANDED, lane); PROGL(); }
    lds_wait_ge(lds, C_QRDY, 1u);
    tag = lds_ld(lds, C_TAG);
    STAMP(2);
    u32x4 qf[2];
    load_qf(qf, lds, 1, c, g);
    unsigned char* const pl = lds + L_P + w * 2048;
    {
      f32x4 d[4];
      unsigned short* sw = reinterpret_cast<unsigned short*>(pl + sc_lane);
      unsigned tp = OFF_SLOT(NI + 3);
#define K_STEP(R, ISSUE_NEXT, SLOT) do { qk_mfma(R, qf, d); PROG(); ISSUE_NEXT; sw[(SLOT) * 32] = qk_score(d, b0, b1); } while (0)
      K_STEP(ra, issue<AUX_K>(ra, r_k, vk0, vk1, t0 + tp), 0);
      K_STEP(rb, issue<AUX_K>(rb, r_k, vk0, vk1, t0 + tp + SLOT1), 1);
#pragma unroll
      for (int i = 2; i < NI; ++i) K_STEP(rx[i - 2], (void)0, i);
      lds_add(lds, C_DONE, NI, lane);
      load_qf(qf, lds, km, c, g);
      sw += (NI + 3) * 32;
      for (int it = 1; it < (29 - NI) / 2; ++it) {
        tp = OFF_SLOT(NI + 3 + 2 * it);
        K_STEP(ra, issue<AUX_K>(ra, r_k, vk0, vk1, t0 + tp), 0);
        K_STEP(rb, issue<AUX_K>(rb, r_k, vk0, vk1, t0 + tp + SLOT1), 1);
        lds_add(lds, C_DONE, 2u, lane);
        sw += 2 * 32;
      }
      K_STEP(ra, issue<AUX_V>(ra, r_v, vv0, vv1, t0), 0);
      K_STEP(rb, issue<AUX_V>(rb, r_v, vv0, vv1, t0 + SLOT1), 1);
#undef K_STEP
    }
    STAMP(12);
    lds_wait_ge(lds, C_KHELP, 3u);
    {
      u32x4* pp = reinterpret_cast<u32x4*>(pl + lane * 32);
      u32x4 t[2] = {pp[0], pp[1]};
      float mx = bf_lo(t[0][0]);
#pragma unroll
      for (int j = 0; j < 2; ++j)
#pragma unroll
        for (int i = 0; i < 4; ++i) mx = fmaxf(mx, fmaxf(bf_lo(t[j][i]), bf_hi(t[j][i])));
      mx = parity_max(mx);
      const float mc = mx * C2;
      float lp = 0.f;
#pragma unroll
      for (int j = 0; j < 2; ++j) {
#pragma unroll
        for (int i = 0; i < 4; ++i) {
          const float p0 = __builtin_amdgcn_exp2f(fmaf(bf_lo(t[j][i]), C2, -mc)), p1 = __builtin_amdgcn_exp2f(fmaf(bf_hi(t[j][i]), C2, -mc));
          lp += p0 + p1; t[j][i] = pk_bf16(p0, p1);
        }
        pp[j] = t[j];
      }
      lp = parity_sum(lp);
      if (lane < 2) *reinterpret_cast<f32x2*>(lds + L_ML + w * 16 + lane * 8) = f32x2{mx, lp};
      asm volatile("s_waitcnt lgkmcnt(0)" ::: "memory");
      lds_add(lds, C_DONE, 0x10002u, lane);
    }
    f32x4 acc0 = {0.f, 0.f, 0.f, 0.f}, acc1 = {0.f, 0.f, 0.f, 0.f};
    {
      const int msel = c >> 1;
      const unsigned char* pr = pl + (c & 1) * 32 + g * 8;
      const unsigned vb = OFF_G(g) + (c & 3) * 16;
      float* const rec = reinterpret_cast<float*>(lds + L_ACC + w * 2048) + 8 * (c & 3) + g;
      auto put = [&](int m) {
        const int e0 = piece_m(m, 0, c >> 2) * 32, e1 = piece_m(m, 1, c >> 2) * 32;
#pragma unroll
        for (int k = 0; k < 4; ++k) { float* r = rec + (k & 1) * 256 + ((k >> 1) ? e1 : e0); r[0] = acc0[k]; r[4] = acc1[k]; }
      };
      auto get = [&](int m) {
        const int e0 = piece_m(m, 0, c >> 2) * 32, e1 = piece_m(m, 1, c >> 2) * 32;
#pragma unroll
        for (int k = 0; k < 4; ++k) { const float* r = rec + (k & 1) * 256 + ((k >> 1) ? e1 : e0); acc0[k] = r[0]; acc1[k] = r[4]; }
      };
      int cur = 0;
#define V_STEP(R, ISSUE_NEXT) do { const u32x2 pk_ = *reinterpret_cast<const u32x2*>(pr); pr += 64; pv_mfma(R, pk_, msel, acc0, acc1); PROG(); ISSUE_NEXT; } while (0)
      for (int it = 0; it < 15; ++it) {
        u32x4 cw;
#pragma nounroll
        for (unsigned spin = 0; spin < SPIN_CAP; ++spin) {
          FENCE();
          cw = *reinterpret_cast<const u32x4*>(lds + C_DONE);
          if ((__builtin_amdgcn_readfirstlane(cw[0]) & 0xffffu) + SLACK >= (unsigned)(128 + 8 * it)) break;
          __builtin_amdgcn_s_sleep(2);
        }
        const unsigned done = __builtin_amdgcn_readfirstlane(cw[0]) & 0xffffu, tau = __builtin_amdgcn_readfirstlane(cw[3]);
        const int lag = (int)(now() - __builtin_amdgcn_readfirstlane(cw[2]) - ((done * tau) >> 8));
        const int nxt = tau == 0u ? 0 : lag > w * CP_STEP ? 1 : lag < -CP_AHEAD - w * CP_STEPS ? 2 : 0;
        const unsigned tp = OFF_SLOT(2 + 2 * it);
        const unsigned n0 = vb + piece_m(nxt, 0, c >> 2) * 64, n1 = vb + piece_m(nxt, 1, c >> 2) * 64;
        V_STEP(ra, issue<AUX_V>(ra, r_v, n0, n1, t0 + tp));
        V_STEP(rb, issue<AUX_V>(rb, r_v, n0, n1, t0 + tp + SLOT1));
        lds_add(lds, C_DONE, 2u, lane);
        if (nxt != cur) { put(cur); FENCE(); get(nxt); cur = nxt; }
        FENCE();
      }
      V_STEP(ra, lds_add(lds, C_DONE, 1u, lane));
      V_STEP(rb, (void)0);
#undef V_STEP
      STAMP(3);
      put(cur);
      asm volatile("" :: "v"(cpf));
    }
  }
  LANE_VARS;
  PROG_OUT();
  STAMP(4);
  lds_barrier();
  STAMP(5);
  if (!helper) {
    const int u = tid - 256;
    float o0 = 0.f, o1 = 0.f;
#pragma unroll
    for (int w = 0; w < 4; ++w) {
      const float sc = *reinterpret_cast<const float*>(lds + L_SC + w * 8 + (u >> 7) * 4);
      const f32x2 t = *reinterpret_cast<const f32x2*>(lds + L_ACC + w * 2048 + u * 8);
      o0 = fmaf(sc, t[0], o0); o1 = fmaf(sc, t[1], o1);
    }
    __builtin_amdgcn_raw_buffer_store_b128(u32x4{__float_as_uint(o0), tag, __float_as_uint(o1), tag}, r_ws, po_line(u >> 3, s) + (u & 7) * 16,
                                           WS_PO + h * (32 * 4096), AUX_ST);
  }
  STAMP(6);
  if (helper) {
    unsigned wop0[8], wop1[8];
    float y0 = 0.f, y1 = 0.f;
#pragma unroll
    for (int j = 0; j < 8; ++j) {
      const float hv = *reinterpret_cast<const float*>(lds + L_H + j * 4);
      y0 = fmaf(hv, bf_lo(draw[j]), y0); y1 = fmaf(hv, bf_hi(draw[j]), y1);
      wop0[j] = __builtin_amdgcn_perm(wraw[2 * j + 1], wraw[2 * j], 0x05040100u); wop1[j] = __builtin_amdgcn_perm(wraw[2 * j + 1], wraw[2 * j], 0x07060302u);
    }
    asm volatile("" : "+v"(y0), "+v"(y1), "+v"(wop0[0]), "+v"(wop0[1]), "+v"(wop0[2]), "+v"(wop0[3]), "+v"(wop0[4]), "+v"(wop0[5]), "+v"(wop0[6]), "+v"(wop0[7]),
                      "+v"(wop1[0]), "+v"(wop1[1]), "+v"(wop1[2]), "+v"(wop1[3]), "+v"(wop1[4]), "+v"(wop1[5]), "+v"(wop1[6]), "+v"(wop1[7]));
    u32x4 po[4];
    unsigned oo[4];
#pragma unroll
    for (int k = 0; k < 4; ++k) oo[k] = po_line(s, (lane >> 3) + 8 * k) + (lane & 7) * 16;
    if (!gather<4, 0>(po, r_ws, oo, WS_PO + h * (32 * 4096), tag)) timed_out = true;
    STAMP(7);
    float o0 = (__uint_as_float(po[0][0]) + __uint_as_float(po[1][0])) + (__uint_as_float(po[2][0]) + __uint_as_float(po[3][0]));
    float o1 = (__uint_as_float(po[0][2]) + __uint_as_float(po[1][2])) + (__uint_as_float(po[2][2]) + __uint_as_float(po[3][2]));
    o0 += dpp<XOR8>(o0); o1 += dpp<XOR8>(o1);
    o0 = x32_sum(x16_sum(o0)); o1 = x32_sum(x16_sum(o1));
    const unsigned opk = pk_bf16(o0, o1);
#pragma unroll
    for (int j = 0; j < 8; ++j) { const unsigned oj = __builtin_amdgcn_readlane(opk, j); y0 = dot2(wop0[j], oj, y0); y1 = dot2(wop1[j], oj, y1); }
    if (timed_out) y0 = __builtin_nanf("");
    const unsigned yoff = tid < D / 2 ? (unsigned)((2 * tid / CPR) * (256 * CPR) + ((wg + (2 * tid / CPR) * YA_ROT) & 255) * CPR + (2 * tid % CPR)) * 8 : OOB;
    __builtin_amdgcn_raw_buffer_store_b128(u32x4{__float_as_uint(y0), tag, __float_as_uint(y1), tag}, r_ws, yoff, WS_YA, AUX_ST);
    STAMP(8);
    if (wave == 0) lds_arrive(lds, C_HOP1, lane);
  }
  if (wg >= NRED) return;
  if (wave >= 4) {
    const unsigned t = __builtin_amdgcn_raw_buffer_load_b32(r_ws, lane < CPR * 4 ? ((wave - 4) * CPR * 4 + lane) * 128 : OOB, WS_YA + wg * (256 * CPR * 8), AUX_SC1);
    asm volatile("" :: "v"(t));
  }
  lds_wait_ge(lds, C_HOP1, 1u);
  {
    constexpr int NLD = (256 * CPR / 2 + NTHR - 1) / NTHR;
    const float xo = tid < CPR ? bf_lo(a.x[wg * CPR + tid]) : 0.f;
    u32x4 t[NLD];
    unsigned yo[NLD];
#pragma unroll
    for (int k = 0; k < NLD; ++k) yo[k] = tid + NTHR * k < 256 * CPR / 2 ? (unsigned)(tid + NTHR * k) * 16 : OOB;
    if (!gather<NLD, 0>(t, r_ws, yo, WS_YA + wg * (256 * CPR * 8), tag)) timed_out = true;
    STAMP(9);
    float s0 = 0.f, s1 = 0.f;
#pragma unroll
    for (int k = 0; k < NLD; ++k) { s0 += __uint_as_float(t[k][0]); s1 += __uint_as_float(t[k][2]); }
    if (CPR / 2 <= 8) { s0 += dpp<XOR8>(s0); s1 += dpp<XOR8>(s1); }
    if (CPR / 2 <= 4) { s0 += dpp<ROR4>(s0); s1 += dpp<ROR4>(s1); }
    if (CPR / 2 <= 2) { s0 += dpp<XOR2>(s0); s1 += dpp<XOR2>(s1); }
    if (CPR / 2 <= 1) { s0 += dpp<XOR1>(s0); s1 += dpp<XOR1>(s1); }
    s0 = x32_sum(x16_sum(s0)); s1 = x32_sum(x16_sum(s1));
    if (timed_out) s0 = __builtin_nanf("");
    if (lane < CPR / 2) *reinterpret_cast<f32x2*>(lds + L_YA + wave * 64 + lane * 8) = f32x2{s0, s1};
    lds_barrier();
    if (tid < CPR) {
      float r = xo;
#pragma unroll
      for (int w = 0; w < 8; ++w) r += *reinterpret_cast<const float*>(lds + L_YA + w * 64 + tid * 4);
#define AUX_OUT 17
      __builtin_amdgcn_raw_buffer_store_b16((unsigned short)(pk_bf16(r, r) & 0xffffu), mk_rsrc(a.out, D * 2), (wg * CPR + tid) * 2, 0, AUX_OUT);
    }
    if (wg == 0 && tid == 64) __builtin_amdgcn_raw_buffer_store_b32(tag, r_ws, 0, WS_EPOCH, AUX_SC1);
    STAMP(10);
  }
}

}

extern "C" void kernel_launch(void* const* d_in, const int* in_sizes, int n_in, void* d_out, int out_size, void* d_ws, size_t ws_size,
                              hipStream_t stream) {
  (void)in_sizes; (void)n_in; (void)out_size; (void)ws_size;
  Args a;
  a.x = (const unsigned short*)d_in[0];
  a.k = (const unsigned char*)d_in[1];
  a.v = (const unsigned char*)d_in[2];
  a.wq = (const unsigned char*)d_in[3];
  a.wo = (const unsigned char*)d_in[4];
  a.wg = (const unsigned char*)d_in[6];
  a.wu = (const unsigned char*)d_in[7];
  a.wd = (const unsigned char*)d_in[8];
  a.out = (unsigned short*)d_out;
  a.ws = (unsigned char*)d_ws;
  (void)hipFuncSetAttribute((const void*)fused_decode, hipFuncAttributeMaxDynamicSharedMemorySize, LDS_BYTES);
  hipLaunchKernelGGL(fused_decode, dim3(NWG), dim3(NTHR), LDS_BYTES, stream, a);
}
```

```cpp
#include <hip/hip_runtime.h>
#include <hip/hip_bf16.h>
#include <cstdint>

namespace {

typedef unsigned u32x4 __attribute__((ext_vector_type(4)));
typedef unsigned u32x2 __attribute__((ext_vector_type(2)));
typedef float f32x4 __attribute__((ext_vector_type(4)));
typedef float f32x2 __attribute__((ext_vector_type(2)));
typedef __bf16 bf16x8 __attribute__((ext_vector_type(8)));
typedef __bf16 bf16x2 __attribute__((ext_vector_type(2)));
typedef __amdgpu_buffer_rsrc_t rsrc_t;

constexpr int D = 448, SK = 65536, DH = 256, FF = 1792, NQ = 4096;
constexpr int NWG = 256, NTHR = 512;
#define CH_W 10
#define NI 5
#define HK_PRIO 2
#define CP_REF 64
#define CP_AT 116
#define CP_WAIT 350
#define CP_OFF -30
#define CP_TAU 1.0f
#define CODE_PF 20000
#define CODE_WGS 32
#define S1 0
#define H2_PRE 8
#define H1_DUE -50
#define H2S 1
#define ROWS_AT 252
#define PREWARM 4
#define PREWARM_AT 240
#define AUX_ST 16
#define AUX_LD 16
#define CP_STEP 60
#define CP_STEPS 30
#define CP_AHEAD 40
#define CH_TIMED 1
#define CH_T0 60
#define CH_TAU 27
#define COMMON_TT 500
#define E_AT 44
#define E_GAP 14
#define E_TAU 6900
#define E_OFF -20
#define KSLACK_FROM 3
#define SLACK 4
#define Q_FIRST 750
#define Q_SLEEP 0
#define GU_AT 1500
#define CPR 8
constexpr int NRED = D / CPR;
#define KEY_MAP {3, 4, 8, 7, 6, 0, 10, 14, 11, 15, 1, 9, 2, 13, 5, 12}
constexpr int KPOS[16] = KEY_MAP;
template <int FIRST, int N> __host__ __device__ constexpr unsigned scatter(unsigned v) {
  unsigned o = 0;
  for (int i = 0; i < N; ++i) o |= ((v >> i) & 1u) << (KPOS[FIRST + i] + 9);
  return o;
}
#define OFF_J(j) scatter<0, 2>(j)
#define OFF_G(G) scatter<2, 2>(G)
#define OFF_W(w) scatter<4, 2>(w)
#define OFF_SLOT(t) scatter<6, 5>(t)
#define OFF_S(s) scatter<11, 5>(s)
constexpr unsigned SLOT1 = OFF_SLOT(1);
constexpr unsigned OOB = 0x40000000u;
constexpr float C2 = 0.0625f * 1.4426950408889634f;
constexpr unsigned SPIN_CAP = 1u << 22;

constexpr unsigned WS_EPOCH = 0;
constexpr unsigned LSTR = 4352;
constexpr unsigned WS_PO = 4096;
constexpr unsigned WS_YA = WS_PO + 8 * 32 * 32 * 16 * 8;
constexpr unsigned WS_GU = WS_YA + 256 * D * 8;
constexpr unsigned WS_QG = WS_GU + 224 * 4 * 128;
constexpr unsigned WS_ML = WS_QG + 128 * LSTR;
constexpr unsigned WS_TT = WS_ML + 256 * LSTR;
constexpr unsigned WS_END = WS_TT + 24 * LSTR;

constexpr int L_RED = 0;
constexpr int L_CTL = 1024;
constexpr int C_PART = L_CTL, C_QRDY = L_CTL + 4, C_TAG = L_CTL + 12, C_LANDED = L_CTL + 16, C_KHELP = L_CTL + 20, C_HRDY = L_CTL + 24, C_HOP1 = L_CTL + 28,
              C_DONE = L_CTL + 32, C_BASE = L_CTL + 40;
constexpr int L_ML = 1088;
constexpr int L_SC = 1152;
constexpr int L_H = 1184;
constexpr int L_YA = 1216;
constexpr int L_Q = 1792;
constexpr int L_XN = 2816;
constexpr int L_ACC = 4864;
constexpr int L_P = L_ACC + 4 * 2048;
#define LDS_KB 44
constexpr int LDS_BYTES = LDS_KB * 1024;

constexpr int AUX_NT = 2, AUX_SC1 = 16;
#define AUX_K 2
#define AUX_V 2
#define SB() __builtin_amdgcn_sched_barrier(0)
#define FENCE() asm volatile("" ::: "memory")

__device__ __forceinline__ float bf_lo(unsigned u) { return __builtin_bit_cast(float, u << 16); }
__device__ __forceinline__ float bf_hi(unsigned u) { return __builtin_bit_cast(float, u & 0xffff0000u); }
__device__ __forceinline__ unsigned pk_bf16(float lo, float hi) {
  bf16x2 v = {(__bf16)lo, (__bf16)hi};
  return __builtin_bit_cast(unsigned, v);
}
__device__ __forceinline__ float rnd_bf16(float x) { return (float)(__bf16)x; }
__device__ __forceinline__ float dot2(unsigned a, unsigned b, float acc) {
  return __builtin_amdgcn_fdot2_f32_bf16(__builtin_bit_cast(bf16x2, a), __builtin_bit_cast(bf16x2, b), acc, false);
}
template <int CTRL> __device__ __forceinline__ float dpp(float x) {
  return __builtin_bit_cast(float, __builtin_amdgcn_mov_dpp(__builtin_bit_cast(int, x), CTRL, 0xf, 0xf, true));
}
constexpr int XOR1 = 0xB1, XOR2 = 0x4E, XOR7 = 0x141, XOR15 = 0x140, XOR8 = 0x128, ROR4 = 0x124;
__device__ __forceinline__ float x16_max(float x) {
  auto s = __builtin_amdgcn_permlane16_swap(__float_as_uint(x), __float_as_uint(x), false, false);
  return fmaxf(__uint_as_float(s[0]), __uint_as_float(s[1]));
}
__device__ __forceinline__ float x32_max(float x) {
  auto s = __builtin_amdgcn_permlane32_swap(__float_as_uint(x), __float_as_uint(x), false, false);
  return fmaxf(__uint_as_float(s[0]), __uint_as_float(s[1]));
}
__device__ __forceinline__ float x16_sum(float x) {
  auto s = __builtin_amdgcn_permlane16_swap(__float_as_uint(x), __float_as_uint(x), false, false);
  return __uint_as_float(s[0]) + __uint_as_float(s[1]);
}
__device__ __forceinline__ float x32_sum(float x) {
  auto s = __builtin_amdgcn_permlane32_swap(__float_as_uint(x), __float_as_uint(x), false, false);
  return __uint_as_float(s[0]) + __uint_as_float(s[1]);
}
__device__ __forceinline__ float wave_sum(float x) {
  x += dpp<XOR1>(x); x += dpp<XOR2>(x); x += dpp<XOR7>(x); x += dpp<XOR15>(x);
  return x32_sum(x16_sum(x));
}
__device__ __forceinline__ float parity_sum(float x) {
  x += dpp<XOR2>(x); x += dpp<ROR4>(x); x += dpp<XOR8>(x);
  return x32_sum(x16_sum(x));
}
__device__ __forceinline__ float parity_max(float x) {
  x = fmaxf(x, dpp<XOR2>(x)); x = fmaxf(x, dpp<ROR4>(x)); x = fmaxf(x, dpp<XOR8>(x));
  return x32_max(x16_max(x));
}
__device__ __forceinline__ void lds_barrier() {
  asm volatile("s_waitcnt lgkmcnt(0)" ::: "memory");
  __builtin_amdgcn_s_barrier();
  FENCE();
}
__device__ __forceinline__ rsrc_t mk_rsrc(const void* p, unsigned bytes) {
  return __builtin_amdgcn_make_buffer_rsrc(const_cast<void*>(p), 0, bytes, 0x00020000);
}
__device__ __forceinline__ unsigned lds_ld(unsigned char* lds, int off) {
  return __builtin_amdgcn_readfirstlane(__hip_atomic_load(reinterpret_cast<unsigned*>(lds + off), __ATOMIC_RELAXED, __HIP_MEMORY_SCOPE_WORKGROUP));
}
template <int SLEEP = 1>
__device__ __forceinline__ void lds_wait_ge(unsigned char* lds, int off, unsigned want) {
#pragma nounroll
  for (unsigned spin = 0; spin < SPIN_CAP * 8u; ++spin) {
    if (lds_ld(lds, off) >= want) break;
    __builtin_amdgcn_s_sleep(SLEEP);
  }
  FENCE();
}
__device__ __forceinline__ void lds_arrive(unsigned char* lds, int off, int lane) {
  asm volatile("s_waitcnt lgkmcnt(0)" ::: "memory");
  if (lane == 0) __hip_atomic_fetch_add(reinterpret_cast<unsigned*>(lds + off), 1u, __ATOMIC_RELAXED, __HIP_MEMORY_SCOPE_WORKGROUP);
}
__device__ __forceinline__ void lds_add(unsigned char* lds, int off, unsigned v, int lane) {
  if (lane == 0) __hip_atomic_fetch_add(reinterpret_cast<unsigned*>(lds + off), v, __ATOMIC_RELAXED, __HIP_MEMORY_SCOPE_WORKGROUP);
}
__device__ __forceinline__ unsigned now() { return (unsigned)__builtin_amdgcn_s_memrealtime(); }
template <int SLEEP>
__device__ __forceinline__ void sleep_until(unsigned t0, unsigned ticks) {
#pragma nounroll
  for (unsigned spin = 0; spin < SPIN_CAP && now() - t0 < ticks; ++spin) __builtin_amdgcn_s_sleep(SLEEP);
}
__device__ __forceinline__ void chain_wait(unsigned char* lds, int idx, unsigned t_start) {
#pragma nounroll
  for (unsigned spin = 0; spin < SPIN_CAP * 8u; ++spin) {
    if (now() - t_start >= (unsigned)(CH_T0 + idx * CH_TAU) || lds_ld(lds, C_QRDY) != 0u) break;
    __builtin_amdgcn_s_sleep(1);
  }
  FENCE();
}
template <int N, int SLEEP>
__device__ __forceinline__ bool gather(u32x4 (&v)[N], rsrc_t rs, const unsigned (&voff)[N], unsigned soff, unsigned tag) {
  bool have[N];
#pragma unroll
  for (int k = 0; k < N; ++k) { have[k] = voff[k] == OOB; v[k] = u32x4{0u, 0u, 0u, 0u}; }
#pragma nounroll
  for (unsigned spin = 0; spin < SPIN_CAP; ++spin) {
    FENCE();
    u32x4 t[N];
#pragma unroll
    for (int k = 0; k < N; ++k) t[k] = __builtin_amdgcn_raw_buffer_load_b128(rs, have[k] ? OOB : voff[k], soff, AUX_LD);
    bool all = true;
#pragma unroll
    for (int k = 0; k < N; ++k) {
      const bool now = !have[k] && t[k][1] == tag && t[k][3] == tag;
      v[k][0] = now ? t[k][0] : v[k][0]; v[k][2] = now ? t[k][2] : v[k][2];
      have[k] = have[k] || now; all = all && have[k];
    }
    if (__builtin_amdgcn_ballot_w64(!all) == 0) return true;
    if (SLEEP > 0) __builtin_amdgcn_s_sleep(SLEEP);
  }
  return false;
}
#define STAMP(slot) do {} while (0)
#define PROG() do {} while (0)
#define PROG_OUT() do {} while (0)
#define PROGL() do {} while (0)

struct Args {
  const unsigned short* x;
  const unsigned char* k;
  const unsigned char* v;
  const unsigned char* wq;
  const unsigned char* wo;
  const unsigned char* wg;
  const unsigned char* wu;
  const unsigned char* wd;
  unsigned short* out;
  unsigned char* ws;
};

__host__ __device__ constexpr int piece(int e, int n) { return n < 2 ? 2 * e + n : 2 * n + e; }
__device__ __forceinline__ int piece_m(int m, int e, int n) { return m == 1 ? 4 * e + n : (m == 0 && n < 2) ? 2 * e + n : 2 * n + e; }
template <int AUX>
__device__ __forceinline__ void issue(u32x4 (&r)[8], rsrc_t rs, unsigned v0, unsigned v1, unsigned soff) {
#pragma unroll
  for (int i = 0; i < 8; ++i) { r[i] = __builtin_amdgcn_raw_buffer_load_b128(rs, (i & 1) ? v1 : v0, soff + OFF_J(i >> 1), AUX); SB(); }
}
__device__ __forceinline__ f32x4 mfma(u32x4 x, u32x4 y, f32x4 acc) {
  return __builtin_amdgcn_mfma_f32_16x16x32_bf16(__builtin_bit_cast(bf16x8, x), __builtin_bit_cast(bf16x8, y), acc, 0, 0, 0);
}
__device__ __forceinline__ void qk_mfma(const u32x4 (&r)[8], const u32x4 (&qf)[2], f32x4 (&d)[4], bool sw) {
#pragma unroll
  for (int j = 0; j < 4; ++j) {
    u32x4 x0, x1;
#pragma unroll
    for (int i = 0; i < 4; ++i) { x0[i] = sw ? r[2 * j + 1][i] : r[2 * j][i]; x1[i] = sw ? r[2 * j][i] : r[2 * j + 1][i]; }
    d[j] = mfma(qf[1], x1, mfma(qf[0], x0, f32x4{0.f, 0.f, 0.f, 0.f}));
  }
  SB();
}
__device__ __forceinline__ unsigned short qk_score(const f32x4 (&d)[4], bool b0, bool b1) {
  float v[4];
#pragma unroll
  for (int j = 0; j < 4; ++j) {
    float e0 = d[j][0], e1 = d[j][1], e2 = d[j][2], e3 = d[j][3];
    asm("" : "+v"(e0), "+v"(e2));
    const float lo = b0 ? e1 : e0, hi = b0 ? e3 : e2;
    v[j] = b1 ? hi : lo;
  }
  const float u0 = (b0 ? v[1] : v[0]) + dpp<XOR1>(b0 ? v[0] : v[1]);
  const float u1 = (b0 ? v[3] : v[2]) + dpp<XOR1>(b0 ? v[2] : v[3]);
  const float w = (b1 ? u1 : u0) + dpp<XOR2>(b1 ? u0 : u1);
  return (unsigned short)(pk_bf16(w, w) & 0xffffu);
}
__device__ __forceinline__ void pv_mfma(const u32x4 (&vr)[8], u32x2 pk, int msel, f32x4& acc0, f32x4& acc1) {
#pragma unroll
  for (int q = 0; q < 4; ++q) {
    const u32x4 pa = {msel == 2 * q ? pk[0] : 0u, msel == 2 * q ? pk[1] : 0u, msel == 2 * q + 1 ? pk[0] : 0u, msel == 2 * q + 1 ? pk[1] : 0u};
    const unsigned sel = (q & 1) ? 0x07060302u : 0x05040100u;
    const int d0 = q >> 1, d1 = 2 + (q >> 1);
    const u32x4 b0 = {__builtin_amdgcn_perm(vr[2][d0], vr[0][d0], sel), __builtin_amdgcn_perm(vr[6][d0], vr[4][d0], sel),
                      __builtin_amdgcn_perm(vr[3][d0], vr[1][d0], sel), __builtin_amdgcn_perm(vr[7][d0], vr[5][d0], sel)};
    const u32x4 b1 = {__builtin_amdgcn_perm(vr[2][d1], vr[0][d1], sel), __builtin_amdgcn_perm(vr[6][d1], vr[4][d1], sel),
                      __builtin_amdgcn_perm(vr[3][d1], vr[1][d1], sel), __builtin_amdgcn_perm(vr[7][d1], vr[5][d1], sel)};
    acc0 = mfma(pa, b0, acc0);
    acc1 = mfma(pa, b1, acc1);
  }
  SB();
}
__device__ __forceinline__ void fma8(float (&s)[8], float xv, u32x4 w) {
#pragma unroll
  for (int i = 0; i < 4; ++i) { s[2 * i] = fmaf(xv, bf_lo(w[i]), s[2 * i]); s[2 * i + 1] = fmaf(xv, bf_hi(w[i]), s[2 * i + 1]); }
}
__device__ __forceinline__ void load_qf(u32x4 (&qf)[2], const unsigned char* lds, int m, int c, int g) {
#pragma unroll
  for (int e = 0; e < 2; ++e) qf[e] = *reinterpret_cast<const u32x4*>(lds + L_Q + ((c >> 2) & 1) * 512 + (piece_m(m, e, g) * 4 + (c & 3)) * 16);
}

__global__ void __launch_bounds__(NTHR, 2) fused_decode(Args a) {
  extern __shared__ __attribute__((aligned(16))) unsigned char lds[];
  const int lane0 = threadIdx.x & 63, wave = __builtin_amdgcn_readfirstlane(threadIdx.x >> 6);
  const int wg = blockIdx.x, h = wg >> 5, s = wg & 31;
  unsigned long long pc;
  asm volatile("s_getpc_b64 %0" : "=s"(pc));
  const unsigned cpf = __builtin_amdgcn_raw_buffer_load_b32(mk_rsrc(reinterpret_cast<const void*>(pc), wg < CODE_WGS ? CODE_PF : 0), threadIdx.x * 128, 0, 0);
  SB();
  const unsigned t_start = now();
  const bool helper = wave < 4;
  const unsigned wg_off = (unsigned)h * (SK * DH * 2) + OFF_S(wg);
  const unsigned vw0 = OFF_G(lane0 >> 2) + (lane0 >> 4) * 64 + (lane0 & 3) * 16, vw1 = vw0 + 256;
  const rsrc_t r_k = mk_rsrc(a.k, 8u * SK * DH * 2);
  u32x4 ra[8], rb[8];
#pragma unroll
  for (int i = 0; i < 8; ++i) asm volatile("" : "=v"(ra[i]), "=v"(rb[i]));
  if (!helper) {
    const unsigned t0 = wg_off + OFF_W(wave);
    issue<AUX_K>(ra, r_k, vw0, vw1, t0);
    issue<AUX_K>(rb, r_k, vw0, vw1, t0 + SLOT1);
  }
  const rsrc_t r_ws = mk_rsrc(a.ws, WS_END);
#define LANE_VARS int tid = threadIdx.x; asm volatile("" : "+v"(tid)); const int lane = tid & 63, c = lane & 15, g = lane >> 4; (void)c; (void)g
#define SCORE_VARS const bool b0 = (c & 1) != 0, b1 = (c & 2) != 0; const int sc_lane = (g & 1) * 32 + c * 2
#define STAMP0() do {} while (0)
  bool timed_out = false;
  unsigned tag;

  if (__builtin_expect(helper, 1)) {
    __builtin_amdgcn_s_setprio(3);
    LANE_VARS;
    const bool qwg = (s & 8) == 0;
    const int qsl = (s & 7) * 2 + (s >> 4);
    const int fb = h * 16 + qsl;
    const int wrow = (lane >> (qwg ? 2 : 3)) + (qwg ? 16 * wave : (fb & 3) * 112 + (wave >> 1) * 56), wstep = qwg ? 64 : 8;
    const u32x4 xv = __builtin_amdgcn_raw_buffer_load_b128(mk_rsrc(a.x, D * 2), lane * 16, 0, 0);
    const unsigned epoch = __builtin_amdgcn_raw_buffer_load_b32(r_ws, 0, WS_EPOCH, AUX_SC1);
    u32x4 wv[7];
    {
      const unsigned rowb = qwg ? NQ * 2 : FF * 2;
      const rsrc_t r0 = mk_rsrc(qwg ? a.wq : (wave & 1) ? a.wu : a.wg, (qwg || fb < 112) ? D * rowb : 0u);
      const unsigned v0 = wrow * rowb + (lane & (qwg ? 3 : 7)) * 16, s0 = qwg ? (h * 512 + qsl * 32) * 2 : (fb >> 2) * 128;
#pragma unroll
      for (int i = 0; i < 7; ++i) wv[i] = __builtin_amdgcn_raw_buffer_load_b128(r0, v0, s0 + i * wstep * rowb, AUX_NT);
    }
    unsigned t_ref = 0, n_ref = 0;
    SB();
    STAMP0();
    SCORE_VARS;
    if (tid < 16) *reinterpret_cast<unsigned*>(lds + L_CTL + tid * 4) = 0u;
    lds_barrier();

    {
      float xs[8];
#pragma unroll
      for (int i = 0; i < 4; ++i) { xs[2 * i] = bf_lo(xv[i]); xs[2 * i + 1] = bf_hi(xv[i]); }
      float sm = 0.f;
#pragma unroll
      for (int i = 0; i < 8; ++i) sm += xs[i];
      const float mean = wave_sum(sm) * (1.0f / D);
      float sq = 0.f;
#pragma unroll
      for (int i = 0; i < 8; ++i) { const float d = xs[i] - mean; sq = fmaf(d, d, sq); }
      sq = lane < D / 8 ? sq : 0.f;
      const float var = wave_sum(sq) * (1.0f / D);
      const float mean_b = rnd_bf16(mean);
      const float den_b = rnd_bf16(__builtin_amdgcn_sqrtf(rnd_bf16(rnd_bf16(var) + 1e-5f)));
      float rcp = __builtin_amdgcn_rcpf(den_b);
      rcp = fmaf(fmaf(-den_b, rcp, 1.0f), rcp, rcp);
#pragma unroll
      for (int i = 0; i < 8; ++i) {
        const float n = rnd_bf16(xs[i] - mean_b), q = n * rcp;
        xs[i] = rnd_bf16(fmaf(fmaf(-den_b, q, n), rcp, q));
      }
      f32x4* xp = reinterpret_cast<f32x4*>(lds + L_XN + lane * 32);
      xp[0] = f32x4{xs[0], xs[1], xs[2], xs[3]}; xp[1] = f32x4{xs[4], xs[5], xs[6], xs[7]};
      FENCE();
    }
    tag = __builtin_amdgcn_readfirstlane(epoch) + 1u;
    asm volatile("" :: "v"(cpf));
    {
      float sacc[8] = {0.f, 0.f, 0.f, 0.f, 0.f, 0.f, 0.f, 0.f};
#pragma unroll
      for (int i = 0; i < 7; ++i) fma8(sacc, *reinterpret_cast<const float*>(lds + L_XN + (wrow + wstep * i) * 4), wv[i]);
#pragma unroll
      for (int i = 0; i < 8; ++i) { float t = sacc[i]; const float r = dpp<ROR4>(t); t += qwg ? r : 0.f; t += dpp<XOR8>(t); sacc[i] = x32_sum(x16_sum(t)); }
      if (lane < 8) {
        f32x4* p = reinterpret_cast<f32x4*>(lds + L_RED + wave * 256 + lane * 32);
        p[0] = f32x4{sacc[0], sacc[1], sacc[2], sacc[3]}; p[1] = f32x4{sacc[4], sacc[5], sacc[6], sacc[7]};
      }
      lds_arrive(lds, C_PART, lane);
      if (wave == 0) {
        lds_wait_ge(lds, C_PART, 4u);
        if (qwg) {
          float q0 = 0.f, q1 = 0.f;
#pragma unroll
          for (int w = 0; w < 4; ++w) { const f32x2 t = *reinterpret_cast<const f32x2*>(lds + L_RED + w * 256 + c * 8); q0 += t[0]; q1 += t[1]; }
          __builtin_amdgcn_raw_buffer_store_b64(u32x2{pk_bf16(q0, q1), tag}, r_ws, lane < 16 ? lane * 8 : OOB, WS_QG + (h * 16 + qsl) * LSTR, AUX_SC1);
        } else {
          const int o = ((lane >> 2) & 1) * 256 + (lane >> 3) * 32 + (lane & 3) * 8;
          const f32x2 t0 = *reinterpret_cast<const f32x2*>(lds + L_RED + o), t1 = *reinterpret_cast<const f32x2*>(lds + L_RED + 512 + o);
          __builtin_amdgcn_raw_buffer_store_b128(u32x4{__float_as_uint(t0[0] + t1[0]), tag, __float_as_uint(t0[1] + t1[1]), tag}, r_ws,
                                                 fb < 112 ? (lane >> 3) * 512 + (lane & 7) * 16 : OOB, WS_GU + (fb >> 2) * 4096 + (fb & 3) * 128, AUX_SC1);
        }
        STAMP(1);
      }
    }
    const bool ffn = wg < FF / 8;
    if (wave == 0) {
      u32x4 qg[2];
      const unsigned qo[2] = {(lane >> 2) * LSTR + (lane & 3) * 32, (lane >> 2) * LSTR + (lane & 3) * 32 + 16};
      sleep_until<1>(t_start, Q_FIRST);
      timed_out = !gather<2, Q_SLEEP>(qg, r_ws, qo, WS_QG + h * 16 * LSTR, tag);
      *reinterpret_cast<u32x4*>(lds + L_Q + lane * 16) = u32x4{qg[0][0], qg[0][2], qg[1][0], qg[1][2]};
      if (lane == 0) *reinterpret_cast<unsigned*>(lds + C_TAG) = tag;
      lds_arrive(lds, C_QRDY, lane);
    }
    if (wave != 0) {
      __builtin_amdgcn_s_setprio(HK_PRIO);
      const int slot = NI + wave - 1;
      u32x4 kt[4][8];
#pragma unroll
      for (int i = 0; i < 4; ++i) { chain_wait(lds, 4 * slot + i, t_start); issue<AUX_K>(kt[i], r_k, vw0, vw1, wg_off + OFF_SLOT(slot) + OFF_W(i)); }
      lds_wait_ge(lds, C_QRDY, 1u);
      u32x4 qf[2];
      load_qf(qf, lds, 1, c, g);
#pragma unroll
      for (int i = 0; i < 4; ++i) {
        f32x4 d[4];
        qk_mfma(kt[i], qf, d, false);
        *reinterpret_cast<unsigned short*>(lds + L_P + i * 2048 + slot * 64 + sc_lane) = qk_score(d, b0, b1);
        PROG();
      }
      lds_arrive(lds, C_KHELP, lane);
      lds_add(lds, C_DONE, 4u, lane);
      __builtin_amdgcn_s_setprio(3);
    }
    STAMP(3);
    {
      if (wave == 3) {
        sleep_until<32>(t_start, GU_AT);
        u32x4 gu[1];
        const unsigned go[1] = {(ffn && lane < 32) ? (unsigned)lane * 16 : OOB};
        if (!gather<1, 64>(gu, r_ws, go, WS_GU + wg * 512, tag)) timed_out = true;
        float g0 = __uint_as_float(gu[0][0]), g1 = __uint_as_float(gu[0][2]);
        g0 += dpp<XOR8>(g0); g1 += dpp<XOR8>(g1);
        g0 = rnd_bf16(x16_sum(g0)); g1 = rnd_bf16(x16_sum(g1));
        const float u0 = dpp<ROR4>(g0), u1 = dpp<ROR4>(g1);
        const float h0 = rnd_bf16((g0 > 0.f ? g0 : g0 * 0.f) * u0), h1 = rnd_bf16((g1 > 0.f ? g1 : g1 * 0.f) * u1);
        if (lane < 4) *reinterpret_cast<f32x2*>(lds + L_H + lane * 8) = f32x2{timed_out ? __builtin_nanf("") : h0, h1};
      }
      if (wave == 2) {
        lds_wait_ge<2>(lds, C_DONE, E_AT);
        const unsigned tb = now() - (((lds_ld(lds, C_DONE) & 0xffffu) * E_TAU) >> 8);
        const bool smp = (s & 7) == ((h + (s >> 3) * 4) & 7) && s < 16;
        __builtin_amdgcn_raw_buffer_store_b128(u32x4{tb, tag, tb, tag}, r_ws, (smp && lane < 8) ? lane * 16 : OOB, WS_TT + (8 + 2 * h + (s >> 3)) * LSTR, AUX_SC1);
        lds_wait_ge<8>(lds, C_DONE, E_AT + E_GAP);
        const u32x4 y = __builtin_amdgcn_raw_buffer_load_b128(r_ws, lane < 16 ? lane * LSTR : OOB, WS_TT + 8 * LSTR, AUX_SC1);
        const bool oy = lane < 16 && y[1] == tag && y[3] == tag;
        if (__builtin_amdgcn_ballot_w64(oy) == 0xffffu) {
          float dl = oy ? (float)(int)(y[0] - tb) : 0.f;
          dl += dpp<XOR1>(dl); dl += dpp<XOR2>(dl); dl += dpp<XOR7>(dl); dl += dpp<XOR15>(dl);
          if (lane == 0) *reinterpret_cast<u32x2*>(lds + C_BASE) = u32x2{tb + (unsigned)(int)(dl * 0.0625f + (float)E_OFF), (unsigned)E_TAU};
        }
      }
      if (wave == 1) { lds_wait_ge<2>(lds, C_DONE, CP_REF); t_ref = now(); n_ref = lds_ld(lds, C_DONE) & 0xffffu; }
    }
    STAMP(11);
    if (wave == 1) {
#pragma nounroll
      for (unsigned spin = 0; spin < SPIN_CAP * 8u && (lds_ld(lds, C_DONE) & 0xffffu) < CP_AT; ++spin) __builtin_amdgcn_s_sleep(2);
      const unsigned t_at = now(), n_cp = lds_ld(lds, C_DONE) & 0xffffu;
      const float tau = (float)(t_at - t_ref) / (float)(n_cp - n_ref);
      const unsigned t_cp = t_at - (unsigned)(tau * (float)(n_cp - CP_AT));
      lds_wait_ge<4>(lds, C_DONE, 0x10000u * 4u);
      const unsigned t_now = now();
      const int hd = lane & 1;
      float mw[4], lw[4];
#pragma unroll
      for (int w = 0; w < 4; ++w) { const f32x2 t = *reinterpret_cast<const f32x2*>(lds + L_ML + w * 16 + hd * 8); mw[w] = t[0]; lw[w] = t[1]; }
      const float M = fmaxf(fmaxf(mw[0], mw[1]), fmaxf(mw[2], mw[3]));
      float L = 0.f;
#pragma unroll
      for (int w = 0; w < 4; ++w) L = fmaf(__builtin_amdgcn_exp2f((mw[w] - M) * C2), lw[w], L);
      __builtin_amdgcn_raw_buffer_store_b128(u32x4{(lane & 2) ? t_cp : __float_as_uint(M), tag, __float_as_uint((lane & 2) ? tau : L), tag}, r_ws,
                                             lane < 8 ? lane * 16 : OOB, WS_ML + wg * LSTR, AUX_SC1);
      sleep_until<16>(t_now, CP_WAIT);
      u32x4 ml[2];
      const unsigned mo[2] = {(lane >> 2) * LSTR + (lane & 3) * 16, (16 + (lane >> 2)) * LSTR + (lane & 3) * 16};
      if (!gather<2, 32>(ml, r_ws, mo, WS_ML + h * 32 * LSTR, tag)) timed_out = true;
      const float Ma = __uint_as_float(ml[0][0]), Mb = __uint_as_float(ml[1][0]);
      float Mg = fmaxf(Ma, Mb);
      Mg = fmaxf(Mg, dpp<ROR4>(Mg)); Mg = fmaxf(Mg, dpp<XOR8>(Mg)); Mg = x32_max(x16_max(Mg));
      float Lg = __builtin_amdgcn_exp2f((Ma - Mg) * C2) * __uint_as_float(ml[0][2]) + __builtin_amdgcn_exp2f((Mb - Mg) * C2) * __uint_as_float(ml[1][2]);
      if (lane & 2) Lg = (lane & 1) ? __uint_as_float(ml[0][2]) + __uint_as_float(ml[1][2]) : (float)(int)(ml[0][0] - t_cp) + (float)(int)(ml[1][0] - t_cp);
      Lg += dpp<ROR4>(Lg); Lg += dpp<XOR8>(Lg); Lg = x32_sum(x16_sum(Lg));
      if (lane < 2) {
#pragma unroll
        for (int w = 0; w < 4; ++w) *reinterpret_cast<float*>(lds + L_SC + w * 8 + lane * 4) = timed_out ? __builtin_nanf("") : __builtin_amdgcn_exp2f((mw[w] - Mg) * C2) / Lg;
      }
      {
        const float tm = dpp<XOR1>(Lg) * (CP_TAU / 32);
        const u32x2 tt = {t_cp + (unsigned)(int)(Lg * (1.0f / 32) + (float)CP_OFF - tm * (float)CP_AT), (unsigned)(tm * 256.f)};
        if (lane == 2 && !timed_out) *reinterpret_cast<u32x2*>(lds + C_BASE) = tt;
        const unsigned b2 = __builtin_amdgcn_readlane(tt[0], 2), u2 = __builtin_amdgcn_readlane(tt[1], 2);
        __builtin_amdgcn_raw_buffer_store_b128(u32x4{b2, tag, u2, tag}, r_ws, (s == 0 && lane < 8) ? lane * 16 : OOB, WS_TT + h * LSTR, AUX_SC1);
        sleep_until<16>(now(), COMMON_TT);
        const u32x4 x = __builtin_amdgcn_raw_buffer_load_b128(r_ws, lane < 8 ? lane * LSTR : OOB, WS_TT, AUX_SC1);
        const bool ok = lane < 8 && x[1] == tag && x[3] == tag;
        if (__builtin_amdgcn_ballot_w64(ok) == 0xffu) {
          float db = ok ? (float)(int)(x[0] - b2) : 0.f, su = ok ? (float)x[2] : 0.f;
          db += dpp<XOR1>(db); su += dpp<XOR1>(su); db += dpp<XOR2>(db); su += dpp<XOR2>(su); db += dpp<XOR7>(db); su += dpp<XOR7>(su);
          if (lane == 0) *reinterpret_cast<u32x2*>(lds + C_BASE) = u32x2{b2 + (unsigned)(int)(db * 0.125f), (unsigned)(su * 0.125f)};
        }
      }
      STAMP(14);
    }
    if (wave != 0) {
      lds_barrier();
    } else {
      lds_wait_ge<4>(lds, C_DONE, 0x10000u * 4u + ROWS_AT);
      const rsrc_t r_wo = mk_rsrc(a.wo, NQ * D * 2), r_d = mk_rsrc(a.wd, ffn ? FF * D * 2 : 0);
      const unsigned coff = lane < D / 8 ? lane * 16 : OOB;
      u32x4 dr[8], wr[16];
#pragma unroll
      for (int j = 0; j < 8; ++j) dr[j] = __builtin_amdgcn_raw_buffer_load_b128(r_d, coff, (wg * 8 + j) * (D * 2), AUX_NT);
#pragma unroll
      for (int j = 0; j < 16; ++j) wr[j] = __builtin_amdgcn_raw_buffer_load_b128(r_wo, coff, (wg * 16 + j) * (D * 2), AUX_NT);
      SB();
      lds_barrier();
      float y[8] = {0.f, 0.f, 0.f, 0.f, 0.f, 0.f, 0.f, 0.f};
#pragma unroll
      for (int j = 0; j < 8; ++j) fma8(y, *reinterpret_cast<const float*>(lds + L_H + j * 4), dr[j]);
#pragma unroll
      for (int j = 0; j < 8; ++j) {
        const u32x4 lo = wr[2 * j], hi = wr[2 * j + 1];
#pragma unroll
        for (int i = 0; i < 4; ++i) { wr[2 * j][i] = __builtin_amdgcn_perm(hi[i], lo[i], 0x05040100u); wr[2 * j + 1][i] = __builtin_amdgcn_perm(hi[i], lo[i], 0x07060302u); }
      }
      asm volatile("" : "+v"(y[0]), "+v"(y[1]), "+v"(y[2]), "+v"(y[3]), "+v"(y[4]), "+v"(y[5]), "+v"(y[6]), "+v"(y[7]), "+v"(wr[0]), "+v"(wr[1]), "+v"(wr[2]), "+v"(wr[3]),
                        "+v"(wr[4]), "+v"(wr[5]), "+v"(wr[6]), "+v"(wr[7]), "+v"(wr[8]), "+v"(wr[9]), "+v"(wr[10]), "+v"(wr[11]), "+v"(wr[12]), "+v"(wr[13]),
                        "+v"(wr[14]), "+v"(wr[15]));
      STAMP(13);
      u32x4 po[4];
      const unsigned oo[4] = {(unsigned)lane * 16, (unsigned)lane * 16 + 1024, (unsigned)lane * 16 + 2048, (unsigned)lane * 16 + 3072};
      {
        const unsigned due = lds_ld(lds, C_BASE) + lds_ld(lds, C_BASE + 4) + (unsigned)(H1_DUE);
#pragma nounroll
        for (int spin = 0; spin < 128 && (int)(now() - due) < 0; ++spin) __builtin_amdgcn_s_sleep(1);
      }
      if (!gather<4, S1>(po, r_ws, oo, WS_PO + wg * 4096, tag)) timed_out = true;
      STAMP(7);
      float o0 = (__uint_as_float(po[0][0]) + __uint_as_float(po[1][0])) + (__uint_as_float(po[2][0]) + __uint_as_float(po[3][0]));
      float o1 = (__uint_as_float(po[0][2]) + __uint_as_float(po[1][2])) + (__uint_as_float(po[2][2]) + __uint_as_float(po[3][2]));
      o0 += dpp<XOR8>(o0); o1 += dpp<XOR8>(o1);
      o0 = x32_sum(x16_sum(o0)); o1 = x32_sum(x16_sum(o1));
      if (timed_out) o0 = __builtin_nanf("");
      const unsigned opk = pk_bf16(o0, o1);
      unsigned oj[8];
#pragma unroll
      for (int j = 0; j < 8; ++j) oj[j] = __builtin_amdgcn_readlane(opk, j);
      const unsigned yoff = lane < D / 8 ? (unsigned)(lane >> 2) * (4 * 256 * CPR * 8) + (lane & 3) * 16 : OOB;
#pragma unroll
      for (int k = 0; k < 4; ++k) {
#pragma unroll
        for (int j = 0; j < 8; ++j) { y[2 * k] = dot2(wr[2 * j][k], oj[j], y[2 * k]); y[2 * k + 1] = dot2(wr[2 * j + 1][k], oj[j], y[2 * k + 1]); }
        __builtin_amdgcn_raw_buffer_store_b128(u32x4{__float_as_uint(y[2 * k]), tag, __float_as_uint(y[2 * k + 1]), tag}, r_ws, yoff,
                                               WS_YA + k * (256 * CPR * 8) + wg * (CPR * 8), AUX_SC1);
      }
      STAMP(8);
      lds_arrive(lds, C_HOP1, lane);
    }
  } else {
    const int w = wave - 4;
    const unsigned t0 = wg_off + OFF_W(wave);
    LANE_VARS;
    SCORE_VARS;
    STAMP0();
    const rsrc_t r_v = mk_rsrc(a.v, 8u * SK * DH * 2);
    const unsigned vv0 = OFF_G(g) + piece(0, c >> 2) * 64 + (c & 3) * 16, vv1 = OFF_G(g) + piece(1, c >> 2) * 64 + (c & 3) * 16;
    u32x4 rx[NI - 2][8];
    static_assert(CH_W >= 5, "");
    lds_barrier();
    if (!CH_TIMED) { asm volatile("s_waitcnt vmcnt(8)" ::: "memory"); lds_arrive(lds, C_LANDED, lane); PROGL(); }
#define CHAIN_STEP(R, SLOT) do { \
      constexpr bool first_ = CH_W < 5;     \
      if (first_ && lds_ld(lds, C_QRDY) == 0u) { asm volatile("s_waitcnt vmcnt(0)" ::: "memory"); lds_arrive(lds, C_LANDED, lane); PROGL(); } \
      chain_wait(lds, 4 * (SLOT) + w, t_start); \
      issue<AUX_K>(R, r_k, vw0, vw1, t0 + OFF_SLOT(SLOT)); \
      if (!CH_TIMED && !first_ && lds_ld(lds, C_QRDY) == 0u) { asm volatile("s_waitcnt vmcnt(8)" ::: "memory"); lds_arrive(lds, C_LANDED, lane); PROGL(); } } while (0)
#pragma unroll
    for (int i = 2; i < NI; ++i) CHAIN_STEP(rx[i - 2], i);
#undef CHAIN_STEP
    if (!CH_TIMED && lds_ld(lds, C_QRDY) == 0u) { asm volatile("s_waitcnt vmcnt(0)" ::: "memory"); lds_arrive(lds, C_LANDED, lane); PROGL(); }
    lds_wait_ge(lds, C_QRDY, 1u);
    tag = lds_ld(lds, C_TAG);
    STAMP(2);
    u32x4 qf[2];
    load_qf(qf, lds, 1, c, g);
    unsigned char* const pl = lds + L_P + w * 2048;
    auto pace = [&](unsigned level) -> int {
      u32x4 cw;
#pragma nounroll
      for (unsigned spin = 0; spin < SPIN_CAP; ++spin) {
        FENCE();
        cw = *reinterpret_cast<const u32x4*>(lds + C_DONE);
        if ((__builtin_amdgcn_readfirstlane(cw[0]) & 0xffffu) + SLACK >= level) break;
        __builtin_amdgcn_s_sleep(2);
      }
      const unsigned done = __builtin_amdgcn_readfirstlane(cw[0]) & 0xffffu, tau = __builtin_amdgcn_readfirstlane(cw[3]);
      const int lag = (int)(now() - __builtin_amdgcn_readfirstlane(cw[2]) - ((done * tau) >> 8));
      return tau == 0u ? 0 : lag > w * CP_STEP ? 1 : lag < -CP_AHEAD - w * CP_STEPS ? 2 : 0;
    };
    {
      f32x4 d[4];
      unsigned short* sw = reinterpret_cast<unsigned short*>(pl + sc_lane);
      unsigned tp = OFF_SLOT(NI + 3);
#define K_STEP(R, SW, ISSUE_NEXT, SLOT) do { qk_mfma(R, qf, d, SW); PROG(); ISSUE_NEXT; sw[(SLOT) * 32] = qk_score(d, b0, b1); } while (0)
      bool ks = g == 3;
      K_STEP(ra, false, issue<AUX_K>(ra, r_k, ks ? vw1 : vw0, ks ? vw0 : vw1, t0 + tp), 0);
      K_STEP(rb, false, issue<AUX_K>(rb, r_k, ks ? vw1 : vw0, ks ? vw0 : vw1, t0 + tp + SLOT1), 1);
#pragma unroll
      for (int i = 2; i < NI; ++i) K_STEP(rx[i - 2], false, (void)0, i);
      lds_add(lds, C_DONE, NI, lane);
      sw += (NI + 3) * 32;
      for (int it = 1; it < (29 - NI) / 2; ++it) {
        tp = OFF_SLOT(NI + 3 + 2 * it);
        const int nk = pace(it >= KSLACK_FROM ? 4 * NI + 4 + 8 * it : 0);
        const bool kn = (g & (nk == 0 ? 3 : nk == 2 ? 1 : 0)) == (nk == 0 ? 3 : 1);
        const unsigned n0 = kn ? vw1 : vw0, n1 = kn ? vw0 : vw1;
        K_STEP(ra, ks, issue<AUX_K>(ra, r_k, n0, n1, t0 + tp), 0);
        K_STEP(rb, ks, issue<AUX_K>(rb, r_k, n0, n1, t0 + tp + SLOT1), 1);
        ks = kn;
        lds_add(lds, C_DONE, 2u, lane);
        sw += 2 * 32;
      }
      K_STEP(ra, ks, issue<AUX_V>(ra, r_v, vv0, vv1, t0), 0);
      K_STEP(rb, ks, issue<AUX_V>(rb, r_v, vv0, vv1, t0 + SLOT1), 1);
#undef K_STEP
    }
    STAMP(12);
    lds_wait_ge(lds, C_KHELP, 3u);
    {
      u32x4* pp = reinterpret_cast<u32x4*>(pl + lane * 32);
      u32x4 t[2] = {pp[0], pp[1]};
      float mx = bf_lo(t[0][0]);
#pragma unroll
      for (int j = 0; j < 2; ++j)
#pragma unroll
        for (int i = 0; i < 4; ++i) mx = fmaxf(mx, fmaxf(bf_lo(t[j][i]), bf_hi(t[j][i])));
      mx = parity_max(mx);
      const float mc = mx * C2;
      float lp = 0.f;
#pragma unroll
      for (int j = 0; j < 2; ++j) {
#pragma unroll
        for (int i = 0; i < 4; ++i) {
          const float p0 = __builtin_amdgcn_exp2f(fmaf(bf_lo(t[j][i]), C2, -mc)), p1 = __builtin_amdgcn_exp2f(fmaf(bf_hi(t[j][i]), C2, -mc));
          lp += p0 + p1; t[j][i] = pk_bf16(p0, p1);
        }
        pp[j] = t[j];
      }
      lp = parity_sum(lp);
      if (lane < 2) *reinterpret_cast<f32x2*>(lds + L_ML + w * 16 + lane * 8) = f32x2{mx, lp};
      asm volatile("s_waitcnt lgkmcnt(0)" ::: "memory");
      lds_add(lds, C_DONE, 0x10002u, lane);
    }
    f32x4 acc0 = {0.f, 0.f, 0.f, 0.f}, acc1 = {0.f, 0.f, 0.f, 0.f};
    {
      const int msel = c >> 1;
      const unsigned char* pr = pl + (c & 1) * 32 + g * 8;
      const unsigned vb = OFF_G(g) + (c & 3) * 16;
      float* const rec = reinterpret_cast<float*>(lds + L_ACC + w * 2048) + 8 * (c & 3) + g;
      auto put = [&](int m) {
        const int e0 = piece_m(m, 0, c >> 2) * 32, e1 = piece_m(m, 1, c >> 2) * 32;
#pragma unroll
        for (int k = 0; k < 4; ++k) { float* r = rec + (k & 1) * 256 + ((k >> 1) ? e1 : e0); r[0] = acc0[k]; r[4] = acc1[k]; }
      };
      auto get = [&](int m) {
        const int e0 = piece_m(m, 0, c >> 2) * 32, e1 = piece_m(m, 1, c >> 2) * 32;
#pragma unroll
        for (int k = 0; k < 4; ++k) { const float* r = rec + (k & 1) * 256 + ((k >> 1) ? e1 : e0); acc0[k] = r[0]; acc1[k] = r[4]; }
      };
      int cur = 0;
#define V_STEP(R, ISSUE_NEXT) do { const u32x2 pk_ = *reinterpret_cast<const u32x2*>(pr); pr += 64; pv_mfma(R, pk_, msel, acc0, acc1); PROG(); ISSUE_NEXT; } while (0)
      for (int it = 0; it < 15; ++it) {
        const int nxt = pace(128 + 8 * it);
        const unsigned tp = OFF_SLOT(2 + 2 * it);
        const unsigned n0 = vb + piece_m(nxt, 0, c >> 2) * 64, n1 = vb + piece_m(nxt, 1, c >> 2) * 64;
        V_STEP(ra, issue<AUX_V>(ra, r_v, n0, n1, t0 + tp));
        V_STEP(rb, issue<AUX_V>(rb, r_v, n0, n1, t0 + tp + SLOT1));
        lds_add(lds, C_DONE, 2u, lane);
        if (nxt != cur) { put(cur); FENCE(); get(nxt); cur = nxt; }
        FENCE();
      }
      V_STEP(ra, lds_add(lds, C_DONE, 1u, lane));
      V_STEP(rb, (void)0);
#undef V_STEP
      STAMP(3);
      put(cur);
      asm volatile("" :: "v"(cpf));
    }
    STAMP(4);
    lds_barrier();
    STAMP(5);
    {
      const int u = tid - 256;
      float o0 = 0.f, o1 = 0.f;
#pragma unroll
      for (int k = 0; k < 4; ++k) {
        const float sc = *reinterpret_cast<const float*>(lds + L_SC + k * 8 + (u >> 7) * 4);
        const f32x2 t = *reinterpret_cast<const f32x2*>(lds + L_ACC + k * 2048 + u * 8);
        o0 = fmaf(sc, t[0], o0); o1 = fmaf(sc, t[1], o1);
      }
      __builtin_amdgcn_raw_buffer_store_b128(u32x4{__float_as_uint(o0), tag, __float_as_uint(o1), tag}, r_ws, (u >> 3) * 4096 + (u & 7) * 16,
                                             WS_PO + h * (32 * 4096) + s * 128, AUX_SC1);
    }
    STAMP(6);
  }
  LANE_VARS;
  PROG_OUT();
  if (wg >= NRED) return;
  if (wave >= 4) {
    const unsigned t = __builtin_amdgcn_raw_buffer_load_b32(r_ws, lane < CPR * 4 ? ((wave - 4) * CPR * 4 + lane) * 128 : OOB, WS_YA + wg * (256 * CPR * 8), AUX_SC1);
    asm volatile("" :: "v"(t));
  }
  lds_wait_ge<H2S>(lds, C_HOP1, 1u);
  if (H2_PRE > 0) __builtin_amdgcn_s_sleep(H2_PRE);
  {
    constexpr int NLD = (256 * CPR / 2 + NTHR - 1) / NTHR;
    static_assert(CPR == 8, "");
    const int col = (wg >> 2) * 32 + ((tid >> 1) & 3) * 8 + (wg & 3) * 2 + (tid & 1);
    const float xo = tid < CPR ? bf_lo(a.x[col]) : 0.f;
    u32x4 t[NLD];
    unsigned yo[NLD];
#pragma unroll
    for (int k = 0; k < NLD; ++k) yo[k] = tid + NTHR * k < 256 * CPR / 2 ? (unsigned)(tid + NTHR * k) * 16 : OOB;
    if (!gather<NLD, 0>(t, r_ws, yo, WS_YA + wg * (256 * CPR * 8), tag)) timed_out = true;
    STAMP(9);
    float s0 = 0.f, s1 = 0.f;
#pragma unroll
    for (int k = 0; k < NLD; ++k) { s0 += __uint_as_float(t[k][0]); s1 += __uint_as_float(t[k][2]); }
    if (CPR / 2 <= 8) { s0 += dpp<XOR8>(s0); s1 += dpp<XOR8>(s1); }
    if (CPR / 2 <= 4) { s0 += dpp<ROR4>(s0); s1 += dpp<ROR4>(s1); }
    if (CPR / 2 <= 2) { s0 += dpp<XOR2>(s0); s1 += dpp<XOR2>(s1); }
    if (CPR / 2 <= 1) { s0 += dpp<XOR1>(s0); s1 += dpp<XOR1>(s1); }
    s0 = x32_sum(x16_sum(s0)); s1 = x32_sum(x16_sum(s1));
    if (timed_out) s0 = __builtin_nanf("");
    if (lane < CPR / 2) *reinterpret_cast<f32x2*>(lds + L_YA + wave * 64 + lane * 8) = f32x2{s0, s1};
    lds_barrier();
    if (tid < CPR) {
      float r = xo;
#pragma unroll
      for (int w = 0; w < 8; ++w) r += *reinterpret_cast<const float*>(lds + L_YA + w * 64 + tid * 4);
#define AUX_OUT 17
      __builtin_amdgcn_raw_buffer_store_b16((unsigned short)(pk_bf16(r, r) & 0xffffu), mk_rsrc(a.out, D * 2), col * 2, 0, AUX_OUT);
    }
    if (wg == 0 && tid == 64) __builtin_amdgcn_raw_buffer_store_b32(tag, r_ws, 0, WS_EPOCH, AUX_SC1);
    STAMP(10);
  }
}

}

extern "C" void kernel_launch(void* const* d_in, const int* in_sizes, int n_in, void* d_out, int out_size, void* d_ws, size_t ws_size,
                              hipStream_t stream) {
  (void)in_sizes; (void)n_in; (void)out_size; (void)ws_size;
  Args a;
  a.x = (const unsigned short*)d_in[0];
  a.k = (const unsigned char*)d_in[1];
  a.v = (const unsigned char*)d_in[2];
  a.wq = (const unsigned char*)d_in[3];
  a.wo = (const unsigned char*)d_in[4];
  a.wg = (const unsigned char*)d_in[6];
  a.wu = (const unsigned char*)d_in[7];
  a.wd = (const unsigned char*)d_in[8];
  a.out = (unsigned short*)d_out;
  a.ws = (unsigned char*)d_ws;
  (void)hipFuncSetAttribute((const void*)fused_decode, hipFuncAttributeMaxDynamicSharedMemorySize, LDS_BYTES);
  hipLaunchKernelGGL(fused_decode, dim3(NWG), dim3(NTHR), LDS_BYTES, stream, a);
}
```

```cpp
#define PROBE 13
#define PROBE_WAVE 4
#define PROBE_WG 8
#define PROBE_SUB 0
#include <hip/hip_runtime.h>
#include <hip/hip_bf16.h>
#include <cstdint>

namespace {

typedef unsigned u32x4 __attribute__((ext_vector_type(4)));
typedef unsigned u32x2 __attribute__((ext_vector_type(2)));
typedef float f32x4 __attribute__((ext_vector_type(4)));
typedef float f32x2 __attribute__((ext_vector_type(2)));
typedef __bf16 bf16x8 __attribute__((ext_vector_type(8)));
typedef __bf16 bf16x2 __attribute__((ext_vector_type(2)));
typedef __amdgpu_buffer_rsrc_t rsrc_t;

constexpr int D = 448, SK = 65536, DH = 256, FF = 1792, NQ = 4096;
constexpr int NWG = 256, NTHR = 512;
#define CH_W 10
#define NI 5
#define HK_PRIO 2
#define CP_REF 64
#define CP_AT 116
#define CP_WAIT 350
#define CP_OFF -30
#define CP_TAU 1.0f
#define CODE_PF 20000
#define CODE_WGS 32
#define S1 0
#define H2_PRE 8
#define H1_DUE -50
#define H2S 1
#define ROWS_AT 252
#define PREWARM 4
#define PREWARM_AT 240
#define AUX_ST 16
#define AUX_LD 16
#define CP_STEP 60
#define CP_STEPS 30
#define CP_AHEAD 40
#define CH_TIMED 1
#define CH_T0 60
#define CH_TAU 27
#define COMMON_TT 500
#define E_AT 44
#define E_GAP 14
#define E_TAU 6900
#define E_OFF -20
#define KSLACK_FROM 3
#define SLACK 4
#define Q_FIRST 750
#define Q_SLEEP 0
#define GU_AT 1500
#define CPR 8
constexpr int NRED = D / CPR;
#define KEY_MAP {3, 4, 8, 7, 6, 0, 10, 14, 11, 15, 1, 9, 2, 13, 5, 12}
constexpr int KPOS[16] = KEY_MAP;
template <int FIRST, int N> __host__ __device__ constexpr unsigned scatter(unsigned v) {
  unsigned o = 0;
  for (int i = 0; i < N; ++i) o |= ((v >> i) & 1u) << (KPOS[FIRST + i] + 9);
  return o;
}
#define OFF_J(j) scatter<0, 2>(j)
#define OFF_G(G) scatter<2, 2>(G)
#define OFF_W(w) scatter<4, 2>(w)
#define OFF_SLOT(t) scatter<6, 5>(t)
#define OFF_S(s) scatter<11, 5>(s)
constexpr unsigned SLOT1 = OFF_SLOT(1);
constexpr unsigned OOB = 0x40000000u;
constexpr float C2 = 0.0625f * 1.4426950408889634f;
constexpr unsigned SPIN_CAP = 1u << 22;

constexpr unsigned WS_EPOCH = 0;
constexpr unsigned LSTR = 4352;
constexpr unsigned WS_PO = 4096;
constexpr unsigned WS_YA = WS_PO + 8 * 32 * 32 * 16 * 8;
constexpr unsigned WS_GU = WS_YA + 256 * D * 8;
constexpr unsigned WS_QG = WS_GU + 224 * 4 * 128;
constexpr unsigned WS_ML = WS_QG + 128 * LSTR;
constexpr unsigned WS_TT = WS_ML + 256 * LSTR;
constexpr unsigned WS_END = WS_TT + 24 * LSTR;

constexpr int L_RED = 0;
constexpr int L_CTL = 1024;
constexpr int C_PART = L_CTL, C_QRDY = L_CTL + 4, C_TAG = L_CTL + 12, C_LANDED = L_CTL + 16, C_KHELP = L_CTL + 20, C_HRDY = L_CTL + 24, C_HOP1 = L_CTL + 28,
              C_DONE = L_CTL + 32, C_BASE = L_CTL + 40;
constexpr int L_ML = 1088;
constexpr int L_SC = 1152;
constexpr int L_H = 1184;
constexpr int L_YA = 1216;
constexpr int L_Q = 1792;
constexpr int L_XN = 2816;
constexpr int L_ACC = 4864;
constexpr int L_P = L_ACC + 4 * 2048;
#define LDS_KB 44
constexpr int LDS_BYTES = LDS_KB * 1024;

constexpr int AUX_NT = 2, AUX_SC1 = 16;
#define AUX_K 2
#define AUX_V 2
#define SB() __builtin_amdgcn_sched_barrier(0)
#define FENCE() asm volatile("" ::: "memory")

__device__ __forceinline__ float bf_lo(unsigned u) { return __builtin_bit_cast(float, u << 16); }
__device__ __forceinline__ float bf_hi(unsigned u) { return __builtin_bit_cast(float, u & 0xffff0000u); }
__device__ __forceinline__ unsigned pk_bf16(float lo, float hi) {
  bf16x2 v = {(__bf16)lo, (__bf16)hi};
  return __builtin_bit_cast(unsigned, v);
}
__device__ __forceinline__ float rnd_bf16(float x) { return (float)(__bf16)x; }
__device__ __forceinline__ float dot2(unsigned a, unsigned b, float acc) {
  return __builtin_amdgcn_fdot2_f32_bf16(__builtin_bit_cast(bf16x2, a), __builtin_bit_cast(bf16x2, b), acc, false);
}
template <int CTRL> __device__ __forceinline__ float dpp(float x) {
  return __builtin_bit_cast(float, __builtin_amdgcn_mov_dpp(__builtin_bit_cast(int, x), CTRL, 0xf, 0xf, true));
}
constexpr int XOR1 = 0xB1, XOR2 = 0x4E, XOR7 = 0x141, XOR15 = 0x140, XOR8 = 0x128, ROR4 = 0x124;
__device__ __forceinline__ float x16_max(float x) {
  auto s = __builtin_amdgcn_permlane16_swap(__float_as_uint(x), __float_as_uint(x), false, false);
  return fmaxf(__uint_as_float(s[0]), __uint_as_float(s[1]));
}
__device__ __forceinline__ float x32_max(float x) {
  auto s = __builtin_amdgcn_permlane32_swap(__float_as_uint(x), __float_as_uint(x), false, false);
  return fmaxf(__uint_as_float(s[0]), __uint_as_float(s[1]));
}
__device__ __forceinline__ float x16_sum(float x) {
  auto s = __builtin_amdgcn_permlane16_swap(__float_as_uint(x), __float_as_uint(x), false, false);
  return __uint_as_float(s[0]) + __uint_as_float(s[1]);
}
__device__ __forceinline__ float x32_sum(float x) {
  auto s = __builtin_amdgcn_permlane32_swap(__float_as_uint(x), __float_as_uint(x), false, false);
  return __uint_as_float(s[0]) + __uint_as_float(s[1]);
}
__device__ __forceinline__ float wave_sum(float x) {
  x += dpp<XOR1>(x); x += dpp<XOR2>(x); x += dpp<XOR7>(x); x += dpp<XOR15>(x);
  return x32_sum(x16_sum(x));
}
__device__ __forceinline__ float parity_sum(float x) {
  x += dpp<XOR2>(x); x += dpp<ROR4>(x); x += dpp<XOR8>(x);
  return x32_sum(x16_sum(x));
}
__device__ __forceinline__ float parity_max(float x) {
  x = fmaxf(x, dpp<XOR2>(x)); x = fmaxf(x, dpp<ROR4>(x)); x = fmaxf(x, dpp<XOR8>(x));
  return x32_max(x16_max(x));
}
__device__ __forceinline__ void lds_barrier() {
  asm volatile("s_waitcnt lgkmcnt(0)" ::: "memory");
  __builtin_amdgcn_s_barrier();
  FENCE();
}
__device__ __forceinline__ rsrc_t mk_rsrc(const void* p, unsigned bytes) {
  return __builtin_amdgcn_make_buffer_rsrc(const_cast<void*>(p), 0, bytes, 0x00020000);
}
__device__ __forceinline__ unsigned lds_ld(unsigned char* lds, int off) {
  return __builtin_amdgcn_readfirstlane(__hip_atomic_load(reinterpret_cast<unsigned*>(lds + off), __ATOMIC_RELAXED, __HIP_MEMORY_SCOPE_WORKGROUP));
}
template <int SLEEP = 1>
__device__ __forceinline__ void lds_wait_ge(unsigned char* lds, int off, unsigned want) {
#pragma nounroll
  for (unsigned spin = 0; spin < SPIN_CAP * 8u; ++spin) {
    if (lds_ld(lds, off) >= want) break;
    __builtin_amdgcn_s_sleep(SLEEP);
  }
  FENCE();
}
__device__ __forceinline__ void lds_arrive(unsigned char* lds, int off, int lane) {
  asm volatile("s_waitcnt lgkmcnt(0)" ::: "memory");
  if (lane == 0) __hip_atomic_fetch_add(reinterpret_cast<unsigned*>(lds + off), 1u, __ATOMIC_RELAXED, __HIP_MEMORY_SCOPE_WORKGROUP);
}
__device__ __forceinline__ void lds_add(unsigned char* lds, int off, unsigned v, int lane) {
  if (lane == 0) __hip_atomic_fetch_add(reinterpret_cast<unsigned*>(lds + off), v, __ATOMIC_RELAXED, __HIP_MEMORY_SCOPE_WORKGROUP);
}
__device__ __forceinline__ unsigned now() { return (unsigned)__builtin_amdgcn_s_memrealtime(); }
template <int SLEEP>
__device__ __forceinline__ void sleep_until(unsigned t0, unsigned ticks) {
#pragma nounroll
  for (unsigned spin = 0; spin < SPIN_CAP && now() - t0 < ticks; ++spin) __builtin_amdgcn_s_sleep(SLEEP);
}
__device__ __forceinline__ void chain_wait(unsigned char* lds, int idx, unsigned t_start) {
#pragma nounroll
  for (unsigned spin = 0; spin < SPIN_CAP * 8u; ++spin) {
    if (now() - t_start >= (unsigned)(CH_T0 + idx * CH_TAU) || lds_ld(lds, C_QRDY) != 0u) break;
    __builtin_amdgcn_s_sleep(1);
  }
  FENCE();
}
template <int N, int SLEEP>
__device__ __forceinline__ bool gather(u32x4 (&v)[N], rsrc_t rs, const unsigned (&voff)[N], unsigned soff, unsigned tag) {
  bool have[N];
#pragma unroll
  for (int k = 0; k < N; ++k) { have[k] = voff[k] == OOB; v[k] = u32x4{0u, 0u, 0u, 0u}; }
#pragma nounroll
  for (unsigned spin = 0; spin < SPIN_CAP; ++spin) {
    FENCE();
    u32x4 t[N];
#pragma unroll
    for (int k = 0; k < N; ++k) t[k] = __builtin_amdgcn_raw_buffer_load_b128(rs, have[k] ? OOB : voff[k], soff, AUX_LD);
    bool all = true;
#pragma unroll
    for (int k = 0; k < N; ++k) {
      const bool now = !have[k] && t[k][1] == tag && t[k][3] == tag;
      v[k][0] = now ? t[k][0] : v[k][0]; v[k][2] = now ? t[k][2] : v[k][2];
      have[k] = have[k] || now; all = all && have[k];
    }
    if (__builtin_amdgcn_ballot_w64(!all) == 0) return true;
    if (SLEEP > 0) __builtin_amdgcn_s_sleep(SLEEP);
  }
  return false;
}
#define STAMP(slot) do { if (lane == 0) __hip_atomic_store(reinterpret_cast<unsigned*>(lds + 40960) + wave * 16 + (slot), now(), __ATOMIC_RELAXED, __HIP_MEMORY_SCOPE_WORKGROUP); } while (0)
#define PROG() do {} while (0)
#define PROG_OUT() do {} while (0)
#define PROGL() do {} while (0)

struct Args {
  const unsigned short* x;
  const unsigned char* k;
  const unsigned char* v;
  const unsigned char* wq;
  const unsigned char* wo;
  const unsigned char* wg;
  const unsigned char* wu;
  const unsigned char* wd;
  unsigned short* out;
  unsigned char* ws;
};

__host__ __device__ constexpr int piece(int e, int n) { return n < 2 ? 2 * e + n : 2 * n + e; }
__device__ __forceinline__ int piece_m(int m, int e, int n) { return m == 1 ? 4 * e + n : (m == 0 && n < 2) ? 2 * e + n : 2 * n + e; }
template <int AUX>
__device__ __forceinline__ void issue(u32x4 (&r)[8], rsrc_t rs, unsigned v0, unsigned v1, unsigned soff) {
#pragma unroll
  for (int i = 0; i < 8; ++i) { r[i] = __builtin_amdgcn_raw_buffer_load_b128(rs, (i & 1) ? v1 : v0, soff + OFF_J(i >> 1), AUX); SB(); }
}
__device__ __forceinline__ f32x4 mfma(u32x4 x, u32x4 y, f32x4 acc) {
  return __builtin_amdgcn_mfma_f32_16x16x32_bf16(__builtin_bit_cast(bf16x8, x), __builtin_bit_cast(bf16x8, y), acc, 0, 0, 0);
}
__device__ __forceinline__ void qk_mfma(const u32x4 (&r)[8], const u32x4 (&qf)[2], f32x4 (&d)[4], bool sw) {
#pragma unroll
  for (int j = 0; j < 4; ++j) {
    u32x4 x0, x1;
#pragma unroll
    for (int i = 0; i < 4; ++i) { x0[i] = sw ? r[2 * j + 1][i] : r[2 * j][i]; x1[i] = sw ? r[2 * j][i] : r[2 * j + 1][i]; }
    d[j] = mfma(qf[1], x1, mfma(qf[0], x0, f32x4{0.f, 0.f, 0.f, 0.f}));
  }
  SB();
}
__device__ __forceinline__ unsigned short qk_score(const f32x4 (&d)[4], bool b0, bool b1) {
  float v[4];
#pragma unroll
  for (int j = 0; j < 4; ++j) {
    float e0 = d[j][0], e1 = d[j][1], e2 = d[j][2], e3 = d[j][3];
    asm("" : "+v"(e0), "+v"(e2));
    const float lo = b0 ? e1 : e0, hi = b0 ? e3 : e2;
    v[j] = b1 ? hi : lo;
  }
  const float u0 = (b0 ? v[1] : v[0]) + dpp<XOR1>(b0 ? v[0] : v[1]);
  const float u1 = (b0 ? v[3] : v[2]) + dpp<XOR1>(b0 ? v[2] : v[3]);
  const float w = (b1 ? u1 : u0) + dpp<XOR2>(b1 ? u0 : u1);
  return (unsigned short)(pk_bf16(w, w) & 0xffffu);
}
__device__ __forceinline__ void pv_mfma(const u32x4 (&vr)[8], u32x2 pk, int msel, f32x4& acc0, f32x4& acc1) {
#pragma unroll
  for (int q = 0; q < 4; ++q) {
    const u32x4 pa = {msel == 2 * q ? pk[0] : 0u, msel == 2 * q ? pk[1] : 0u, msel == 2 * q + 1 ? pk[0] : 0u, msel == 2 * q + 1 ? pk[1] : 0u};
    const unsigned sel = (q & 1) ? 0x07060302u : 0x05040100u;
    const int d0 = q >> 1, d1 = 2 + (q >> 1);
    const u32x4 b0 = {__builtin_amdgcn_perm(vr[2][d0], vr[0][d0], sel), __builtin_amdgcn_perm(vr[6][d0], vr[4][d0], sel),
                      __builtin_amdgcn_perm(vr[3][d0], vr[1][d0], sel), __builtin_amdgcn_perm(vr[7][d0], vr[5][d0], sel)};
    const u32x4 b1 = {__builtin_amdgcn_perm(vr[2][d1], vr[0][d1], sel), __builtin_amdgcn_perm(vr[6][d1], vr[4][d1], sel),
                      __builtin_amdgcn_perm(vr[3][d1], vr[1][d1], sel), __builtin_amdgcn_perm(vr[7][d1], vr[5][d1], sel)};
    acc0 = mfma(pa, b0, acc0);
    acc1 = mfma(pa, b1, acc1);
  }
  SB();
}
__device__ __forceinline__ void fma8(float (&s)[8], float xv, u32x4 w) {
#pragma unroll
  for (int i = 0; i < 4; ++i) { s[2 * i] = fmaf(xv, bf_lo(w[i]), s[2 * i]); s[2 * i + 1] = fmaf(xv, bf_hi(w[i]), s[2 * i + 1]); }
}
__device__ __forceinline__ void load_qf(u32x4 (&qf)[2], const unsigned char* lds, int m, int c, int g) {
#pragma unroll
  for (int e = 0; e < 2; ++e) qf[e] = *reinterpret_cast<const u32x4*>(lds + L_Q + ((c >> 2) & 1) * 512 + (piece_m(m, e, g) * 4 + (c & 3)) * 16);
}

__global__ void __launch_bounds__(NTHR, 2) fused_decode(Args a) {
  extern __shared__ __attribute__((aligned(16))) unsigned char lds[];
  const int lane0 = threadIdx.x & 63, wave = __builtin_amdgcn_readfirstlane(threadIdx.x >> 6);
  const int wg = blockIdx.x, h = wg >> 5, s = wg & 31;
  const unsigned tstart0 = now();
  unsigned long long pc;
  asm volatile("s_getpc_b64 %0" : "=s"(pc));
  const unsigned cpf = __builtin_amdgcn_raw_buffer_load_b32(mk_rsrc(reinterpret_cast<const void*>(pc), wg < CODE_WGS ? CODE_PF : 0), threadIdx.x * 128, 0, 0);
  SB();
  const unsigned t_start = now();
  const bool helper = wave < 4;
  const unsigned wg_off = (unsigned)h * (SK * DH * 2) + OFF_S(wg);
  const unsigned vw0 = OFF_G(lane0 >> 2) + (lane0 >> 4) * 64 + (lane0 & 3) * 16, vw1 = vw0 + 256;
  const rsrc_t r_k = mk_rsrc(a.k, 8u * SK * DH * 2);
  u32x4 ra[8], rb[8];
#pragma unroll
  for (int i = 0; i < 8; ++i) asm volatile("" : "=v"(ra[i]), "=v"(rb[i]));
  if (!helper) {
    const unsigned t0 = wg_off + OFF_W(wave);
    issue<AUX_K>(ra, r_k, vw0, vw1, t0);
    issue<AUX_K>(rb, r_k, vw0, vw1, t0 + SLOT1);
  }
  const rsrc_t r_ws = mk_rsrc(a.ws, WS_END);
#define LANE_VARS int tid = threadIdx.x; asm volatile("" : "+v"(tid)); const int lane = tid & 63, c = lane & 15, g = lane >> 4; (void)c; (void)g
#define SCORE_VARS const bool b0 = (c & 1) != 0, b1 = (c & 2) != 0; const int sc_lane = (g & 1) * 32 + c * 2
#define STAMP0() do { if (lane == 0) __hip_atomic_store(reinterpret_cast<unsigned*>(lds + 40960) + wave * 16, tstart0, __ATOMIC_RELAXED, __HIP_MEMORY_SCOPE_WORKGROUP); } while (0)
  bool timed_out = false;
  unsigned tag;

  if (__builtin_expect(helper, 1)) {
    __builtin_amdgcn_s_setprio(3);
    LANE_VARS;
    const bool qwg = (s & 8) == 0;
    const int qsl = (s & 7) * 2 + (s >> 4);
    const int fb = h * 16 + qsl;
    const int wrow = (lane >> (qwg ? 2 : 3)) + (qwg ? 16 * wave : (fb & 3) * 112 + (wave >> 1) * 56), wstep = qwg ? 64 : 8;
    const u32x4 xv = __builtin_amdgcn_raw_buffer_load_b128(mk_rsrc(a.x, D * 2), lane * 16, 0, 0);
    const unsigned epoch = __builtin_amdgcn_raw_buffer_load_b32(r_ws, 0, WS_EPOCH, AUX_SC1);
    u32x4 wv[7];
    {
      const unsigned rowb = qwg ? NQ * 2 : FF * 2;
      const rsrc_t r0 = mk_rsrc(qwg ? a.wq : (wave & 1) ? a.wu : a.wg, (qwg || fb < 112) ? D * rowb : 0u);
      const unsigned v0 = wrow * rowb + (lane & (qwg ? 3 : 7)) * 16, s0 = qwg ? (h * 512 + qsl * 32) * 2 : (fb >> 2) * 128;
#pragma unroll
      for (int i = 0; i < 7; ++i) wv[i] = __builtin_amdgcn_raw_buffer_load_b128(r0, v0, s0 + i * wstep * rowb, AUX_NT);
    }
    unsigned t_ref = 0, n_ref = 0;
    SB();
    STAMP0();
    SCORE_VARS;
    if (tid < 16) *reinterpret_cast<unsigned*>(lds + L_CTL + tid * 4) = 0u;
    lds_barrier();

    {
      float xs[8];
#pragma unroll
      for (int i = 0; i < 4; ++i) { xs[2 * i] = bf_lo(xv[i]); xs[2 * i + 1] = bf_hi(xv[i]); }
      float sm = 0.f;
#pragma unroll
      for (int i = 0; i < 8; ++i) sm += xs[i];
      const float mean = wave_sum(sm) * (1.0f / D);
      float sq = 0.f;
#pragma unroll
      for (int i = 0; i < 8; ++i) { const float d = xs[i] - mean; sq = fmaf(d, d, sq); }
      sq = lane < D / 8 ? sq : 0.f;
      const float var = wave_sum(sq) * (1.0f / D);
      const float mean_b = rnd_bf16(mean);
      const float den_b = rnd_bf16(__builtin_amdgcn_sqrtf(rnd_bf16(rnd_bf16(var) + 1e-5f)));
      float rcp = __builtin_amdgcn_rcpf(den_b);
      rcp = fmaf(fmaf(-den_b, rcp, 1.0f), rcp, rcp);
#pragma unroll
      for (int i = 0; i < 8; ++i) {
        const float n = rnd_bf16(xs[i] - mean_b), q = n * rcp;
        xs[i] = rnd_bf16(fmaf(fmaf(-den_b, q, n), rcp, q));
      }
      f32x4* xp = reinterpret_cast<f32x4*>(lds + L_XN + lane * 32);
      xp[0] = f32x4{xs[0], xs[1], xs[2], xs[3]}; xp[1] = f32x4{xs[4], xs[5], xs[6], xs[7]};
      FENCE();
    }
    tag = __builtin_amdgcn_readfirstlane(epoch) + 1u;
    asm volatile("" :: "v"(cpf));
    {
      float sacc[8] = {0.f, 0.f, 0.f, 0.f, 0.f, 0.f, 0.f, 0.f};
#pragma unroll
      for (int i = 0; i < 7; ++i) fma8(sacc, *reinterpret_cast<const float*>(lds + L_XN + (wrow + wstep * i) * 4), wv[i]);
#pragma unroll
      for (int i = 0; i < 8; ++i) { float t = sacc[i]; const float r = dpp<ROR4>(t); t += qwg ? r : 0.f; t += dpp<XOR8>(t); sacc[i] = x32_sum(x16_sum(t)); }
      if (lane < 8) {
        f32x4* p = reinterpret_cast<f32x4*>(lds + L_RED + wave * 256 + lane * 32);
        p[0] = f32x4{sacc[0], sacc[1], sacc[2], sacc[3]}; p[1] = f32x4{sacc[4], sacc[5], sacc[6], sacc[7]};
      }
      lds_arrive(lds, C_PART, lane);
      if (wave == 0) {
        lds_wait_ge(lds, C_PART, 4u);
        if (qwg) {
          float q0 = 0.f, q1 = 0.f;
#pragma unroll
          for (int w = 0; w < 4; ++w) { const f32x2 t = *reinterpret_cast<const f32x2*>(lds + L_RED + w * 256 + c * 8); q0 += t[0]; q1 += t[1]; }
          __builtin_amdgcn_raw_buffer_store_b64(u32x2{pk_bf16(q0, q1), tag}, r_ws, lane < 16 ? lane * 8 : OOB, WS_QG + (h * 16 + qsl) * LSTR, AUX_SC1);
        } else {
          const int o = ((lane >> 2) & 1) * 256 + (lane >> 3) * 32 + (lane & 3) * 8;
          const f32x2 t0 = *reinterpret_cast<const f32x2*>(lds + L_RED + o), t1 = *reinterpret_cast<const f32x2*>(lds + L_RED + 512 + o);
          __builtin_amdgcn_raw_buffer_store_b128(u32x4{__float_as_uint(t0[0] + t1[0]), tag, __float_as_uint(t0[1] + t1[1]), tag}, r_ws,
                                                 fb < 112 ? (lane >> 3) * 512 + (lane & 7) * 16 : OOB, WS_GU + (fb >> 2) * 4096 + (fb & 3) * 128, AUX_SC1);
        }
        STAMP(1);
      }
    }
    const bool ffn = wg < FF / 8;
    if (wave == 0) {
      u32x4 qg[2];
      const unsigned qo[2] = {(lane >> 2) * LSTR + (lane & 3) * 32, (lane >> 2) * LSTR + (lane & 3) * 32 + 16};
      sleep_until<1>(t_start, Q_FIRST);
      timed_out = !gather<2, Q_SLEEP>(qg, r_ws, qo, WS_QG + h * 16 * LSTR, tag);
      *reinterpret_cast<u32x4*>(lds + L_Q + lane * 16) = u32x4{qg[0][0], qg[0][2], qg[1][0], qg[1][2]};
      if (lane == 0) *reinterpret_cast<unsigned*>(lds + C_TAG) = tag;
      lds_arrive(lds, C_QRDY, lane);
    }
    if (wave != 0) {
      __builtin_amdgcn_s_setprio(HK_PRIO);
      const int slot = NI + wave - 1;
      u32x4 kt[4][8];
#pragma unroll
      for (int i = 0; i < 4; ++i) { chain_wait(lds, 4 * slot + i, t_start); issue<AUX_K>(kt[i], r_k, vw0, vw1, wg_off + OFF_SLOT(slot) + OFF_W(i)); }
      lds_wait_ge(lds, C_QRDY, 1u);
      u32x4 qf[2];
      load_qf(qf, lds, 1, c, g);
#pragma unroll
      for (int i = 0; i < 4; ++i) {
        f32x4 d[4];
        qk_mfma(kt[i], qf, d, false);
        *reinterpret_cast<unsigned short*>(lds + L_P + i * 2048 + slot * 64 + sc_lane) = qk_score(d, b0, b1);
        PROG();
      }
      lds_arrive(lds, C_KHELP, lane);
      lds_add(lds, C_DONE, 4u, lane);
      __builtin_amdgcn_s_setprio(3);
    }
    STAMP(3);
    {
      if (wave == 3) {
        sleep_until<32>(t_start, GU_AT);
        u32x4 gu[1];
        const unsigned go[1] = {(ffn && lane < 32) ? (unsigned)lane * 16 : OOB};
        if (!gather<1, 64>(gu, r_ws, go, WS_GU + wg * 512, tag)) timed_out = true;
        float g0 = __uint_as_float(gu[0][0]), g1 = __uint_as_float(gu[0][2]);
        g0 += dpp<XOR8>(g0); g1 += dpp<XOR8>(g1);
        g0 = rnd_bf16(x16_sum(g0)); g1 = rnd_bf16(x16_sum(g1));
        const float u0 = dpp<ROR4>(g0), u1 = dpp<ROR4>(g1);
        const float h0 = rnd_bf16((g0 > 0.f ? g0 : g0 * 0.f) * u0), h1 = rnd_bf16((g1 > 0.f ? g1 : g1 * 0.f) * u1);
        if (lane < 4) *reinterpret_cast<f32x2*>(lds + L_H + lane * 8) = f32x2{timed_out ? __builtin_nanf("") : h0, h1};
      }
      if (wave == 2) {
        lds_wait_ge<2>(lds, C_DONE, E_AT);
        const unsigned tb = now() - (((lds_ld(lds, C_DONE) & 0xffffu) * E_TAU) >> 8);
        const bool smp = (s & 7) == ((h + (s >> 3) * 4) & 7) && s < 16;
        __builtin_amdgcn_raw_buffer_store_b128(u32x4{tb, tag, tb, tag}, r_ws, (smp && lane < 8) ? lane * 16 : OOB, WS_TT + (8 + 2 * h + (s >> 3)) * LSTR, AUX_SC1);
        lds_wait_ge<8>(lds, C_DONE, E_AT + E_GAP);
        const u32x4 y = __builtin_amdgcn_raw_buffer_load_b128(r_ws, lane < 16 ? lane * LSTR : OOB, WS_TT + 8 * LSTR, AUX_SC1);
        const bool oy = lane < 16 && y[1] == tag && y[3] == tag;
        if (__builtin_amdgcn_ballot_w64(oy) == 0xffffu) {
          float dl = oy ? (float)(int)(y[0] - tb) : 0.f;
          dl += dpp<XOR1>(dl); dl += dpp<XOR2>(dl); dl += dpp<XOR7>(dl); dl += dpp<XOR15>(dl);
          if (lane == 0) *reinterpret_cast<u32x2*>(lds + C_BASE) = u32x2{tb + (unsigned)(int)(dl * 0.0625f + (float)E_OFF), (unsigned)E_TAU};
        }
      }
      if (wave == 1) { lds_wait_ge<2>(lds, C_DONE, CP_REF); t_ref = now(); n_ref = lds_ld(lds, C_DONE) & 0xffffu; }
    }
    STAMP(11);
    if (wave == 1) {
#pragma nounroll
      for (unsigned spin = 0; spin < SPIN_CAP * 8u && (lds_ld(lds, C_DONE) & 0xffffu) < CP_AT; ++spin) __builtin_amdgcn_s_sleep(2);
      const unsigned t_at = now(), n_cp = lds_ld(lds, C_DONE) & 0xffffu;
      const float tau = (float)(t_at - t_ref) / (float)(n_cp - n_ref);
      const unsigned t_cp = t_at - (unsigned)(tau * (float)(n_cp - CP_AT));
      lds_wait_ge<4>(lds, C_DONE, 0x10000u * 4u);
      const unsigned t_now = now();
      const int hd = lane & 1;
      float mw[4], lw[4];
#pragma unroll
      for (int w = 0; w < 4; ++w) { const f32x2 t = *reinterpret_cast<const f32x2*>(lds + L_ML + w * 16 + hd * 8); mw[w] = t[0]; lw[w] = t[1]; }
      const float M = fmaxf(fmaxf(mw[0], mw[1]), fmaxf(mw[2], mw[3]));
      float L = 0.f;
#pragma unroll
      for (int w = 0; w < 4; ++w) L = fmaf(__builtin_amdgcn_exp2f((mw[w] - M) * C2), lw[w], L);
      __builtin_amdgcn_raw_buffer_store_b128(u32x4{(lane & 2) ? t_cp : __float_as_uint(M), tag, __float_as_uint((lane & 2) ? tau : L), tag}, r_ws,
                                             lane < 8 ? lane * 16 : OOB, WS_ML + wg * LSTR, AUX_SC1);
      sleep_until<16>(t_now, CP_WAIT);
      u32x4 ml[2];
      const unsigned mo[2] = {(lane >> 2) * LSTR + (lane & 3) * 16, (16 + (lane >> 2)) * LSTR + (lane & 3) * 16};
      if (!gather<2, 32>(ml, r_ws, mo, WS_ML + h * 32 * LSTR, tag)) timed_out = true;
      const float Ma = __uint_as_float(ml[0][0]), Mb = __uint_as_float(ml[1][0]);
      float Mg = fmaxf(Ma, Mb);
      Mg = fmaxf(Mg, dpp<ROR4>(Mg)); Mg = fmaxf(Mg, dpp<XOR8>(Mg)); Mg = x32_max(x16_max(Mg));
      float Lg = __builtin_amdgcn_exp2f((Ma - Mg) * C2) * __uint_as_float(ml[0][2]) + __builtin_amdgcn_exp2f((Mb - Mg) * C2) * __uint_as_float(ml[1][2]);
      if (lane & 2) Lg = (lane & 1) ? __uint_as_float(ml[0][2]) + __uint_as_float(ml[1][2]) : (float)(int)(ml[0][0] - t_cp) + (float)(int)(ml[1][0] - t_cp);
      Lg += dpp<ROR4>(Lg); Lg += dpp<XOR8>(Lg); Lg = x32_sum(x16_sum(Lg));
      if (lane < 2) {
#pragma unroll
        for (int w = 0; w < 4; ++w) *reinterpret_cast<float*>(lds + L_SC + w * 8 + lane * 4) = timed_out ? __builtin_nanf("") : __builtin_amdgcn_exp2f((mw[w] - Mg) * C2) / Lg;
      }
      {
        const float tm = dpp<XOR1>(Lg) * (CP_TAU / 32);
        const u32x2 tt = {t_cp + (unsigned)(int)(Lg * (1.0f / 32) + (float)CP_OFF - tm * (float)CP_AT), (unsigned)(tm * 256.f)};
        if (lane == 2 && !timed_out) *reinterpret_cast<u32x2*>(lds + C_BASE) = tt;
        const unsigned b2 = __builtin_amdgcn_readlane(tt[0], 2), u2 = __builtin_amdgcn_readlane(tt[1], 2);
        __builtin_amdgcn_raw_buffer_store_b128(u32x4{b2, tag, u2, tag}, r_ws, (s == 0 && lane < 8) ? lane * 16 : OOB, WS_TT + h * LSTR, AUX_SC1);
        sleep_until<16>(now(), COMMON_TT);
        const u32x4 x = __builtin_amdgcn_raw_buffer_load_b128(r_ws, lane < 8 ? lane * LSTR : OOB, WS_TT, AUX_SC1);
        const bool ok = lane < 8 && x[1] == tag && x[3] == tag;
        if (__builtin_amdgcn_ballot_w64(ok) == 0xffu) {
          float db = ok ? (float)(int)(x[0] - b2) : 0.f, su = ok ? (float)x[2] : 0.f;
          db += dpp<XOR1>(db); su += dpp<XOR1>(su); db += dpp<XOR2>(db); su += dpp<XOR2>(su); db += dpp<XOR7>(db); su += dpp<XOR7>(su);
          if (lane == 0) *reinterpret_cast<u32x2*>(lds + C_BASE) = u32x2{b2 + (unsigned)(int)(db * 0.125f), (unsigned)(su * 0.125f)};
        }
      }
      STAMP(14);
    }
    if (wave != 0) {
      lds_barrier();
    } else {
      lds_wait_ge<4>(lds, C_DONE, 0x10000u * 4u + ROWS_AT);
      const rsrc_t r_wo = mk_rsrc(a.wo, NQ * D * 2), r_d = mk_rsrc(a.wd, ffn ? FF * D * 2 : 0);
      const unsigned coff = lane < D / 8 ? lane * 16 : OOB;
      u32x4 dr[8], wr[16];
#pragma unroll
      for (int j = 0; j < 8; ++j) dr[j] = __builtin_amdgcn_raw_buffer_load_b128(r_d, coff, (wg * 8 + j) * (D * 2), AUX_NT);
#pragma unroll
      for (int j = 0; j < 16; ++j) wr[j] = __builtin_amdgcn_raw_buffer_load_b128(r_wo, coff, (wg * 16 + j) * (D * 2), AUX_NT);
      SB();
      lds_barrier();
      float y[8] = {0.f, 0.f, 0.f, 0.f, 0.f, 0.f, 0.f, 0.f};
#pragma unroll
      for (int j = 0; j < 8; ++j) fma8(y, *reinterpret_cast<const float*>(lds + L_H + j * 4), dr[j]);
#pragma unroll
      for (int j = 0; j < 8; ++j) {
        const u32x4 lo = wr[2 * j], hi = wr[2 * j + 1];
#pragma unroll
        for (int i = 0; i < 4; ++i) { wr[2 * j][i] = __builtin_amdgcn_perm(hi[i], lo[i], 0x05040100u); wr[2 * j + 1][i] = __builtin_amdgcn_perm(hi[i], lo[i], 0x07060302u); }
      }
      asm volatile("" : "+v"(y[0]), "+v"(y[1]), "+v"(y[2]), "+v"(y[3]), "+v"(y[4]), "+v"(y[5]), "+v"(y[6]), "+v"(y[7]), "+v"(wr[0]), "+v"(wr[1]), "+v"(wr[2]), "+v"(wr[3]),
                        "+v"(wr[4]), "+v"(wr[5]), "+v"(wr[6]), "+v"(wr[7]), "+v"(wr[8]), "+v"(wr[9]), "+v"(wr[10]), "+v"(wr[11]), "+v"(wr[12]), "+v"(wr[13]),
                        "+v"(wr[14]), "+v"(wr[15]));
      STAMP(13);
      u32x4 po[4];
      const unsigned oo[4] = {(unsigned)lane * 16, (unsigned)lane * 16 + 1024, (unsigned)lane * 16 + 2048, (unsigned)lane * 16 + 3072};
      {
        const unsigned due = lds_ld(lds, C_BASE) + lds_ld(lds, C_BASE + 4) + (unsigned)(H1_DUE);
#pragma nounroll
        for (int spin = 0; spin < 128 && (int)(now() - due) < 0; ++spin) __builtin_amdgcn_s_sleep(1);
      }
      if (!gather<4, S1>(po, r_ws, oo, WS_PO + wg * 4096, tag)) timed_out = true;
      STAMP(7);
      float o0 = (__uint_as_float(po[0][0]) + __uint_as_float(po[1][0])) + (__uint_as_float(po[2][0]) + __uint_as_float(po[3][0]));
      float o1 = (__uint_as_float(po[0][2]) + __uint_as_float(po[1][2])) + (__uint_as_float(po[2][2]) + __uint_as_float(po[3][2]));
      o0 += dpp<XOR8>(o0); o1 += dpp<XOR8>(o1);
      o0 = x32_sum(x16_sum(o0)); o1 = x32_sum(x16_sum(o1));
      if (timed_out) o0 = __builtin_nanf("");
      const unsigned opk = pk_bf16(o0, o1);
      unsigned oj[8];
#pragma unroll
      for (int j = 0; j < 8; ++j) oj[j] = __builtin_amdgcn_readlane(opk, j);
      const unsigned yoff = lane < D / 8 ? (unsigned)(lane >> 2) * (4 * 256 * CPR * 8) + (lane & 3) * 16 : OOB;
#pragma unroll
      for (int k = 0; k < 4; ++k) {
#pragma unroll
        for (int j = 0; j < 8; ++j) { y[2 * k] = dot2(wr[2 * j][k], oj[j], y[2 * k]); y[2 * k + 1] = dot2(wr[2 * j + 1][k], oj[j], y[2 * k + 1]); }
        __builtin_amdgcn_raw_buffer_store_b128(u32x4{__float_as_uint(y[2 * k]), tag, __float_as_uint(y[2 * k + 1]), tag}, r_ws, yoff,
                                               WS_YA + k * (256 * CPR * 8) + wg * (CPR * 8), AUX_SC1);
      }
      STAMP(8);
      lds_arrive(lds, C_HOP1, lane);
    }
  } else {
    const int w = wave - 4;
    const unsigned t0 = wg_off + OFF_W(wave);
    LANE_VARS;
    SCORE_VARS;
    STAMP0();
    const rsrc_t r_v = mk_rsrc(a.v, 8u * SK * DH * 2);
    const unsigned vv0 = OFF_G(g) + piece(0, c >> 2) * 64 + (c & 3) * 16, vv1 = OFF_G(g) + piece(1, c >> 2) * 64 + (c & 3) * 16;
    u32x4 rx[NI - 2][8];
    STAMP(13);
    static_assert(CH_W >= 5, "");
    lds_barrier();
    if (!CH_TIMED) { asm volatile("s_waitcnt vmcnt(8)" ::: "memory"); lds_arrive(lds, C_LANDED, lane); PROGL(); }
#define CHAIN_STEP(R, SLOT) do { \
      constexpr bool first_ = CH_W < 5;     \
      if (first_ && lds_ld(lds, C_QRDY) == 0u) { asm volatile("s_waitcnt vmcnt(0)" ::: "memory"); lds_arrive(lds, C_LANDED, lane); PROGL(); } \
      chain_wait(lds, 4 * (SLOT) + w, t_start); \
      issue<AUX_K>(R, r_k, vw0, vw1, t0 + OFF_SLOT(SLOT)); \
      if (!CH_TIMED && !first_ && lds_ld(lds, C_QRDY) == 0u) { asm volatile("s_waitcnt vmcnt(8)" ::: "memory"); lds_arrive(lds, C_LANDED, lane); PROGL(); } } while (0)
    STAMP(15);
#pragma unroll
    for (int i = 2; i < NI; ++i) CHAIN_STEP(rx[i - 2], i);
#undef CHAIN_STEP
    if (!CH_TIMED && lds_ld(lds, C_QRDY) == 0u) { asm volatile("s_waitcnt vmcnt(0)" ::: "memory"); lds_arrive(lds, C_LANDED, lane); PROGL(); }
    lds_wait_ge(lds, C_QRDY, 1u);
    tag = lds_ld(lds, C_TAG);
    STAMP(2);
    u32x4 qf[2];
    load_qf(qf, lds, 1, c, g);
    unsigned char* const pl = lds + L_P + w * 2048;
    auto pace = [&](unsigned level) -> int {
      u32x4 cw;
#pragma nounroll
      for (unsigned spin = 0; spin < SPIN_CAP; ++spin) {
        FENCE();
        cw = *reinterpret_cast<const u32x4*>(lds + C_DONE);
        if ((__builtin_amdgcn_readfirstlane(cw[0]) & 0xffffu) + SLACK >= level) break;
        __builtin_amdgcn_s_sleep(2);
      }
      const unsigned done = __builtin_amdgcn_readfirstlane(cw[0]) & 0xffffu, tau = __builtin_amdgcn_readfirstlane(cw[3]);
      const int lag = (int)(now() - __builtin_amdgcn_readfirstlane(cw[2]) - ((done * tau) >> 8));
      return tau == 0u ? 0 : lag > w * CP_STEP ? 1 : lag < -CP_AHEAD - w * CP_STEPS ? 2 : 0;
    };
    {
      f32x4 d[4];
      unsigned short* sw = reinterpret_cast<unsigned short*>(pl + sc_lane);
      unsigned tp = OFF_SLOT(NI + 3);
#define K_STEP(R, SW, ISSUE_NEXT, SLOT) do { qk_mfma(R, qf, d, SW); PROG(); ISSUE_NEXT; sw[(SLOT) * 32] = qk_score(d, b0, b1); } while (0)
      bool ks = g == 3;
      K_STEP(ra, false, issue<AUX_K>(ra, r_k, ks ? vw1 : vw0, ks ? vw0 : vw1, t0 + tp), 0);
      K_STEP(rb, false, issue<AUX_K>(rb, r_k, ks ? vw1 : vw0, ks ? vw0 : vw1, t0 + tp + SLOT1), 1);
#pragma unroll
      for (int i = 2; i < NI; ++i) K_STEP(rx[i - 2], false, (void)0, i);
      lds_add(lds, C_DONE, NI, lane);
      sw += (NI + 3) * 32;
      for (int it = 1; it < (29 - NI) / 2; ++it) {
        tp = OFF_SLOT(NI + 3 + 2 * it);
        const int nk = pace(it >= KSLACK_FROM ? 4 * NI + 4 + 8 * it : 0);
        const bool kn = (g & (nk == 0 ? 3 : nk == 2 ? 1 : 0)) == (nk == 0 ? 3 : 1);
        const unsigned n0 = kn ? vw1 : vw0, n1 = kn ? vw0 : vw1;
        K_STEP(ra, ks, issue<AUX_K>(ra, r_k, n0, n1, t0 + tp), 0);
        K_STEP(rb, ks, issue<AUX_K>(rb, r_k, n0, n1, t0 + tp + SLOT1), 1);
        ks = kn;
        lds_add(lds, C_DONE, 2u, lane);
        sw += 2 * 32;
      }
      K_STEP(ra, ks, issue<AUX_V>(ra, r_v, vv0, vv1, t0), 0);
      K_STEP(rb, ks, issue<AUX_V>(rb, r_v, vv0, vv1, t0 + SLOT1), 1);
#undef K_STEP
    }
    STAMP(12);
    lds_wait_ge(lds, C_KHELP, 3u);
    {
      u32x4* pp = reinterpret_cast<u32x4*>(pl + lane * 32);
      u32x4 t[2] = {pp[0], pp[1]};
      float mx = bf_lo(t[0][0]);
#pragma unroll
      for (int j = 0; j < 2; ++j)
#pragma unroll
        for (int i = 0; i < 4; ++i) mx = fmaxf(mx, fmaxf(bf_lo(t[j][i]), bf_hi(t[j][i])));
      mx = parity_max(mx);
      const float mc = mx * C2;
      float lp = 0.f;
#pragma unroll
      for (int j = 0; j < 2; ++j) {
#pragma unroll
        for (int i = 0; i < 4; ++i) {
          const float p0 = __builtin_amdgcn_exp2f(fmaf(bf_lo(t[j][i]), C2, -mc)), p1 = __builtin_amdgcn_exp2f(fmaf(bf_hi(t[j][i]), C2, -mc));
          lp += p0 + p1; t[j][i] = pk_bf16(p0, p1);
        }
        pp[j] = t[j];
      }
      lp = parity_sum(lp);
      if (lane < 2) *reinterpret_cast<f32x2*>(lds + L_ML + w * 16 + lane * 8) = f32x2{mx, lp};
      asm volatile("s_waitcnt lgkmcnt(0)" ::: "memory");
      lds_add(lds, C_DONE, 0x10002u, lane);
    }
    f32x4 acc0 = {0.f, 0.f, 0.f, 0.f}, acc1 = {0.f, 0.f, 0.f, 0.f};
    {
      const int msel = c >> 1;
      const unsigned char* pr = pl + (c & 1) * 32 + g * 8;
      const unsigned vb = OFF_G(g) + (c & 3) * 16;
      float* const rec = reinterpret_cast<float*>(lds + L_ACC + w * 2048) + 8 * (c & 3) + g;
      auto put = [&](int m) {
        const int e0 = piece_m(m, 0, c >> 2) * 32, e1 = piece_m(m, 1, c >> 2) * 32;
#pragma unroll
        for (int k = 0; k < 4; ++k) { float* r = rec + (k & 1) * 256 + ((k >> 1) ? e1 : e0); r[0] = acc0[k]; r[4] = acc1[k]; }
      };
      auto get = [&](int m) {
        const int e0 = piece_m(m, 0, c >> 2) * 32, e1 = piece_m(m, 1, c >> 2) * 32;
#pragma unroll
        for (int k = 0; k < 4; ++k) { const float* r = rec + (k & 1) * 256 + ((k >> 1) ? e1 : e0); acc0[k] = r[0]; acc1[k] = r[4]; }
      };
      int cur = 0;
#define V_STEP(R, ISSUE_NEXT) do { const u32x2 pk_ = *reinterpret_cast<const u32x2*>(pr); pr += 64; pv_mfma(R, pk_, msel, acc0, acc1); PROG(); ISSUE_NEXT; } while (0)
      for (int it = 0; it < 15; ++it) {
        const int nxt = pace(128 + 8 * it);
        const unsigned tp = OFF_SLOT(2 + 2 * it);
        const unsigned n0 = vb + piece_m(nxt, 0, c >> 2) * 64, n1 = vb + piece_m(nxt, 1, c >> 2) * 64;
        V_STEP(ra, issue<AUX_V>(ra, r_v, n0, n1, t0 + tp));
        V_STEP(rb, issue<AUX_V>(rb, r_v, n0, n1, t0 + tp + SLOT1));
        lds_add(lds, C_DONE, 2u, lane);
        if (nxt != cur) { put(cur); FENCE(); get(nxt); cur = nxt; }
        FENCE();
      }
      V_STEP(ra, lds_add(lds, C_DONE, 1u, lane));
      V_STEP(rb, (void)0);
#undef V_STEP
      STAMP(3);
      put(cur);
      asm volatile("" :: "v"(cpf));
    }
    STAMP(4);
    lds_barrier();
    STAMP(5);
    {
      const int u = tid - 256;
      float o0 = 0.f, o1 = 0.f;
#pragma unroll
      for (int k = 0; k < 4; ++k) {
        const float sc = *reinterpret_cast<const float*>(lds + L_SC + k * 8 + (u >> 7) * 4);
        const f32x2 t = *reinterpret_cast<const f32x2*>(lds + L_ACC + k * 2048 + u * 8);
        o0 = fmaf(sc, t[0], o0); o1 = fmaf(sc, t[1], o1);
      }
      __builtin_amdgcn_raw_buffer_store_b128(u32x4{__float_as_uint(o0), tag, __float_as_uint(o1), tag}, r_ws, (u >> 3) * 4096 + (u & 7) * 16,
                                             WS_PO + h * (32 * 4096) + s * 128, AUX_SC1);
    }
    STAMP(6);
  }
  LANE_VARS;
  PROG_OUT();
  if (wg >= NRED) return;
  if (wave >= 4) {
    const unsigned t = __builtin_amdgcn_raw_buffer_load_b32(r_ws, lane < CPR * 4 ? ((wave - 4) * CPR * 4 + lane) * 128 : OOB, WS_YA + wg * (256 * CPR * 8), AUX_SC1);
    asm volatile("" :: "v"(t));
  }
  lds_wait_ge<H2S>(lds, C_HOP1, 1u);
  if (H2_PRE > 0) __builtin_amdgcn_s_sleep(H2_PRE);
  {
    constexpr int NLD = (256 * CPR / 2 + NTHR - 1) / NTHR;
    static_assert(CPR == 8, "");
    const int col = (wg >> 2) * 32 + ((tid >> 1) & 3) * 8 + (wg & 3) * 2 + (tid & 1);
    const float xo = tid < CPR ? bf_lo(a.x[col]) : 0.f;
    u32x4 t[NLD];
    unsigned yo[NLD];
#pragma unroll
    for (int k = 0; k < NLD; ++k) yo[k] = tid + NTHR * k < 256 * CPR / 2 ? (unsigned)(tid + NTHR * k) * 16 : OOB;
    if (!gather<NLD, 0>(t, r_ws, yo, WS_YA + wg * (256 * CPR * 8), tag)) timed_out = true;
    STAMP(9);
    float s0 = 0.f, s1 = 0.f;
#pragma unroll
    for (int k = 0; k < NLD; ++k) { s0 += __uint_as_float(t[k][0]); s1 += __uint_as_float(t[k][2]); }
    if (CPR / 2 <= 8) { s0 += dpp<XOR8>(s0); s1 += dpp<XOR8>(s1); }
    if (CPR / 2 <= 4) { s0 += dpp<ROR4>(s0); s1 += dpp<ROR4>(s1); }
    if (CPR / 2 <= 2) { s0 += dpp<XOR2>(s0); s1 += dpp<XOR2>(s1); }
    if (CPR / 2 <= 1) { s0 += dpp<XOR1>(s0); s1 += dpp<XOR1>(s1); }
    s0 = x32_sum(x16_sum(s0)); s1 = x32_sum(x16_sum(s1));
    if (timed_out) s0 = __builtin_nanf("");
    if (lane < CPR / 2) *reinterpret_cast<f32x2*>(lds + L_YA + wave * 64 + lane * 8) = f32x2{s0, s1};
    lds_barrier();
    if (tid < CPR) {
      float r = xo;
#pragma unroll
      for (int w = 0; w < 8; ++w) r += *reinterpret_cast<const float*>(lds + L_YA + w * 64 + tid * 4);
#define AUX_OUT 17
      __builtin_amdgcn_raw_buffer_store_b16((unsigned short)(pk_bf16(r, r) & 0xffffu), mk_rsrc(a.out, D * 2), col * 2, 0, AUX_OUT);
    }
    if (wg == 0 && tid == 64) __builtin_amdgcn_raw_buffer_store_b32(tag, r_ws, 0, WS_EPOCH, AUX_SC1);
    STAMP(10);
    if (wg == PROBE_WG && wave == 0) {
      const unsigned ts_ = lds_ld(lds, 40960), tx_ = lds_ld(lds, 40960 + (PROBE_WAVE * 16 + PROBE) * 4);
      sleep_until<1>(ts_, 10000u + 10u * (tx_ - ts_ - PROBE_SUB));
    }
  }
}

}

extern "C" void kernel_launch(void* const* d_in, const int* in_sizes, int n_in, void* d_out, int out_size, void* d_ws, size_t ws_size,
                              hipStream_t stream) {
  (void)in_sizes; (void)n_in; (void)out_size; (void)ws_size;
  Args a;
  a.x = (const unsigned short*)d_in[0];
  a.k = (const unsigned char*)d_in[1];
  a.v = (const unsigned char*)d_in[2];
  a.wq = (const unsigned char*)d_in[3];
  a.wo = (const unsigned char*)d_in[4];
  a.wg = (const unsigned char*)d_in[6];
  a.wu = (const unsigned char*)d_in[7];
  a.wd = (const unsigned char*)d_in[8];
  a.out = (unsigned short*)d_out;
  a.ws = (unsigned char*)d_ws;
  (void)hipFuncSetAttribute((const void*)fused_decode, hipFuncAttributeMaxDynamicSharedMemorySize, LDS_BYTES);
  hipLaunchKernelGGL(fused_decode, dim3(NWG), dim3(NTHR), LDS_BYTES, stream, a);
}
```

```cpp
#include <hip/hip_runtime.h>
#include <hip/hip_bf16.h>
#include <cstdint>

namespace {

typedef unsigned u32x4 __attribute__((ext_vector_type(4)));
typedef unsigned u32x2 __attribute__((ext_vector_type(2)));
typedef float f32x4 __attribute__((ext_vector_type(4)));
typedef float f32x2 __attribute__((ext_vector_type(2)));
typedef __bf16 bf16x8 __attribute__((ext_vector_type(8)));
typedef __bf16 bf16x2 __attribute__((ext_vector_type(2)));
typedef __amdgpu_buffer_rsrc_t rsrc_t;

constexpr int D = 448, SK = 65536, DH = 256, FF = 1792, NQ = 4096;
constexpr int NWG = 256, NTHR = 512;
constexpr int NRED = 56;
constexpr int NI = 5;
constexpr unsigned OOB = 0x40000000u;
constexpr float C2 = 0.0625f * 1.4426950408889634f;
constexpr unsigned POLL_CAP = 1u << 20, SPIN_CAP = 1u << 26;

constexpr int CH_T0 = 85, CH_TAU = 29;
constexpr int Q_FIRST = 870, GU_AT = 1500, ML_WAIT = 600;
constexpr int TT_N = 4, TT_AT = 32, TT_PER = 44, TT_GAP = 18;
constexpr int TT_TAU = 6915, TT_OFF0 = -60, TT_OFF = -40;
constexpr int STEP_FAST = 20, AHEAD = -50, STEP_SLOW = 70;
constexpr int SLACK = 3;
constexpr int ROWS_AT = 251, H1_DUE = 0, H2_PRE = 10;

constexpr unsigned KCOL[19] =
    {0x40, 0x10, 0x100, 0x8, 0x9000, 0x8001, 0x2083, 0x6803, 0x6205, 0xb880, 0x8ea5, 0x9627, 0xec27, 0x6aa5, 0x81, 0x1201, 0x16a85, 0x290a0, 0x49200};
template <int FIRST, int N> __host__ __device__ constexpr unsigned keymap(unsigned v) {
  unsigned o = 0;
  for (int i = 0; i < N; ++i) o ^= ((v >> i) & 1u) ? KCOL[FIRST + i] << 9 : 0u;
  return o;
}
constexpr bool keymap_ok() {
  unsigned m[19] = {}, lanebits = 0;
  for (int i = 0; i < 4; ++i) lanebits |= KCOL[i];
  for (int i = 0; i < 19; ++i) {
    m[i] = KCOL[i];
    if (i < 4 ? (KCOL[i] & (KCOL[i] - 1)) != 0 : (KCOL[i] & lanebits) != 0) return false;
    if ((KCOL[i] >> 16) != (i < 16 ? 0u : 1u << (i - 16))) return false;
  }
  for (int b = 0; b < 19; ++b) {
    int piv = -1;
    for (int i = b; i < 19; ++i) if ((m[i] >> b) & 1u) piv = i;
    if (piv < 0) return false;
    const unsigned t = m[piv]; m[piv] = m[b]; m[b] = t;
    for (int i = 0; i < 19; ++i) if (i != b && ((m[i] >> b) & 1u)) m[i] ^= t;
  }
  return true;
}
static_assert(keymap_ok(), "the key map is a bijection");
#define OFF_J(j) keymap<0, 2>(j)
#define OFF_G(G) keymap<2, 2>(G)
#define OFF_W(w) keymap<4, 2>(w)
#define OFF_SLOT(t) keymap<6, 5>(t)
#define OFF_WG(wg) keymap<11, 8>(wg)
constexpr unsigned SLOT1 = OFF_SLOT(1);

constexpr unsigned WS_EPOCH = 0;
constexpr unsigned LSTR = 4352;
constexpr unsigned WS_PO = 4096;
constexpr unsigned WS_YA = WS_PO + 8 * 32 * 32 * 16 * 8;
constexpr unsigned WS_GU = WS_YA + 256 * D * 8;
constexpr unsigned WS_QG = WS_GU + 224 * 4 * 128;
constexpr unsigned WS_ML = WS_QG + 128 * LSTR;
constexpr unsigned WS_TT = WS_ML + 256 * LSTR;
constexpr unsigned WS_END = WS_TT + TT_N * 16 * LSTR;

constexpr int L_RED = 0;
constexpr int L_CTL = 1024;
constexpr int C_PART = L_CTL, C_QRDY = L_CTL + 4, C_TAG = L_CTL + 8, C_KHELP = L_CTL + 12, C_MID = L_CTL + 16, C_HOP1 = L_CTL + 20,
              C_DONE = L_CTL + 24, C_BASE = L_CTL + 28;
constexpr int L_ML = 1088;
constexpr int L_SC = 1152;
constexpr int L_H = 1184;
constexpr int L_YA = 1216;
constexpr int L_Q = 1536;
constexpr int L_XN = 2560;
constexpr int L_ACC = 4608;
constexpr int L_P = L_ACC + 4 * 2048;
constexpr int LDS_BYTES = L_P + 4 * 2048;

constexpr int AUX_NT = 2, AUX_SC1 = 16, AUX_SYS = 17;
#define SB() __builtin_amdgcn_sched_barrier(0)
#define FENCE() asm volatile("" ::: "memory")

__device__ __forceinline__ float bf_lo(unsigned u) { return __builtin_bit_cast(float, u << 16); }
__device__ __forceinline__ float bf_hi(unsigned u) { return __builtin_bit_cast(float, u & 0xffff0000u); }
__device__ __forceinline__ unsigned pk_bf16(float lo, float hi) {
  bf16x2 v = {(__bf16)lo, (__bf16)hi};
  return __builtin_bit_cast(unsigned, v);
}
__device__ __forceinline__ float rnd_bf16(float x) { return (float)(__bf16)x; }
__device__ __forceinline__ float dot2(unsigned a, unsigned b, float acc) {
  return __builtin_amdgcn_fdot2_f32_bf16(__builtin_bit_cast(bf16x2, a), __builtin_bit_cast(bf16x2, b), acc, false);
}
template <int CTRL> __device__ __forceinline__ float dpp(float x) {
  return __builtin_bit_cast(float, __builtin_amdgcn_mov_dpp(__builtin_bit_cast(int, x), CTRL, 0xf, 0xf, true));
}
constexpr int XOR1 = 0xB1, XOR2 = 0x4E, XOR7 = 0x141, XOR15 = 0x140, XOR8 = 0x128, ROR4 = 0x124;
__device__ __forceinline__ float x16_max(float x) {
  auto s = __builtin_amdgcn_permlane16_swap(__float_as_uint(x), __float_as_uint(x), false, false);
  return fmaxf(__uint_as_float(s[0]), __uint_as_float(s[1]));
}
__device__ __forceinline__ float x32_max(float x) {
  auto s = __builtin_amdgcn_permlane32_swap(__float_as_uint(x), __float_as_uint(x), false, false);
  return fmaxf(__uint_as_float(s[0]), __uint_as_float(s[1]));
}
__device__ __forceinline__ float x16_sum(float x) {
  auto s = __builtin_amdgcn_permlane16_swap(__float_as_uint(x), __float_as_uint(x), false, false);
  return __uint_as_float(s[0]) + __uint_as_float(s[1]);
}
__device__ __forceinline__ float x32_sum(float x) {
  auto s = __builtin_amdgcn_permlane32_swap(__float_as_uint(x), __float_as_uint(x), false, false);
  return __uint_as_float(s[0]) + __uint_as_float(s[1]);
}
__device__ __forceinline__ float row_sum(float x) { x += dpp<XOR1>(x); x += dpp<XOR2>(x); x += dpp<XOR7>(x); x += dpp<XOR15>(x); return x; }
__device__ __forceinline__ float wave_sum(float x) { return x32_sum(x16_sum(row_sum(x))); }
__device__ __forceinline__ float parity_sum(float x) {
  x += dpp<XOR2>(x); x += dpp<ROR4>(x); x += dpp<XOR8>(x);
  return x32_sum(x16_sum(x));
}
__device__ __forceinline__ float parity_max(float x) {
  x = fmaxf(x, dpp<XOR2>(x)); x = fmaxf(x, dpp<ROR4>(x)); x = fmaxf(x, dpp<XOR8>(x));
  return x32_max(x16_max(x));
}
__device__ __forceinline__ void lds_barrier() {
  asm volatile("s_waitcnt lgkmcnt(0)" ::: "memory");
  __builtin_amdgcn_s_barrier();
  FENCE();
}
__device__ __forceinline__ rsrc_t mk_rsrc(const void* p, unsigned bytes) {
  return __builtin_amdgcn_make_buffer_rsrc(const_cast<void*>(p), 0, bytes, 0x00020000);
}
__device__ __forceinline__ unsigned lds_ld(unsigned char* lds, int off) {
  return __builtin_amdgcn_readfirstlane(__hip_atomic_load(reinterpret_cast<unsigned*>(lds + off), __ATOMIC_RELAXED, __HIP_MEMORY_SCOPE_WORKGROUP));
}
template <int SLEEP = 1>
__device__ __forceinline__ void lds_wait_ge(unsigned char* lds, int off, unsigned want) {
#pragma nounroll
  for (unsigned spin = 0; spin < SPIN_CAP; ++spin) {
    if (lds_ld(lds, off) >= want) break;
    __builtin_amdgcn_s_sleep(SLEEP);
  }
  FENCE();
}
__device__ __forceinline__ void lds_add(unsigned char* lds, int off, unsigned v, int lane) {
  if (lane == 0) __hip_atomic_fetch_add(reinterpret_cast<unsigned*>(lds + off), v, __ATOMIC_RELAXED, __HIP_MEMORY_SCOPE_WORKGROUP);
}
__device__ __forceinline__ void lds_arrive(unsigned char* lds, int off, int lane) {
  asm volatile("s_waitcnt lgkmcnt(0)" ::: "memory");
  lds_add(lds, off, 1u, lane);
}
__device__ __forceinline__ unsigned now() { return (unsigned)__builtin_amdgcn_s_memrealtime(); }
template <int SLEEP>
__device__ __forceinline__ void sleep_until(unsigned t0, unsigned ticks) {
#pragma nounroll
  for (unsigned spin = 0; spin < POLL_CAP && now() - t0 < ticks; ++spin) __builtin_amdgcn_s_sleep(SLEEP);
}
__device__ __forceinline__ void start_wait(unsigned char* lds, int idx, unsigned t_start) {
#pragma nounroll
  for (unsigned spin = 0; spin < POLL_CAP; ++spin) {
    if (now() - t_start >= (unsigned)(CH_T0 + idx * CH_TAU) || lds_ld(lds, C_QRDY) != 0u) break;
    __builtin_amdgcn_s_sleep(1);
  }
  FENCE();
}
template <int N, int SLEEP>
__device__ __forceinline__ bool gather(u32x4 (&v)[N], rsrc_t rs, const unsigned (&voff)[N], unsigned soff, unsigned tag) {
  bool have[N];
#pragma unroll
  for (int k = 0; k < N; ++k) { have[k] = voff[k] == OOB; v[k] = u32x4{0u, 0u, 0u, 0u}; }
#pragma nounroll
  for (unsigned spin = 0; spin < POLL_CAP; ++spin) {
    FENCE();
    u32x4 t[N];
#pragma unroll
    for (int k = 0; k < N; ++k) t[k] = __builtin_amdgcn_raw_buffer_load_b128(rs, have[k] ? OOB : voff[k], soff, AUX_SC1);
    bool all = true;
#pragma unroll
    for (int k = 0; k < N; ++k) {
      const bool got = !have[k] && t[k][1] == tag && t[k][3] == tag;
      v[k][0] = got ? t[k][0] : v[k][0]; v[k][2] = got ? t[k][2] : v[k][2];
      have[k] = have[k] || got; all = all && have[k];
    }
    if (__builtin_amdgcn_ballot_w64(!all) == 0) return true;
    if (SLEEP > 0) __builtin_amdgcn_s_sleep(SLEEP);
  }
  return false;
}

struct Args {
  const unsigned short* x;
  const unsigned char* k;
  const unsigned char* v;
  const unsigned char* wq;
  const unsigned char* wo;
  const unsigned char* wg;
  const unsigned char* wu;
  const unsigned char* wd;
  unsigned short* out;
  unsigned char* ws;
  unsigned wg_off[NWG];
};

__device__ __forceinline__ bool swaps(int mode, int n) { return (n & (mode == 0 ? 3 : mode == 2 ? 1 : 0)) == (mode == 0 ? 3 : 1); }
__device__ __forceinline__ void issue(u32x4 (&r)[8], rsrc_t rs, unsigned v0, unsigned v1, unsigned soff) {
#pragma unroll
  for (int i = 0; i < 8; ++i) { r[i] = __builtin_amdgcn_raw_buffer_load_b128(rs, (i & 1) ? v1 : v0, soff + OFF_J(i >> 1), AUX_NT); SB(); }
}
__device__ __forceinline__ f32x4 mfma(u32x4 x, u32x4 y, f32x4 acc) {
  return __builtin_amdgcn_mfma_f32_16x16x32_bf16(__builtin_bit_cast(bf16x8, x), __builtin_bit_cast(bf16x8, y), acc, 0, 0, 0);
}
__device__ __forceinline__ void qk_mfma(const u32x4 (&r)[8], const u32x4 (&qf)[2], f32x4 (&d)[4], bool sw) {
#pragma unroll
  for (int j = 0; j < 4; ++j) {
    u32x4 x0, x1;
#pragma unroll
    for (int i = 0; i < 4; ++i) { x0[i] = sw ? r[2 * j + 1][i] : r[2 * j][i]; x1[i] = sw ? r[2 * j][i] : r[2 * j + 1][i]; }
    d[j] = mfma(qf[1], x1, mfma(qf[0], x0, f32x4{0.f, 0.f, 0.f, 0.f}));
  }
  SB();
}
__device__ __forceinline__ unsigned short qk_score(const f32x4 (&d)[4], bool b0, bool b1) {
  float v[4];
#pragma unroll
  for (int j = 0; j < 4; ++j) {
    float e0 = d[j][0], e1 = d[j][1], e2 = d[j][2], e3 = d[j][3];
    asm("" : "+v"(e0), "+v"(e2));
    const float lo = b0 ? e1 : e0, hi = b0 ? e3 : e2;
    v[j] = b1 ? hi : lo;
  }
  const float u0 = (b0 ? v[1] : v[0]) + dpp<XOR1>(b0 ? v[0] : v[1]);
  const float u1 = (b0 ? v[3] : v[2]) + dpp<XOR1>(b0 ? v[2] : v[3]);
  const float w = (b1 ? u1 : u0) + dpp<XOR2>(b1 ? u0 : u1);
  return (unsigned short)(pk_bf16(w, w) & 0xffffu);
}
__device__ __forceinline__ void pv_mfma(const u32x4 (&vr)[8], u32x2 pk, int msel, f32x4& acc0, f32x4& acc1) {
#pragma unroll
  for (int q = 0; q < 4; ++q) {
    const u32x4 pa = {msel == 2 * q ? pk[0] : 0u, msel == 2 * q ? pk[1] : 0u, msel == 2 * q + 1 ? pk[0] : 0u, msel == 2 * q + 1 ? pk[1] : 0u};
    const unsigned sel = (q & 1) ? 0x07060302u : 0x05040100u;
    const int d0 = q >> 1, d1 = 2 + (q >> 1);
    const u32x4 b0 = {__builtin_amdgcn_perm(vr[2][d0], vr[0][d0], sel), __builtin_amdgcn_perm(vr[6][d0], vr[4][d0], sel),
                      __builtin_amdgcn_perm(vr[3][d0], vr[1][d0], sel), __builtin_amdgcn_perm(vr[7][d0], vr[5][d0], sel)};
    const u32x4 b1 = {__builtin_amdgcn_perm(vr[2][d1], vr[0][d1], sel), __builtin_amdgcn_perm(vr[6][d1], vr[4][d1], sel),
                      __builtin_amdgcn_perm(vr[3][d1], vr[1][d1], sel), __builtin_amdgcn_perm(vr[7][d1], vr[5][d1], sel)};
    acc0 = mfma(pa, b0, acc0);
    acc1 = mfma(pa, b1, acc1);
  }
  SB();
}
__device__ __forceinline__ f32x4 swap_halves(f32x4 x, bool t) { return f32x4{t ? x[2] : x[0], t ? x[3] : x[1], t ? x[0] : x[2], t ? x[1] : x[3]}; }
__device__ __forceinline__ void fma8(float (&s)[8], float xv, u32x4 w) {
#pragma unroll
  for (int i = 0; i < 4; ++i) { s[2 * i] = fmaf(xv, bf_lo(w[i]), s[2 * i]); s[2 * i + 1] = fmaf(xv, bf_hi(w[i]), s[2 * i + 1]); }
}
__device__ __forceinline__ void load_qf(u32x4 (&qf)[2], const unsigned char* lds, int c, int g) {
#pragma unroll
  for (int e = 0; e < 2; ++e) qf[e] = *reinterpret_cast<const u32x4*>(lds + L_Q + ((c >> 2) & 1) * 512 + (4 * e + g) * 64 + (c & 3) * 16);
}

__global__ void __launch_bounds__(NTHR, 2) fused_decode(Args a) {
  extern __shared__ __attribute__((aligned(16))) unsigned char lds[];
  const int lane0 = threadIdx.x & 63, wave = __builtin_amdgcn_readfirstlane(threadIdx.x >> 6);
  const int wg = blockIdx.x, h = wg >> 5, s = wg & 31;
  unsigned long long pc;
  unsigned code_bytes;
  asm volatile("s_getpc_b64 %0\n.Lfd_pc:\n\ts_mov_b32 %1, .Lfd_end-.Lfd_pc" : "=s"(pc), "=s"(code_bytes));
  const unsigned cpf = __builtin_amdgcn_raw_buffer_load_b32(mk_rsrc(reinterpret_cast<const void*>(pc), (wg & 0xc8) == 8 ? code_bytes : 0u), threadIdx.x * 128u, 0, 0);
  SB();
  const unsigned t_start = now();
  const bool helper = wave < 4;
  const unsigned wg_off = a.wg_off[wg];
  const unsigned vk0 = OFF_G(lane0 >> 2) + (lane0 >> 4) * 64 + (lane0 & 3) * 16, vk1 = vk0 + 256;
  const rsrc_t r_k = mk_rsrc(a.k, 8u * SK * DH * 2);
  u32x4 ra[8], rb[8];
#pragma unroll
  for (int i = 0; i < 8; ++i) asm volatile("" : "=v"(ra[i]), "=v"(rb[i]));
  if (!helper) {
    const unsigned t0 = wg_off ^ OFF_W(wave);
    issue(ra, r_k, vk0, vk1, t0);
    issue(rb, r_k, vk0, vk1, t0 ^ SLOT1);
  }
  const rsrc_t r_ws = mk_rsrc(a.ws, WS_END);
#define LANE_VARS int tid = threadIdx.x; asm volatile("" : "+v"(tid)); const int lane = tid & 63, c = lane & 15, g = lane >> 4; (void)c; (void)g
#define SCORE_VARS const bool b0 = (c & 1) != 0, b1 = (c & 2) != 0; const int sc_lane = (g & 1) * 32 + c * 2
  unsigned tag;

  if (__builtin_expect(helper, 1)) {
    __builtin_amdgcn_s_setprio(3);
    LANE_VARS;
    const bool qwg = (s & 8) == 0;
    const int qsl = (s & 7) * 2 + (s >> 4);
    const int fb = h * 16 + qsl;
    const int wrow = (lane >> (qwg ? 2 : 3)) + (qwg ? 16 * wave : (fb & 3) * 112 + (wave >> 1) * 56), wstep = qwg ? 64 : 8;
    const unsigned rowb = qwg ? NQ * 2 : FF * 2;
    const rsrc_t r0 = mk_rsrc(qwg ? a.wq : (wave & 1) ? a.wu : a.wg, (qwg || fb < 112) ? D * rowb : 0u);
    const unsigned v0 = wrow * rowb + (lane & (qwg ? 3 : 7)) * 16;
    const unsigned s0 = qwg ? (h * 512 + qsl * 32) * 2 : (fb >> 2) * 128;
    const u32x4 xv = __builtin_amdgcn_raw_buffer_load_b128(mk_rsrc(a.x, D * 2), lane * 16, 0, 0);
    const unsigned epoch = __builtin_amdgcn_raw_buffer_load_b32(r_ws, 0, WS_EPOCH, AUX_SC1);
    u32x4 wv[7];
#pragma unroll
    for (int i = 0; i < 7; ++i) wv[i] = __builtin_amdgcn_raw_buffer_load_b128(r0, v0, s0 + i * wstep * rowb, AUX_NT);
    SB();
    if (tid < 16) *reinterpret_cast<unsigned*>(lds + L_CTL + tid * 4) = 0u;
    lds_barrier();

    {
      float xs[8];
#pragma unroll
      for (int i = 0; i < 4; ++i) { xs[2 * i] = bf_lo(xv[i]); xs[2 * i + 1] = bf_hi(xv[i]); }
      float sm = 0.f;
#pragma unroll
      for (int i = 0; i < 8; ++i) sm += xs[i];
      const float mean = wave_sum(sm) * (1.0f / D);
      float sq = 0.f;
#pragma unroll
      for (int i = 0; i < 8; ++i) { const float d = xs[i] - mean; sq = fmaf(d, d, sq); }
      sq = lane < D / 8 ? sq : 0.f;
      const float var = wave_sum(sq) * (1.0f / D);
      const float mean_b = rnd_bf16(mean);
      const float den_b = rnd_bf16(__builtin_amdgcn_sqrtf(rnd_bf16(rnd_bf16(var) + 1e-5f)));
      float rcp = __builtin_amdgcn_rcpf(den_b);
      rcp = fmaf(fmaf(-den_b, rcp, 1.0f), rcp, rcp);
#pragma unroll
      for (int i = 0; i < 8; ++i) {
        const float n = rnd_bf16(xs[i] - mean_b), q = n * rcp;
        xs[i] = rnd_bf16(fmaf(fmaf(-den_b, q, n), rcp, q));
      }
      f32x4* xp = reinterpret_cast<f32x4*>(lds + L_XN + lane * 32);
      xp[0] = f32x4{xs[0], xs[1], xs[2], xs[3]}; xp[1] = f32x4{xs[4], xs[5], xs[6], xs[7]};
      FENCE();
    }
    tag = __builtin_amdgcn_readfirstlane(epoch) + 1u;
    asm volatile("" :: "v"(cpf));
    {
      float sacc[8] = {0.f, 0.f, 0.f, 0.f, 0.f, 0.f, 0.f, 0.f};
#pragma unroll
      for (int i = 0; i < 7; ++i) fma8(sacc, *reinterpret_cast<const float*>(lds + L_XN + (wrow + wstep * i) * 4), wv[i]);
#pragma unroll
      for (int i = 0; i < 8; ++i) { float t = sacc[i]; const float r = dpp<ROR4>(t); t += qwg ? r : 0.f; t += dpp<XOR8>(t); sacc[i] = x32_sum(x16_sum(t)); }
      if (lane < 8) {
        f32x4* p = reinterpret_cast<f32x4*>(lds + L_RED + wave * 256 + lane * 32);
        p[0] = f32x4{sacc[0], sacc[1], sacc[2], sacc[3]}; p[1] = f32x4{sacc[4], sacc[5], sacc[6], sacc[7]};
      }
      lds_arrive(lds, C_PART, lane);
    }
    const bool ffn = wg < FF / 8;
    if (wave == 0) {
      lds_wait_ge(lds, C_PART, 4u);
      if (qwg) {
        float q0 = 0.f, q1 = 0.f;
#pragma unroll
        for (int w = 0; w < 4; ++w) { const f32x2 t = *reinterpret_cast<const f32x2*>(lds + L_RED + w * 256 + c * 8); q0 += t[0]; q1 += t[1]; }
        __builtin_amdgcn_raw_buffer_store_b64(u32x2{pk_bf16(q0, q1), tag}, r_ws, lane < 16 ? lane * 8 : OOB, WS_QG + (h * 16 + qsl) * LSTR, AUX_SC1);
      } else {
        const int o = ((lane >> 2) & 1) * 256 + (lane >> 3) * 32 + (lane & 3) * 8;
        const f32x2 t0 = *reinterpret_cast<const f32x2*>(lds + L_RED + o), t1 = *reinterpret_cast<const f32x2*>(lds + L_RED + 512 + o);
        __builtin_amdgcn_raw_buffer_store_b128(u32x4{__float_as_uint(t0[0] + t1[0]), tag, __float_as_uint(t0[1] + t1[1]), tag}, r_ws,
                                               fb < 112 ? (lane >> 3) * 512 + (lane & 7) * 16 : OOB, WS_GU + (fb >> 2) * 4096 + (fb & 3) * 128, AUX_SC1);
      }
      bool ok;
      {
        u32x4 qg[2];
        const unsigned qo[2] = {(lane >> 2) * LSTR + (lane & 3) * 32, (lane >> 2) * LSTR + (lane & 3) * 32 + 16};
        sleep_until<1>(t_start, Q_FIRST);
        ok = gather<2, 0>(qg, r_ws, qo, WS_QG + h * 16 * LSTR, tag);
        *reinterpret_cast<u32x4*>(lds + L_Q + lane * 16) = u32x4{qg[0][0], qg[0][2], qg[1][0], qg[1][2]};
        if (lane == 0) *reinterpret_cast<unsigned*>(lds + C_TAG) = tag;
        lds_arrive(lds, C_QRDY, lane);
      }
      lds_wait_ge<32>(lds, C_DONE, ROWS_AT - 10);
      lds_wait_ge(lds, C_DONE, ROWS_AT);
      const rsrc_t r_wo = mk_rsrc(a.wo, NQ * D * 2), r_d = mk_rsrc(a.wd, ffn ? FF * D * 2 : 0);
      const unsigned coff = lane < D / 8 ? lane * 16 : OOB;
      u32x4 dr[8], wr[16];
#pragma unroll
      for (int j = 0; j < 8; ++j) dr[j] = __builtin_amdgcn_raw_buffer_load_b128(r_d, coff, (wg * 8 + j) * (D * 2), AUX_NT);
#pragma unroll
      for (int j = 0; j < 16; ++j) wr[j] = __builtin_amdgcn_raw_buffer_load_b128(r_wo, coff, (wg * 16 + j) * (D * 2), AUX_NT);
      SB();
      lds_barrier();
      float y[8];
#pragma unroll
      for (int i = 0; i < 4; ++i) { y[2 * i] = wg == NWG - 1 ? bf_lo(xv[i]) : 0.f; y[2 * i + 1] = wg == NWG - 1 ? bf_hi(xv[i]) : 0.f; }
#pragma unroll
      for (int j = 0; j < 8; ++j) fma8(y, *reinterpret_cast<const float*>(lds + L_H + j * 4), dr[j]);
#pragma unroll
      for (int j = 0; j < 8; ++j) {
        const u32x4 lo = wr[2 * j], hi = wr[2 * j + 1];
#pragma unroll
        for (int i = 0; i < 4; ++i) { wr[2 * j][i] = __builtin_amdgcn_perm(hi[i], lo[i], 0x05040100u); wr[2 * j + 1][i] = __builtin_amdgcn_perm(hi[i], lo[i], 0x07060302u); }
      }
      asm volatile("" : "+v"(y[0]), "+v"(y[1]), "+v"(y[2]), "+v"(y[3]), "+v"(y[4]), "+v"(y[5]), "+v"(y[6]), "+v"(y[7]), "+v"(wr[0]), "+v"(wr[1]), "+v"(wr[2]), "+v"(wr[3]),
                        "+v"(wr[4]), "+v"(wr[5]), "+v"(wr[6]), "+v"(wr[7]), "+v"(wr[8]), "+v"(wr[9]), "+v"(wr[10]), "+v"(wr[11]), "+v"(wr[12]), "+v"(wr[13]),
                        "+v"(wr[14]), "+v"(wr[15]));
      {
        const unsigned base = lds_ld(lds, C_BASE), due = base + (unsigned)(TT_TAU + H1_DUE);
#pragma nounroll
        for (int spin = 0; spin < 128 && base != 0u && (int)(now() - due) < 0; ++spin) __builtin_amdgcn_s_sleep(1);
      }
      u32x4 po[4];
      const unsigned oo[4] = {(unsigned)lane * 16, (unsigned)lane * 16 + 1024, (unsigned)lane * 16 + 2048, (unsigned)lane * 16 + 3072};
      ok = gather<4, 0>(po, r_ws, oo, WS_PO + wg * 4096, tag) && ok;
      float o0 = (__uint_as_float(po[0][0]) + __uint_as_float(po[1][0])) + (__uint_as_float(po[2][0]) + __uint_as_float(po[3][0]));
      float o1 = (__uint_as_float(po[0][2]) + __uint_as_float(po[1][2])) + (__uint_as_float(po[2][2]) + __uint_as_float(po[3][2]));
      o0 += dpp<XOR8>(o0); o1 += dpp<XOR8>(o1);
      o0 = x32_sum(x16_sum(o0)); o1 = x32_sum(x16_sum(o1));
      if (!ok) o0 = __builtin_nanf("");
      const unsigned opk = pk_bf16(o0, o1);
      unsigned oj[8];
#pragma unroll
      for (int j = 0; j < 8; ++j) oj[j] = __builtin_amdgcn_readlane(opk, j);
      const unsigned yoff = lane < D / 8 ? (unsigned)(lane >> 2) * (4 * 16384) + (lane & 3) * 16 : OOB;
#pragma unroll
      for (int k = 0; k < 4; ++k) {
#pragma unroll
        for (int j = 0; j < 8; ++j) { y[2 * k] = dot2(wr[2 * j][k], oj[j], y[2 * k]); y[2 * k + 1] = dot2(wr[2 * j + 1][k], oj[j], y[2 * k + 1]); }
        __builtin_amdgcn_raw_buffer_store_b128(u32x4{__float_as_uint(y[2 * k]), tag, __float_as_uint(y[2 * k + 1]), tag}, r_ws, yoff,
                                               WS_YA + k * 16384 + wg * 64, AUX_SC1);
      }
      lds_add(lds, C_HOP1, 1u, lane);
    } else {
      {
        SCORE_VARS;
        __builtin_amdgcn_s_setprio(2);
        const int slot = NI + wave - 1;
        u32x4 kt[4][8];
#pragma unroll
        for (int i = 0; i < 4; ++i) { start_wait(lds, 4 * slot + i, t_start); issue(kt[i], r_k, vk0, vk1, wg_off ^ OFF_SLOT(slot) ^ OFF_W(i)); }
        lds_wait_ge(lds, C_QRDY, 1u);
        u32x4 qf[2];
        load_qf(qf, lds, c, g);
#pragma unroll
        for (int i = 0; i < 4; ++i) {
          f32x4 d[4];
          qk_mfma(kt[i], qf, d, false);
          *reinterpret_cast<unsigned short*>(lds + L_P + i * 2048 + slot * 64 + sc_lane) = qk_score(d, b0, b1);
        }
        lds_arrive(lds, C_KHELP, lane);
        lds_add(lds, C_DONE, 4u, lane);
      }
      if (wave == 3) {
        sleep_until<32>(t_start, GU_AT);
        u32x4 gu[1];
        const unsigned go[1] = {(ffn && lane < 32) ? (unsigned)lane * 16 : OOB};
        const bool ok = gather<1, 64>(gu, r_ws, go, WS_GU + wg * 512, tag);
        float g0 = __uint_as_float(gu[0][0]), g1 = __uint_as_float(gu[0][2]);
        g0 += dpp<XOR8>(g0); g1 += dpp<XOR8>(g1);
        g0 = rnd_bf16(x16_sum(g0)); g1 = rnd_bf16(x16_sum(g1));
        const float u0 = dpp<ROR4>(g0), u1 = dpp<ROR4>(g1);
        const float h0 = rnd_bf16((g0 > 0.f ? g0 : g0 * 0.f) * u0), h1 = rnd_bf16((g1 > 0.f ? g1 : g1 * 0.f) * u1);
        if (lane < 4) *reinterpret_cast<f32x2*>(lds + L_H + lane * 8) = f32x2{ok ? h0 : __builtin_nanf(""), h1};
      } else if (wave == 2) {
        const bool smp = (s & 7) == ((h + (s >> 3) * 4) & 7) && s < 16;
#pragma nounroll
        for (int k = 0; k < TT_N; ++k) {
          lds_wait_ge<32>(lds, C_DONE, TT_AT + k * TT_PER - 6);
          lds_wait_ge<2>(lds, C_DONE, TT_AT + k * TT_PER);
          const unsigned tb = now() - ((lds_ld(lds, C_DONE) * TT_TAU) >> 8);
          __builtin_amdgcn_raw_buffer_store_b128(u32x4{tb, tag, tb, tag}, r_ws, (smp && lane < 8) ? lane * 16 : OOB, WS_TT + (16 * k + 2 * h + (s >> 3)) * LSTR, AUX_SC1);
          lds_wait_ge<8>(lds, C_DONE, TT_AT + k * TT_PER + TT_GAP);
          const u32x4 y = __builtin_amdgcn_raw_buffer_load_b128(r_ws, lane < 16 ? lane * LSTR : OOB, WS_TT + 16 * k * LSTR, AUX_SC1);
          const bool oy = lane < 16 && y[1] == tag && y[3] == tag;
          if (__builtin_amdgcn_ballot_w64(oy) == 0xffffu) {
            const float dl = row_sum(oy ? (float)(int)(y[0] - tb) : 0.f);
            if (lane == 0) *reinterpret_cast<unsigned*>(lds + C_BASE) = (tb + (unsigned)(int)(dl * 0.0625f + (float)(k ? TT_OFF : TT_OFF0))) | 1u;
          }
        }
      } else {
        lds_wait_ge<32>(lds, C_MID, 4u);
        const int hd = lane & 1;
        float mw[4], lw[4];
#pragma unroll
        for (int w = 0; w < 4; ++w) { const f32x2 t = *reinterpret_cast<const f32x2*>(lds + L_ML + w * 16 + hd * 8); mw[w] = t[0]; lw[w] = t[1]; }
        const float M = fmaxf(fmaxf(mw[0], mw[1]), fmaxf(mw[2], mw[3]));
        float L = 0.f;
#pragma unroll
        for (int w = 0; w < 4; ++w) L = fmaf(__builtin_amdgcn_exp2f((mw[w] - M) * C2), lw[w], L);
        __builtin_amdgcn_raw_buffer_store_b128(u32x4{__float_as_uint(M), tag, __float_as_uint(L), tag}, r_ws, lane < 8 ? lane * 16 : OOB, WS_ML + wg * LSTR, AUX_SC1);
        sleep_until<16>(now(), ML_WAIT);
        u32x4 ml[1];
        const unsigned mo[1] = {(lane >> 1) * LSTR + hd * 16};
        const bool ok = gather<1, 32>(ml, r_ws, mo, WS_ML + h * 32 * LSTR, tag);
        const float Ms = __uint_as_float(ml[0][0]), Mg = parity_max(Ms);
        const float Lg = parity_sum(__builtin_amdgcn_exp2f((Ms - Mg) * C2) * __uint_as_float(ml[0][2]));
        if (lane < 2) {
#pragma unroll
          for (int w = 0; w < 4; ++w) *reinterpret_cast<float*>(lds + L_SC + w * 8 + lane * 4) = ok ? __builtin_amdgcn_exp2f((mw[w] - Mg) * C2) / Lg : __builtin_nanf("");
        }
      }
      lds_barrier();
    }
  } else {
    const int w = wave - 4;
    const unsigned t0 = wg_off ^ OFF_W(wave);
    LANE_VARS;
    SCORE_VARS;
    const rsrc_t r_v = mk_rsrc(a.v, 8u * SK * DH * 2);
    const unsigned vv0 = OFF_G(g) + c * 16, vv1 = vv0 + 256;
    u32x4 rx[NI - 2][8];
    lds_barrier();
#pragma unroll
    for (int i = 2; i < NI; ++i) { start_wait(lds, 4 * i + w, t_start); issue(rx[i - 2], r_k, vk0, vk1, t0 ^ OFF_SLOT(i)); }
    lds_wait_ge(lds, C_QRDY, 1u);
    tag = lds_ld(lds, C_TAG);
    u32x4 qf[2];
    load_qf(qf, lds, c, g);
    unsigned char* const pl = lds + L_P + w * 2048;
    auto pace = [&](unsigned level) -> int {
      u32x2 cw;
#pragma nounroll
      for (unsigned spin = 0; spin < SPIN_CAP; ++spin) {
        FENCE();
        cw = *reinterpret_cast<const u32x2*>(lds + C_DONE);
        if (__builtin_amdgcn_readfirstlane(cw[0]) + SLACK >= level) break;
        __builtin_amdgcn_s_sleep(2);
      }
      const unsigned done = __builtin_amdgcn_readfirstlane(cw[0]), base = __builtin_amdgcn_readfirstlane(cw[1]);
      const int lag = (int)(now() - base - ((done * TT_TAU) >> 8));
      return base == 0u ? 0 : lag > w * STEP_FAST ? 1 : lag < -AHEAD - w * STEP_SLOW ? 2 : 0;
    };
    bool vs = swaps(0, c >> 2);
    {
      f32x4 d[4];
      unsigned short* sw = reinterpret_cast<unsigned short*>(pl + sc_lane);
#define K_STEP(R, SW, ISSUE_NEXT, SLOT) do { qk_mfma(R, qf, d, SW); ISSUE_NEXT; sw[(SLOT) * 32] = qk_score(d, b0, b1); } while (0)
      bool ks = swaps(0, g), kt = ks;
      K_STEP(ra, false, issue(ra, r_k, ks ? vk1 : vk0, ks ? vk0 : vk1, t0 ^ OFF_SLOT(NI + 3)), 0);
      K_STEP(rb, false, issue(rb, r_k, ks ? vk1 : vk0, ks ? vk0 : vk1, t0 ^ OFF_SLOT(NI + 4)), 1);
#pragma unroll
      for (int i = 2; i < NI; ++i) K_STEP(rx[i - 2], false, (void)0, i);
      lds_add(lds, C_DONE, NI, lane);
      sw += (NI + 3) * 32;
      static_assert((NI + 3) % 2 == 0, "the remaining slots come in pairs");
      for (int it = 1; it < (29 - NI) / 2; ++it) {
        const unsigned tp = t0 ^ OFF_SLOT(NI + 3 + 2 * it);
        const bool na = swaps(pace(it >= 3 ? 4 * NI + 4 + 8 * it : 0), g);
        K_STEP(ra, ks, issue(ra, r_k, na ? vk1 : vk0, na ? vk0 : vk1, tp), 0);
        lds_add(lds, C_DONE, 1u, lane);
        const bool nb = swaps(pace(it >= 3 ? 4 * NI + 8 + 8 * it : 0), g);
        K_STEP(rb, kt, issue(rb, r_k, nb ? vk1 : vk0, nb ? vk0 : vk1, tp ^ SLOT1), 1);
        lds_add(lds, C_DONE, 1u, lane);
        ks = na; kt = nb;
        sw += 2 * 32;
      }
      K_STEP(ra, ks, issue(ra, r_v, vs ? vv1 : vv0, vs ? vv0 : vv1, t0), 0);
      lds_add(lds, C_DONE, 1u, lane);
      K_STEP(rb, kt, issue(rb, r_v, vs ? vv1 : vv0, vs ? vv0 : vv1, t0 ^ SLOT1), 1);
      lds_add(lds, C_DONE, 1u, lane);
#undef K_STEP
    }
    lds_wait_ge(lds, C_KHELP, 3u);
    {
      u32x4* pp = reinterpret_cast<u32x4*>(pl + lane * 32);
      u32x4 t[2] = {pp[0], pp[1]};
      float mx = -3.0e38f;
#pragma unroll
      for (int j = 0; j < 2; ++j)
#pragma unroll
        for (int i = 0; i < 4; ++i) mx = fmaxf(mx, fmaxf(bf_lo(t[j][i]), bf_hi(t[j][i])));
      mx = parity_max(mx);
      float lp = 0.f;
#pragma unroll
      for (int j = 0; j < 2; ++j) {
#pragma unroll
        for (int i = 0; i < 4; ++i) {
          const float p0 = __builtin_amdgcn_exp2f((bf_lo(t[j][i]) - mx) * C2), p1 = __builtin_amdgcn_exp2f((bf_hi(t[j][i]) - mx) * C2);
          lp += p0 + p1; t[j][i] = pk_bf16(p0, p1);
        }
        pp[j] = t[j];
      }
      lp = parity_sum(lp);
      if (lane < 2) *reinterpret_cast<f32x2*>(lds + L_ML + w * 16 + lane * 8) = f32x2{mx, lp};
      lds_arrive(lds, C_MID, lane);
    }
    {
      f32x4 acc0 = {0.f, 0.f, 0.f, 0.f}, acc1 = {0.f, 0.f, 0.f, 0.f};
      const int msel = c >> 1;
      bool vt = vs;
      const unsigned char* pr = pl + (c & 1) * 32 + g * 8;
#define V_STEP(R, ISSUE_NEXT) do { const u32x2 pk_ = *reinterpret_cast<const u32x2*>(pr); pr += 64; pv_mfma(R, pk_, msel, acc0, acc1); ISSUE_NEXT; } while (0)
      for (int it = 0; it < 15; ++it) {
        const unsigned tp = t0 ^ OFF_SLOT(2 + 2 * it);
        const bool na = swaps(pace(128 + 8 * it), c >> 2);
        V_STEP(ra, issue(ra, r_v, na ? vv1 : vv0, na ? vv0 : vv1, tp));
        lds_add(lds, C_DONE, 1u, lane);
        acc0 = swap_halves(acc0, vt != vs); acc1 = swap_halves(acc1, vt != vs);
        const bool nb = swaps(pace(132 + 8 * it), c >> 2);
        V_STEP(rb, issue(rb, r_v, nb ? vv1 : vv0, nb ? vv0 : vv1, tp ^ SLOT1));
        lds_add(lds, C_DONE, 1u, lane);
        acc0 = swap_halves(acc0, na != vt); acc1 = swap_halves(acc1, na != vt);
        vs = na; vt = nb;
      }
      V_STEP(ra, lds_add(lds, C_DONE, 1u, lane));
      acc0 = swap_halves(acc0, vt != vs); acc1 = swap_halves(acc1, vt != vs);
      vs = vt;
      V_STEP(rb, (void)0);
#undef V_STEP
      float* const rec = reinterpret_cast<float*>(lds + L_ACC + w * 2048) + 8 * c + g;
#pragma unroll
      for (int k = 0; k < 4; ++k) { float* r = rec + (k & 1) * 256 + (((k >> 1) != 0) != vs ? 128 : 0); r[0] = acc0[k]; r[4] = acc1[k]; }
      asm volatile("" :: "v"(cpf));
    }
    lds_barrier();
    {
      const int u = tid - 256;
      float o0 = 0.f, o1 = 0.f;
#pragma unroll
      for (int k = 0; k < 4; ++k) {
        const float sc = *reinterpret_cast<const float*>(lds + L_SC + k * 8 + (u >> 7) * 4);
        const f32x2 t = *reinterpret_cast<const f32x2*>(lds + L_ACC + k * 2048 + u * 8);
        o0 = fmaf(sc, t[0], o0); o1 = fmaf(sc, t[1], o1);
      }
      __builtin_amdgcn_raw_buffer_store_b128(u32x4{__float_as_uint(o0), tag, __float_as_uint(o1), tag}, r_ws, (u >> 3) * 4096 + (u & 7) * 16,
                                             WS_PO + h * (32 * 4096) + s * 128, AUX_SC1);
      const unsigned t = __builtin_amdgcn_raw_buffer_load_b32(r_ws, (wg < NRED && lane < 32) ? (w * 32 + lane) * 128 : OOB, WS_YA + wg * 16384, AUX_SC1);
      asm volatile(".Lfd_end:" :: "v"(t));
    }
  }
  if (wg >= NRED) return;
  {
    LANE_VARS;
    lds_wait_ge(lds, C_HOP1, 1u);
    __builtin_amdgcn_s_sleep(H2_PRE);
    u32x4 t[2];
    const unsigned yo[2] = {(unsigned)tid * 16, (unsigned)tid * 16 + NTHR * 16};
    const bool ok = gather<2, 0>(t, r_ws, yo, WS_YA + wg * 16384, tag);
    float s0 = __uint_as_float(t[0][0]) + __uint_as_float(t[1][0]), s1 = __uint_as_float(t[0][2]) + __uint_as_float(t[1][2]);
    s0 += dpp<XOR8>(s0); s1 += dpp<XOR8>(s1);
    s0 += dpp<ROR4>(s0); s1 += dpp<ROR4>(s1);
    s0 = x32_sum(x16_sum(s0)); s1 = x32_sum(x16_sum(s1));
    if (!ok) s0 = __builtin_nanf("");
    if (lane < 4) *reinterpret_cast<f32x2*>(lds + L_YA + wave * 32 + lane * 8) = f32x2{s0, s1};
    lds_barrier();
    if (wave == 0) {
      const f32x2 p = *reinterpret_cast<const f32x2*>(lds + L_YA + (lane & 31) * 8);
      float r0 = p[0], r1 = p[1];
      r0 += dpp<XOR8>(r0); r1 += dpp<XOR8>(r1);
      r0 += dpp<ROR4>(r0); r1 += dpp<ROR4>(r1);
      __builtin_amdgcn_raw_buffer_store_b32(pk_bf16(x16_sum(r0), x16_sum(r1)), mk_rsrc(a.out, D * 2),
                                            lane < 4 ? ((wg >> 2) * 32 + lane * 8 + (wg & 3) * 2) * 2 : OOB, 0, AUX_SYS);
    }
    if (wg == 0 && tid == 64) __builtin_amdgcn_raw_buffer_store_b32(tag, r_ws, 0, WS_EPOCH, AUX_SC1);
  }
}

}

extern "C" void kernel_launch(void* const* d_in, const int* in_sizes, int n_in, void* d_out, int out_size, void* d_ws, size_t ws_size,
                              hipStream_t stream) {
  (void)in_sizes; (void)n_in; (void)out_size; (void)ws_size;
  Args a;
  a.x = (const unsigned short*)d_in[0];
  a.k = (const unsigned char*)d_in[1];
  a.v = (const unsigned char*)d_in[2];
  a.wq = (const unsigned char*)d_in[3];
  a.wo = (const unsigned char*)d_in[4];
  a.wg = (const unsigned char*)d_in[6];
  a.wu = (const unsigned char*)d_in[7];
  a.wd = (const unsigned char*)d_in[8];
  a.out = (unsigned short*)d_out;
  a.ws = (unsigned char*)d_ws;
  for (int i = 0; i < NWG; ++i) a.wg_off[i] = OFF_WG(i);
  hipLaunchKernelGGL(fused_decode, dim3(NWG), dim3(NTHR), LDS_BYTES, stream, a);
}
```
